```python
import torch

def _softmax(x, axis=-1):
    return torch.softmax(x, dim=axis)
def _gelu(x):
    return torch.nn.functional.gelu(x, approximate="tanh")
def _silu(x):
    return torch.nn.functional.silu(x)
def _sigmoid(x):
    return torch.sigmoid(x)
def _layernorm(x, axis=-1, eps=1e-5):
    m = x.mean(dim=axis, keepdim=True)
    v = x.var(dim=axis, keepdim=True, unbiased=False)
    return (x - m) / torch.sqrt(v + eps)
def _scatter_add(tgt, idx, src, axis=0):
    return tgt.clone().index_add_(axis, idx, src)
def _segment_sum(src, seg, n):
    out = torch.zeros((n,) + src.shape[1:], dtype=src.dtype)
    return out.index_add_(0, seg, src)
def _pad(x, pw):
    flat = tuple(v for pair in reversed(pw) for v in pair)
    return torch.nn.functional.pad(x, flat)

kernel_name = 'recurrent_scan__sliding_block__glu_gated_glu__prompt'

def setup_inputs():

    _w = (lambda a: (lambda t: t.to(torch.bfloat16) if t.is_floating_point() else t)(torch.as_tensor(a)))
    def _A_setup():
        B, S, D = 1, 2048, 576
        return {
            "x": _w((lambda *s: torch.randn(*s, dtype=torch.bfloat16) * 0.05)(B, S, D)),
            "wqkv": _w((lambda *s: torch.randn(*s, dtype=torch.bfloat16) * 0.05)(D, 3 * D)),
            "wo": _w((lambda *s: torch.randn(*s, dtype=torch.bfloat16) * 0.05)(D, D)),
        }
    _A_in = _A_setup()

    def _B_setup():
        B, S, D, F = 1, 2048, 576, 4608
        return {
            "x": _w((lambda *s: torch.randn(*s, dtype=torch.bfloat16) * 0.05)(B, S, D)),
            "w_gate": _w((lambda *s: torch.randn(*s, dtype=torch.bfloat16) * 0.05)(D, F)),
            "w_up": _w((lambda *s: torch.randn(*s, dtype=torch.bfloat16) * 0.05)(D, F)),
            "w_down": _w((lambda *s: torch.randn(*s, dtype=torch.bfloat16) * 0.05)(F, D)),
        }
    _B_in = _B_setup()

    return {
        "A_x": _A_in["x"],
        "A_wqkv": _A_in["wqkv"],
        "A_wo": _A_in["wo"],
        "B_x": _B_in["x"],
        "B_w_gate": _B_in["w_gate"],
        "B_w_up": _B_in["w_up"],
        "B_w_down": _B_in["w_down"],
    }

def reference(A_x, A_wqkv, A_wo, B_x, B_w_gate, B_w_up, B_w_down):
    def _A_ref(x, wqkv, wo):
        B, S, D = x.shape
        H = 32; Dh = D // H; W = 256
        qkv = (lambda x, *d: x.permute(*d))((x @ wqkv).reshape(B, S, 3, H, Dh), 2, 0, 3, 1, 4)
        q, k, v = qkv[0], qkv[1], qkv[2]
        scores = torch.einsum("bhqd,bhkd->bhqk", q, k) * (Dh ** -0.5)
        idx = torch.arange(S)
        mask = (idx[:, None] - idx[None, :] >= 0) & (idx[:, None] - idx[None, :] < W)
        scores = torch.where(mask[None, None], scores, -1e9)
        p = _softmax(scores, axis=-1)
        o = torch.einsum("bhqk,bhkd->bhqd", p, v)
        return (lambda x, *d: x.permute(*d))(o, 0, 2, 1, 3).reshape(B, S, D) @ wo

    def _B_ref(x, w_gate, w_up, w_down):
        g = torch.einsum("bsd,df->bsf", x, w_gate)
        u = torch.einsum("bsd,df->bsf", x, w_up)
        h = _sigmoid(g) * u
        return torch.einsum("bsf,fd->bsd", h, w_down)

    x = A_x
    xn = _layernorm(x, axis=-1)
    s = _A_ref(xn, A_wqkv, A_wo)
    g = _silu(_B_ref(xn, B_w_gate, B_w_up, B_w_down))
    return x + s * g
```

```cpp
#include <hip/hip_runtime.h>
#include <hip/hip_bf16.h>
#include <stdint.h>

typedef unsigned short u16;
__device__ __forceinline__ float bf2f(u16 v) { return __uint_as_float(((unsigned)v) << 16); }
__device__ __forceinline__ u16 f2bf(float f) { __hip_bfloat16 b = __float2bfloat16(f); return *(u16*)&b; }

constexpr int S = 2048, D = 576, H = 32, DH = 18, W = 256, F = 4608;

__global__ void k_ln(const u16* x, u16* xn) {
  int row = blockIdx.x; int lane = threadIdx.x;
  float v[9]; float s = 0;
  for (int i = 0; i < 9; i++) { v[i] = bf2f(x[row * D + lane + 64 * i]); s += v[i]; }
  for (int o = 32; o > 0; o >>= 1) s += __shfl_xor(s, o);
  float m = s / D; float q = 0;
  for (int i = 0; i < 9; i++) { float d = v[i] - m; q += d * d; }
  for (int o = 32; o > 0; o >>= 1) q += __shfl_xor(q, o);
  float r = rsqrtf(q / D + 1e-5f);
  for (int i = 0; i < 9; i++) xn[row * D + lane + 64 * i] = f2bf((v[i] - m) * r);
}

__global__ void k_gemm(const u16* A, const u16* B, u16* C, int M, int N, int K) {
  __shared__ float As[32][33]; __shared__ float Bs[32][33];
  int tx = threadIdx.x & 31, ty = threadIdx.x >> 5;
  int n = blockIdx.x * 32 + tx; int m0 = blockIdx.y * 32;
  float acc[4] = {0, 0, 0, 0};
  for (int k0 = 0; k0 < K; k0 += 32) {
    for (int i = 0; i < 4; i++) { int r = ty + 8 * i; As[r][tx] = bf2f(A[(size_t)(m0 + r) * K + k0 + tx]); Bs[r][tx] = (n < N) ? bf2f(B[(size_t)(k0 + r) * N + n]) : 0.f; }
    __syncthreads();
    for (int k = 0; k < 32; k++) { float b = Bs[k][tx]; for (int i = 0; i < 4; i++) acc[i] += As[ty + 8 * i][k] * b; }
    __syncthreads();
  }
  if (n < N) for (int i = 0; i < 4; i++) C[(size_t)(m0 + ty + 8 * i) * N + n] = f2bf(acc[i]);
}

__global__ void k_attn(const u16* qkv, u16* o) {
  int q = blockIdx.x, h = blockIdx.y, lane = threadIdx.x;
  float qv[DH];
  for (int d = 0; d < DH; d++) qv[d] = bf2f(qkv[(size_t)q * 3 * D + h * DH + d]);
  float sc[4]; float mx = -1e30f;
  for (int i = 0; i < 4; i++) {
    int k = q - (lane + 64 * i);
    float s = -1e30f;
    if (k >= 0) { s = 0; for (int d = 0; d < DH; d++) s += qv[d] * bf2f(qkv[(size_t)k * 3 * D + D + h * DH + d]); s *= 0.23570226f; }
    sc[i] = s; mx = fmaxf(mx, s);
  }
  for (int of = 32; of > 0; of >>= 1) mx = fmaxf(mx, __shfl_xor(mx, of));
  float sum = 0; float acc[DH]; for (int d = 0; d < DH; d++) acc[d] = 0;
  for (int i = 0; i < 4; i++) {
    int k = q - (lane + 64 * i);
    if (k >= 0) { float p = __expf(sc[i] - mx); sum += p; for (int d = 0; d < DH; d++) acc[d] += p * bf2f(qkv[(size_t)k * 3 * D + 2 * D + h * DH + d]); }
  }
  for (int of = 32; of > 0; of >>= 1) { sum += __shfl_xor(sum, of); for (int d = 0; d < DH; d++) acc[d] += __shfl_xor(acc[d], of); }
  if (lane < DH) { float v = 0; for (int d = 0; d < DH; d++) if (d == lane) v = acc[d]; o[(size_t)q * D + h * DH + lane] = f2bf(v / sum); }
}

__global__ void k_glu(const u16* g, const u16* u, u16* h, int n) {
  int i = blockIdx.x * blockDim.x + threadIdx.x; if (i < n) { float a = bf2f(g[i]); h[i] = f2bf(bf2f(u[i]) / (1.f + __expf(-a))); }
}
__global__ void k_final(const u16* x, const u16* s, const u16* d, u16* out, int n) {
  int i = blockIdx.x * blockDim.x + threadIdx.x; if (i < n) { float a = bf2f(d[i]); float g = a / (1.f + __expf(-a)); out[i] = f2bf(bf2f(x[i]) + bf2f(s[i]) * g); }
}

extern "C" void kernel_launch(void* const* d_in, const int* in_sizes, int n_in, void* d_out, int out_size, void* d_ws, size_t ws_size, hipStream_t stream) {
  const u16* x = (const u16*)d_in[0]; const u16* wqkv = (const u16*)d_in[1]; const u16* wo = (const u16*)d_in[2];
  const u16* wg = (const u16*)d_in[4]; const u16* wu = (const u16*)d_in[5]; const u16* wd = (const u16*)d_in[6];
  u16* ws = (u16*)d_ws;
  u16* xn = ws; u16* qkv = xn + S * D; u16* o = qkv + S * 3 * D; u16* s = o + S * D; u16* g = s + S * D; u16* u = g + (size_t)S * F; u16* h = u + (size_t)S * F; u16* d = h + (size_t)S * F;
  k_ln<<<S, 64, 0, stream>>>(x, xn);
  k_gemm<<<dim3(3 * D / 32, S / 32), 256, 0, stream>>>(xn, wqkv, qkv, S, 3 * D, D);
  k_attn<<<dim3(S, H), 64, 0, stream>>>(qkv, o);
  k_gemm<<<dim3(D / 32, S / 32), 256, 0, stream>>>(o, wo, s, S, D, D);
  k_gemm<<<dim3(F / 32, S / 32), 256, 0, stream>>>(xn, wg, g, S, F, D);
  k_gemm<<<dim3(F / 32, S / 32), 256, 0, stream>>>(xn, wu, u, S, F, D);
  k_glu<<<(S * F + 255) / 256, 256, 0, stream>>>(g, u, h, S * F);
  k_gemm<<<dim3(D / 32, S / 32), 256, 0, stream>>>(h, wd, d, S, D, F);
  k_final<<<(S * D + 255) / 256, 256, 0, stream>>>(x, s, d, (u16*)d_out, S * D);
}
```

```cpp
#include <hip/hip_runtime.h>
#include <hip/hip_bf16.h>
#include <stdint.h>
typedef unsigned short u16;
typedef unsigned int u32;
#define RFL(x) __builtin_amdgcn_readfirstlane(x)

__global__ __launch_bounds__(512, 2) void k1_kernel(const u16* __restrict__ x, const u16* __restrict__ wqkv, const u16* __restrict__ wg,
                                                    const u16* __restrict__ wu, const u16* __restrict__ wd, const u16* __restrict__ wo, char* __restrict__ ws) {
  __shared__ __attribute__((aligned(16))) char smem[163840];
  const int b = blockIdx.x, xcd = b & 7, idx = b >> 3;
  const int tb = (xcd & 1) * 8 + (idx & 7), gq = xcd >> 1, gi = idx >> 3;
  int nq, qcol0, isv, nu, f0;
  if (gi < 2) { const int bi = gq * 2 + gi; nu = 5; f0 = 64 * (gq * 18 + 5 * gi); isv = 0; nq = bi < 3 ? 1 : 0; qcol0 = 64 * (15 + bi); }
  else { const int si = gq * 2 + gi - 2; nu = 4; f0 = 64 * (gq * 18 + 10 + 4 * (gi - 2)); nq = 3; isv = si < 3 ? 1 : 0; qcol0 = si < 3 ? 1152 + 192 * si : 192 * (si - 3); }
  const int wid = RFL(threadIdx.x >> 6);
  nq = RFL(nq); qcol0 = RFL(qcol0); isv = RFL(isv); nu = RFL(nu); f0 = RFL(f0);
  const uint64_t px = (uint64_t)x, pq = (uint64_t)wqkv, pg = (uint64_t)wg, pu = (uint64_t)wu, pw = (uint64_t)ws, pd = (uint64_t)wd, po = (uint64_t)wo;
  u32 lds = (u32)(uintptr_t)smem;
  asm volatile(
"k1_code_start:\n"
"s_mov_b32 s48, %[wqkv_lo]\n"
"s_and_b32 s49, %[wqkv_hi], 0xffff\n"
"s_mov_b32 s50, 1990656\n"
"s_mov_b32 s51, 0x20000\n"
"s_mov_b32 s52, %[wg_lo]\n"
"s_and_b32 s53, %[wg_hi], 0xffff\n"
"s_mov_b32 s54, 5308416\n"
"s_mov_b32 s55, 0x20000\n"
"s_mov_b32 s56, %[wu_lo]\n"
"s_and_b32 s57, %[wu_hi], 0xffff\n"
"s_mov_b32 s58, 5308416\n"
"s_mov_b32 s59, 0x20000\n"
"s_mov_b32 s60, %[wd_lo]\n"
"s_and_b32 s61, %[wd_hi], 0xffff\n"
"s_mov_b32 s62, 5308416\n"
"s_mov_b32 s63, 0x20000\n"
"s_mov_b32 s44, %[ws_lo]\n"
"s_and_b32 s45, %[ws_hi], 0xffff\n"
"s_mov_b32 s46, 40670464\n"
"s_mov_b32 s47, 0x20000\n"
"s_mov_b32 s68, %[nq]\n"
"s_mov_b32 s80, %[isv]\n"
"s_mov_b32 s81, %[nu]\n"
"s_and_b32 s66, s65, 3\n"
"s_lshr_b32 s67, s65, 2\n"
"s_lshl_b32 s93, s64, 2\n"
"s_add_u32 s93, s93, s66\n"
"s_mov_b32 s40, %[x_lo]\n"
"s_and_b32 s41, %[x_hi], 0xffff\n"
"s_mov_b32 s64, %[tb]\n"
"s_mov_b32 s65, %[wid]\n"
"s_mov_b32 s48, %[wqkv_lo]\n"
"s_and_b32 s49, %[wqkv_hi], 0xffff\n"
"s_mov_b32 s50, 1990656\n"
"s_mov_b32 s51, 0x20000\n"
"s_mov_b32 s52, %[wg_lo]\n"
"s_and_b32 s53, %[wg_hi], 0xffff\n"
"s_mov_b32 s54, 5308416\n"
"s_mov_b32 s55, 0x20000\n"
"s_mov_b32 s56, %[wu_lo]\n"
"s_and_b32 s57, %[wu_hi], 0xffff\n"
"s_mov_b32 s58, 5308416\n"
"s_mov_b32 s59, 0x20000\n"
"s_mov_b32 s60, %[wd_lo]\n"
"s_and_b32 s61, %[wd_hi], 0xffff\n"
"s_mov_b32 s62, 5308416\n"
"s_mov_b32 s63, 0x20000\n"
"s_mov_b32 s44, %[ws_lo]\n"
"s_and_b32 s45, %[ws_hi], 0xffff\n"
"s_mov_b32 s46, 40670464\n"
"s_mov_b32 s47, 0x20000\n"
"s_mov_b32 s68, %[nq]\n"
"s_mov_b32 s80, %[isv]\n"
"s_mov_b32 s81, %[nu]\n"
"s_and_b32 s66, s65, 3\n"
"s_lshr_b32 s67, s65, 2\n"
"s_lshl_b32 s93, s64, 2\n"
"s_add_u32 s93, s93, s66\n"
"v_mbcnt_lo_u32_b32 v0, -1, 0\n"
"v_mbcnt_hi_u32_b32 v0, -1, v0\n"
"s_lshl_b32 s91, s65, 10\n"
"v_lshrrev_b32 v13, 3, v0\n"
"v_and_b32 v14, 7, v0\n"
"v_lshrrev_b32 v15, 4, v0\n"
"s_and_b32 s70, s65, 1\n"
"s_lshl_b32 s70, s70, 2\n"
"v_add_u32 v15, s70, v15\n"
"v_xor_b32 v15, v14, v15\n"
"v_lshlrev_b32 v15, 4, v15\n"
"v_mul_u32_u24 v16, 1152, v13\n"
"v_add_u32 v16, v16, v15\n"
"s_mul_i32 s71, s64, 147456\n"
"s_mul_i32 s70, s65, 9216\n"
"s_add_u32 s71, s71, s70\n"
"s_mov_b32 m0, s91\n"
"s_mov_b32 s42, 2359296\n"
"s_mov_b32 s43, 0x20000\n"
"s_add_u32 s70, s71, 0\n"
"buffer_load_dwordx4 v16, s[40:43], s70 offen lds\n"
"s_add_u32 m0, m0, 0x2000\n"
"s_add_u32 s70, s71, 73728\n"
"buffer_load_dwordx4 v16, s[40:43], s70 offen lds\n"
"s_add_u32 m0, m0, 0x2000\n"
"s_add_u32 s70, s71, 128\n"
"buffer_load_dwordx4 v16, s[40:43], s70 offen lds\n"
"s_add_u32 m0, m0, 0x2000\n"
"s_add_u32 s70, s71, 73856\n"
"buffer_load_dwordx4 v16, s[40:43], s70 offen lds\n"
"s_add_u32 m0, m0, 0x2000\n"
"s_add_u32 s70, s71, 256\n"
"buffer_load_dwordx4 v16, s[40:43], s70 offen lds\n"
"s_add_u32 m0, m0, 0x2000\n"
"s_add_u32 s70, s71, 73984\n"
"buffer_load_dwordx4 v16, s[40:43], s70 offen lds\n"
"s_add_u32 m0, m0, 0x2000\n"
"s_add_u32 s70, s71, 384\n"
"buffer_load_dwordx4 v16, s[40:43], s70 offen lds\n"
"s_add_u32 m0, m0, 0x2000\n"
"s_add_u32 s70, s71, 74112\n"
"buffer_load_dwordx4 v16, s[40:43], s70 offen lds\n"
"s_add_u32 m0, m0, 0x2000\n"
"s_add_u32 s70, s71, 512\n"
"buffer_load_dwordx4 v16, s[40:43], s70 offen lds\n"
"s_add_u32 m0, m0, 0x2000\n"
"s_add_u32 s70, s71, 74240\n"
"buffer_load_dwordx4 v16, s[40:43], s70 offen lds\n"
"s_add_u32 m0, m0, 0x2000\n"
"s_add_u32 s70, s71, 640\n"
"buffer_load_dwordx4 v16, s[40:43], s70 offen lds\n"
"s_add_u32 m0, m0, 0x2000\n"
"s_add_u32 s70, s71, 74368\n"
"buffer_load_dwordx4 v16, s[40:43], s70 offen lds\n"
"s_add_u32 m0, m0, 0x2000\n"
"s_add_u32 s70, s71, 768\n"
"buffer_load_dwordx4 v16, s[40:43], s70 offen lds\n"
"s_add_u32 m0, m0, 0x2000\n"
"s_add_u32 s70, s71, 74496\n"
"buffer_load_dwordx4 v16, s[40:43], s70 offen lds\n"
"s_add_u32 m0, m0, 0x2000\n"
"s_add_u32 s70, s71, 896\n"
"buffer_load_dwordx4 v16, s[40:43], s70 offen lds\n"
"s_add_u32 m0, m0, 0x2000\n"
"s_add_u32 s70, s71, 74624\n"
"buffer_load_dwordx4 v16, s[40:43], s70 offen lds\n"
"s_add_u32 m0, m0, 0x2000\n"
"s_add_u32 s70, s71, 1024\n"
"buffer_load_dwordx4 v16, s[40:43], s70 offen lds\n"
"s_add_u32 m0, m0, 0x2000\n"
"s_add_u32 s70, s71, 74752\n"
"buffer_load_dwordx4 v16, s[40:43], s70 offen lds\n"
"s_add_u32 m0, m0, 0x2000\n"
"v_lshrrev_b32 v13, 3, v0\n"
"v_and_b32 v14, 7, v0\n"
"s_getpc_b64 s[76:77]\n"
".Lk1_pcref:\n"
"s_sub_u32 s76, s76, .Lk1_pcref-k1_code_start\n"
"s_subb_u32 s77, s77, 0\n"
"s_and_b32 s77, s77, 0xffff\n"
"s_mov_b32 s78, kb_code_end-k1_code_start\n"
"s_mov_b32 s79, 0x20000\n"
"v_lshlrev_b32 v28, 7, v0\n"
"s_lshl_b32 s70, s65, 13\n"
"buffer_load_dword v29, v28, s[76:79], s70 offen\n"
"s_cmp_eq_u32 %[bid], 0\n"
"s_cbranch_scc0 .Lkc1_zs_1_%=\n"
"s_cmp_eq_u32 s65, 7\n"
"s_cbranch_scc0 .Lkc1_zs_1_%=\n"
"v_lshlrev_b32 v15, 7, v0\n"
"v_mov_b32 v16, 0\n"
"s_mov_b32 s70, 40108032\n"
"buffer_store_dword v16, v15, s[44:47], s70 offen sc1\n"
"v_lshlrev_b32 v15, 4, v0\n"
"v_mov_b32 v18, 0x3f803f80\n"
"v_mov_b32 v19, 0x3f803f80\n"
"v_mov_b32 v20, 0x3f803f80\n"
"v_mov_b32 v21, 0x3f803f80\n"
"s_mov_b32 s70, 40116224\n"
"buffer_store_dwordx4 v[18:21], v15, s[44:47], s70 offen sc1\n"
"buffer_store_dwordx4 v[18:21], v15, s[44:47], s70 offen offset:1024 sc1\n"
"s_waitcnt vmcnt(0)\n"
".Lkc1_zs_1_%=:\n"
"s_lshl_b32 s69, %[qcol0], 1\n"
"s_mul_i32 s70, s65, 27648\n"
"s_add_u32 s69, s69, s70\n"
"s_lshl_b32 s82, %[f0], 1\n"
"s_mul_i32 s70, s65, 73728\n"
"s_add_u32 s82, s82, s70\n"
"s_mul_i32 s70, s93, 72\n"
"s_lshr_b32 s71, %[qcol0], 4\n"
"s_add_u32 s70, s70, s71\n"
"s_mul_i32 s72, s93, 36\n"
"s_add_u32 s72, s72, s71\n"
"s_add_u32 s72, s72, 4536\n"
"s_cmp_eq_u32 s80, 0\n"
"s_cselect_b32 s70, s70, s72\n"
"s_lshl_b32 s71, s67, 1\n"
"s_add_u32 s70, s70, s71\n"
"s_lshl_b32 s85, s70, 10\n"
"s_mul_i32 s70, s93, 288\n"
"s_lshr_b32 s72, %[f0], 4\n"
"s_add_u32 s70, s70, s72\n"
"s_add_u32 s70, s70, s71\n"
"s_add_u32 s70, s70, 6912\n"
"s_lshl_b32 s90, s70, 10\n"
"s_lshl_b32 s70, %[bid], 3\n"
"s_add_u32 s70, s70, s65\n"
"s_mul_i32 s92, s70, 2688\n"
"v_bfe_u32 v15, v0, 4, 1\n"
"v_lshlrev_b32 v15, 2, v15\n"
"v_xor_b32 v15, v14, v15\n"
"v_lshlrev_b32 v15, 4, v15\n"
"v_mul_u32_u24 v7, 3456, v13\n"
"v_add_u32 v7, v7, v15\n"
"v_add_u32 v8, 221184, v7\n"
"v_add_u32 v9, 442368, v7\n"
"v_mul_u32_u24 v4, 9216, v13\n"
"v_add_u32 v4, v4, v15\n"
"v_add_u32 v5, 589824, v4\n"
"v_add_u32 v6, 1179648, v4\n"
"v_lshlrev_b32 v10, 4, v0\n"
"v_lshrrev_b32 v13, 5, v0\n"
"v_lshlrev_b32 v1, 10, v13\n"
"v_bfe_u32 v14, v0, 2, 2\n"
"v_lshl_add_u32 v1, v14, 7, v1\n"
"v_lshrrev_b32 v14, 1, v14\n"
"v_xor_b32 v14, s67, v14\n"
"v_lshl_add_u32 v1, v14, 6, v1\n"
"v_bfe_u32 v14, v0, 4, 1\n"
"v_lshl_add_u32 v1, v14, 5, v1\n"
"v_and_b32 v14, 3, v0\n"
"v_lshl_add_u32 v1, v14, 3, v1\n"
"v_add_u32 v2, 49152, v1\n"
"v_add_u32 v3, 98304, v1\n"
"v_and_b32 v14, 31, v0\n"
"s_and_b32 s70, s64, 7\n"
"s_lshl_b32 s70, s70, 3\n"
"s_add_u32 s70, s70, s65\n"
"s_mul_i32 s70, s70, 9\n"
"s_lshl_b32 s71, s65, 3\n"
"s_sub_u32 s70, s70, s71\n"
"v_min_u32 v11, 8, v0\n"
"v_add_u32 v11, s70, v11\n"
"v_mul_i32_i24 v11, 9216, v11\n"
"v_min_u32 v12, 2, v0\n"
"v_lshlrev_b32 v12, 7, v12\n"
"s_lshl_b32 s70, s66, 12\n"
"v_lshlrev_b32 v15, 7, v14\n"
"v_add_u32 v15, s70, v15\n"
"v_bfe_u32 v16, v0, 1, 3\n"
"s_lshl_b32 s71, s67, 2\n"
"v_add_u32 v17, 0, v13\n"
"v_add_u32 v17, s71, v17\n"
"v_xor_b32 v17, v17, v16\n"
"v_lshl_add_u32 v17, v17, 4, v15\n"
"v_add_u32 v19, 0x10000, v17\n"
"v_add_u32 v18, 2, v13\n"
"v_add_u32 v18, s71, v18\n"
"v_xor_b32 v18, v18, v16\n"
"v_lshl_add_u32 v18, v18, 4, v15\n"
"v_add_u32 v20, 0x10000, v18\n"
"s_lshl_b32 s70, s65, 8\n"
"s_add_u32 s70, s70, 155648\n"
"s_xor_b32 s72, s65, 4\n"
"s_lshl_b32 s72, s72, 8\n"
"s_add_u32 s72, s72, 155648\n"
"v_lshlrev_b32 v14, 3, v14\n"
"v_add_u32 v21, s70, v14\n"
"v_add_u32 v22, s72, v14\n"
"v_mov_b32 v248, 0\n"
"v_mov_b32 v249, 0\n"
"v_mov_b32 v250, 0\n"
"v_mov_b32 v251, 0\n"
"v_mov_b32 v252, 0\n"
"v_mov_b32 v253, 0\n"
"v_mov_b32 v254, 0\n"
"v_mov_b32 v255, 0\n"
"v_mov_b32 v25, 0xffff0000\n"
"s_waitcnt vmcnt(13)\n"
"s_barrier\n"
"ds_read_b128 v[32:35], v17 offset:0\n"
"ds_read_b128 v[36:39], v18 offset:0\n"
"ds_read_b128 v[40:43], v17 offset:16384\n"
"ds_read_b128 v[44:47], v18 offset:16384\n"
"ds_read_b128 v[48:51], v17 offset:32768\n"
"ds_read_b128 v[52:55], v18 offset:32768\n"
"s_waitcnt lgkmcnt(5)\n"
"v_lshlrev_b32 v88, 16, v32\n"
"v_and_b32 v89, v25, v32\n"
"v_lshlrev_b32 v90, 16, v33\n"
"v_and_b32 v91, v25, v33\n"
"v_lshlrev_b32 v92, 16, v34\n"
"v_and_b32 v93, v25, v34\n"
"v_lshlrev_b32 v94, 16, v35\n"
"v_and_b32 v95, v25, v35\n"
"v_pk_add_f32 v[248:249], v[248:249], v[88:89]\n"
"v_pk_fma_f32 v[250:251], v[88:89], v[88:89], v[250:251]\n"
"v_pk_add_f32 v[252:253], v[252:253], v[90:91]\n"
"v_pk_fma_f32 v[254:255], v[90:91], v[90:91], v[254:255]\n"
"v_pk_add_f32 v[248:249], v[248:249], v[92:93]\n"
"v_pk_fma_f32 v[250:251], v[92:93], v[92:93], v[250:251]\n"
"v_pk_add_f32 v[252:253], v[252:253], v[94:95]\n"
"v_pk_fma_f32 v[254:255], v[94:95], v[94:95], v[254:255]\n"
"s_waitcnt lgkmcnt(4)\n"
"v_lshlrev_b32 v96, 16, v36\n"
"v_and_b32 v97, v25, v36\n"
"v_lshlrev_b32 v98, 16, v37\n"
"v_and_b32 v99, v25, v37\n"
"v_lshlrev_b32 v100, 16, v38\n"
"v_and_b32 v101, v25, v38\n"
"v_lshlrev_b32 v102, 16, v39\n"
"v_and_b32 v103, v25, v39\n"
"v_pk_add_f32 v[248:249], v[248:249], v[96:97]\n"
"v_pk_fma_f32 v[250:251], v[96:97], v[96:97], v[250:251]\n"
"v_pk_add_f32 v[252:253], v[252:253], v[98:99]\n"
"v_pk_fma_f32 v[254:255], v[98:99], v[98:99], v[254:255]\n"
"v_pk_add_f32 v[248:249], v[248:249], v[100:101]\n"
"v_pk_fma_f32 v[250:251], v[100:101], v[100:101], v[250:251]\n"
"v_pk_add_f32 v[252:253], v[252:253], v[102:103]\n"
"v_pk_fma_f32 v[254:255], v[102:103], v[102:103], v[254:255]\n"
"s_waitcnt lgkmcnt(3)\n"
"v_lshlrev_b32 v104, 16, v40\n"
"v_and_b32 v105, v25, v40\n"
"v_lshlrev_b32 v106, 16, v41\n"
"v_and_b32 v107, v25, v41\n"
"v_lshlrev_b32 v108, 16, v42\n"
"v_and_b32 v109, v25, v42\n"
"v_lshlrev_b32 v110, 16, v43\n"
"v_and_b32 v111, v25, v43\n"
"v_pk_add_f32 v[248:249], v[248:249], v[104:105]\n"
"v_pk_fma_f32 v[250:251], v[104:105], v[104:105], v[250:251]\n"
"v_pk_add_f32 v[252:253], v[252:253], v[106:107]\n"
"v_pk_fma_f32 v[254:255], v[106:107], v[106:107], v[254:255]\n"
"v_pk_add_f32 v[248:249], v[248:249], v[108:109]\n"
"v_pk_fma_f32 v[250:251], v[108:109], v[108:109], v[250:251]\n"
"v_pk_add_f32 v[252:253], v[252:253], v[110:111]\n"
"v_pk_fma_f32 v[254:255], v[110:111], v[110:111], v[254:255]\n"
"s_waitcnt lgkmcnt(2)\n"
"v_lshlrev_b32 v112, 16, v44\n"
"v_and_b32 v113, v25, v44\n"
"v_lshlrev_b32 v114, 16, v45\n"
"v_and_b32 v115, v25, v45\n"
"v_lshlrev_b32 v116, 16, v46\n"
"v_and_b32 v117, v25, v46\n"
"v_lshlrev_b32 v118, 16, v47\n"
"v_and_b32 v119, v25, v47\n"
"v_pk_add_f32 v[248:249], v[248:249], v[112:113]\n"
"v_pk_fma_f32 v[250:251], v[112:113], v[112:113], v[250:251]\n"
"v_pk_add_f32 v[252:253], v[252:253], v[114:115]\n"
"v_pk_fma_f32 v[254:255], v[114:115], v[114:115], v[254:255]\n"
"v_pk_add_f32 v[248:249], v[248:249], v[116:117]\n"
"v_pk_fma_f32 v[250:251], v[116:117], v[116:117], v[250:251]\n"
"v_pk_add_f32 v[252:253], v[252:253], v[118:119]\n"
"v_pk_fma_f32 v[254:255], v[118:119], v[118:119], v[254:255]\n"
"s_waitcnt lgkmcnt(1)\n"
"v_lshlrev_b32 v120, 16, v48\n"
"v_and_b32 v121, v25, v48\n"
"v_lshlrev_b32 v122, 16, v49\n"
"v_and_b32 v123, v25, v49\n"
"v_lshlrev_b32 v124, 16, v50\n"
"v_and_b32 v125, v25, v50\n"
"v_lshlrev_b32 v126, 16, v51\n"
"v_and_b32 v127, v25, v51\n"
"v_pk_add_f32 v[248:249], v[248:249], v[120:121]\n"
"v_pk_fma_f32 v[250:251], v[120:121], v[120:121], v[250:251]\n"
"v_pk_add_f32 v[252:253], v[252:253], v[122:123]\n"
"v_pk_fma_f32 v[254:255], v[122:123], v[122:123], v[254:255]\n"
"v_pk_add_f32 v[248:249], v[248:249], v[124:125]\n"
"v_pk_fma_f32 v[250:251], v[124:125], v[124:125], v[250:251]\n"
"v_pk_add_f32 v[252:253], v[252:253], v[126:127]\n"
"v_pk_fma_f32 v[254:255], v[126:127], v[126:127], v[254:255]\n"
"s_waitcnt lgkmcnt(0)\n"
"v_lshlrev_b32 v128, 16, v52\n"
"v_and_b32 v129, v25, v52\n"
"v_lshlrev_b32 v130, 16, v53\n"
"v_and_b32 v131, v25, v53\n"
"v_lshlrev_b32 v132, 16, v54\n"
"v_and_b32 v133, v25, v54\n"
"v_lshlrev_b32 v134, 16, v55\n"
"v_and_b32 v135, v25, v55\n"
"v_pk_add_f32 v[248:249], v[248:249], v[128:129]\n"
"v_pk_fma_f32 v[250:251], v[128:129], v[128:129], v[250:251]\n"
"v_pk_add_f32 v[252:253], v[252:253], v[130:131]\n"
"v_pk_fma_f32 v[254:255], v[130:131], v[130:131], v[254:255]\n"
"v_pk_add_f32 v[248:249], v[248:249], v[132:133]\n"
"v_pk_fma_f32 v[250:251], v[132:133], v[132:133], v[250:251]\n"
"v_pk_add_f32 v[252:253], v[252:253], v[134:135]\n"
"v_pk_fma_f32 v[254:255], v[134:135], v[134:135], v[254:255]\n"
"s_waitcnt vmcnt(7)\n"
"s_barrier\n"
"ds_read_b128 v[56:59], v17 offset:49152\n"
"ds_read_b128 v[60:63], v18 offset:49152\n"
"ds_read_b128 v[64:67], v19 offset:0\n"
"ds_read_b128 v[68:71], v20 offset:0\n"
"ds_read_b128 v[72:75], v19 offset:16384\n"
"ds_read_b128 v[76:79], v20 offset:16384\n"
"s_cmp_eq_u32 s68, 0\n"
"s_cselect_b32 s40, s52, s48\n"
"s_cselect_b32 s41, s53, s49\n"
"s_cselect_b32 s42, s54, s50\n"
"s_cselect_b32 s72, s82, s69\n"
"s_mov_b32 s74, 1769472\n"
"s_cselect_b32 s73, s74, 663552\n"
"s_cselect_b64 vcc, -1, 0\n"
"v_cndmask_b32 v26, v7, v4, vcc\n"
"v_cndmask_b32 v27, v8, v5, vcc\n"
"v_cndmask_b32 v28, v9, v6, vcc\n"
"s_add_u32 m0, s91, 0\n"
"s_nop 0\n"
"buffer_load_dwordx4 v26, s[40:43], s72 offen lds\n"
"s_waitcnt lgkmcnt(5)\n"
"v_lshlrev_b32 v136, 16, v56\n"
"v_and_b32 v137, v25, v56\n"
"v_lshlrev_b32 v138, 16, v57\n"
"v_and_b32 v139, v25, v57\n"
"v_lshlrev_b32 v140, 16, v58\n"
"v_and_b32 v141, v25, v58\n"
"v_lshlrev_b32 v142, 16, v59\n"
"v_and_b32 v143, v25, v59\n"
"v_pk_add_f32 v[248:249], v[248:249], v[136:137]\n"
"v_pk_fma_f32 v[250:251], v[136:137], v[136:137], v[250:251]\n"
"v_pk_add_f32 v[252:253], v[252:253], v[138:139]\n"
"v_pk_fma_f32 v[254:255], v[138:139], v[138:139], v[254:255]\n"
"v_pk_add_f32 v[248:249], v[248:249], v[140:141]\n"
"v_pk_fma_f32 v[250:251], v[140:141], v[140:141], v[250:251]\n"
"v_pk_add_f32 v[252:253], v[252:253], v[142:143]\n"
"v_pk_fma_f32 v[254:255], v[142:143], v[142:143], v[254:255]\n"
"s_add_u32 m0, s91, 8192\n"
"s_nop 0\n"
"buffer_load_dwordx4 v27, s[40:43], s72 offen lds\n"
"s_waitcnt lgkmcnt(4)\n"
"v_lshlrev_b32 v144, 16, v60\n"
"v_and_b32 v145, v25, v60\n"
"v_lshlrev_b32 v146, 16, v61\n"
"v_and_b32 v147, v25, v61\n"
"v_lshlrev_b32 v148, 16, v62\n"
"v_and_b32 v149, v25, v62\n"
"v_lshlrev_b32 v150, 16, v63\n"
"v_and_b32 v151, v25, v63\n"
"v_pk_add_f32 v[248:249], v[248:249], v[144:145]\n"
"v_pk_fma_f32 v[250:251], v[144:145], v[144:145], v[250:251]\n"
"v_pk_add_f32 v[252:253], v[252:253], v[146:147]\n"
"v_pk_fma_f32 v[254:255], v[146:147], v[146:147], v[254:255]\n"
"v_pk_add_f32 v[248:249], v[248:249], v[148:149]\n"
"v_pk_fma_f32 v[250:251], v[148:149], v[148:149], v[250:251]\n"
"v_pk_add_f32 v[252:253], v[252:253], v[150:151]\n"
"v_pk_fma_f32 v[254:255], v[150:151], v[150:151], v[254:255]\n"
"s_add_u32 m0, s91, 16384\n"
"s_nop 0\n"
"buffer_load_dwordx4 v28, s[40:43], s72 offen lds\n"
"s_add_u32 s72, s72, s73\n"
"s_waitcnt lgkmcnt(3)\n"
"v_lshlrev_b32 v152, 16, v64\n"
"v_and_b32 v153, v25, v64\n"
"v_lshlrev_b32 v154, 16, v65\n"
"v_and_b32 v155, v25, v65\n"
"v_lshlrev_b32 v156, 16, v66\n"
"v_and_b32 v157, v25, v66\n"
"v_lshlrev_b32 v158, 16, v67\n"
"v_and_b32 v159, v25, v67\n"
"v_pk_add_f32 v[248:249], v[248:249], v[152:153]\n"
"v_pk_fma_f32 v[250:251], v[152:153], v[152:153], v[250:251]\n"
"v_pk_add_f32 v[252:253], v[252:253], v[154:155]\n"
"v_pk_fma_f32 v[254:255], v[154:155], v[154:155], v[254:255]\n"
"v_pk_add_f32 v[248:249], v[248:249], v[156:157]\n"
"v_pk_fma_f32 v[250:251], v[156:157], v[156:157], v[250:251]\n"
"v_pk_add_f32 v[252:253], v[252:253], v[158:159]\n"
"v_pk_fma_f32 v[254:255], v[158:159], v[158:159], v[254:255]\n"
"s_add_u32 m0, s91, 24576\n"
"s_nop 0\n"
"buffer_load_dwordx4 v26, s[40:43], s72 offen lds\n"
"s_waitcnt lgkmcnt(2)\n"
"v_lshlrev_b32 v160, 16, v68\n"
"v_and_b32 v161, v25, v68\n"
"v_lshlrev_b32 v162, 16, v69\n"
"v_and_b32 v163, v25, v69\n"
"v_lshlrev_b32 v164, 16, v70\n"
"v_and_b32 v165, v25, v70\n"
"v_lshlrev_b32 v166, 16, v71\n"
"v_and_b32 v167, v25, v71\n"
"v_pk_add_f32 v[248:249], v[248:249], v[160:161]\n"
"v_pk_fma_f32 v[250:251], v[160:161], v[160:161], v[250:251]\n"
"v_pk_add_f32 v[252:253], v[252:253], v[162:163]\n"
"v_pk_fma_f32 v[254:255], v[162:163], v[162:163], v[254:255]\n"
"v_pk_add_f32 v[248:249], v[248:249], v[164:165]\n"
"v_pk_fma_f32 v[250:251], v[164:165], v[164:165], v[250:251]\n"
"v_pk_add_f32 v[252:253], v[252:253], v[166:167]\n"
"v_pk_fma_f32 v[254:255], v[166:167], v[166:167], v[254:255]\n"
"s_add_u32 m0, s91, 32768\n"
"s_nop 0\n"
"buffer_load_dwordx4 v27, s[40:43], s72 offen lds\n"
"s_waitcnt lgkmcnt(1)\n"
"v_lshlrev_b32 v168, 16, v72\n"
"v_and_b32 v169, v25, v72\n"
"v_lshlrev_b32 v170, 16, v73\n"
"v_and_b32 v171, v25, v73\n"
"v_lshlrev_b32 v172, 16, v74\n"
"v_and_b32 v173, v25, v74\n"
"v_lshlrev_b32 v174, 16, v75\n"
"v_and_b32 v175, v25, v75\n"
"v_pk_add_f32 v[248:249], v[248:249], v[168:169]\n"
"v_pk_fma_f32 v[250:251], v[168:169], v[168:169], v[250:251]\n"
"v_pk_add_f32 v[252:253], v[252:253], v[170:171]\n"
"v_pk_fma_f32 v[254:255], v[170:171], v[170:171], v[254:255]\n"
"v_pk_add_f32 v[248:249], v[248:249], v[172:173]\n"
"v_pk_fma_f32 v[250:251], v[172:173], v[172:173], v[250:251]\n"
"v_pk_add_f32 v[252:253], v[252:253], v[174:175]\n"
"v_pk_fma_f32 v[254:255], v[174:175], v[174:175], v[254:255]\n"
"s_add_u32 m0, s91, 40960\n"
"s_nop 0\n"
"buffer_load_dwordx4 v28, s[40:43], s72 offen lds\n"
"s_add_u32 s72, s72, s73\n"
"s_waitcnt lgkmcnt(0)\n"
"v_lshlrev_b32 v176, 16, v76\n"
"v_and_b32 v177, v25, v76\n"
"v_lshlrev_b32 v178, 16, v77\n"
"v_and_b32 v179, v25, v77\n"
"v_lshlrev_b32 v180, 16, v78\n"
"v_and_b32 v181, v25, v78\n"
"v_lshlrev_b32 v182, 16, v79\n"
"v_and_b32 v183, v25, v79\n"
"v_pk_add_f32 v[248:249], v[248:249], v[176:177]\n"
"v_pk_fma_f32 v[250:251], v[176:177], v[176:177], v[250:251]\n"
"v_pk_add_f32 v[252:253], v[252:253], v[178:179]\n"
"v_pk_fma_f32 v[254:255], v[178:179], v[178:179], v[254:255]\n"
"v_pk_add_f32 v[248:249], v[248:249], v[180:181]\n"
"v_pk_fma_f32 v[250:251], v[180:181], v[180:181], v[250:251]\n"
"v_pk_add_f32 v[252:253], v[252:253], v[182:183]\n"
"v_pk_fma_f32 v[254:255], v[182:183], v[182:183], v[254:255]\n"
"s_waitcnt vmcnt(7)\n"
"s_barrier\n"
"ds_read_b128 v[80:83], v19 offset:32768\n"
"ds_read_b128 v[84:87], v20 offset:32768\n"
"ds_read_b128 v[232:235], v19 offset:49152\n"
"ds_read_b128 v[236:239], v20 offset:49152\n"
"v_add_u32 v16, 0x20000, v17\n"
"ds_read_b128 v[240:243], v16\n"
"v_add_u32 v16, 0x20000, v18\n"
"ds_read_b128 v[244:247], v16\n"
"s_add_u32 m0, s91, 49152\n"
"s_nop 0\n"
"buffer_load_dwordx4 v26, s[40:43], s72 offen lds\n"
"s_waitcnt lgkmcnt(5)\n"
"v_lshlrev_b32 v184, 16, v80\n"
"v_and_b32 v185, v25, v80\n"
"v_lshlrev_b32 v186, 16, v81\n"
"v_and_b32 v187, v25, v81\n"
"v_lshlrev_b32 v188, 16, v82\n"
"v_and_b32 v189, v25, v82\n"
"v_lshlrev_b32 v190, 16, v83\n"
"v_and_b32 v191, v25, v83\n"
"v_pk_add_f32 v[248:249], v[248:249], v[184:185]\n"
"v_pk_fma_f32 v[250:251], v[184:185], v[184:185], v[250:251]\n"
"v_pk_add_f32 v[252:253], v[252:253], v[186:187]\n"
"v_pk_fma_f32 v[254:255], v[186:187], v[186:187], v[254:255]\n"
"v_pk_add_f32 v[248:249], v[248:249], v[188:189]\n"
"v_pk_fma_f32 v[250:251], v[188:189], v[188:189], v[250:251]\n"
"v_pk_add_f32 v[252:253], v[252:253], v[190:191]\n"
"v_pk_fma_f32 v[254:255], v[190:191], v[190:191], v[254:255]\n"
"s_add_u32 m0, s91, 57344\n"
"s_nop 0\n"
"buffer_load_dwordx4 v27, s[40:43], s72 offen lds\n"
"s_waitcnt lgkmcnt(4)\n"
"v_lshlrev_b32 v192, 16, v84\n"
"v_and_b32 v193, v25, v84\n"
"v_lshlrev_b32 v194, 16, v85\n"
"v_and_b32 v195, v25, v85\n"
"v_lshlrev_b32 v196, 16, v86\n"
"v_and_b32 v197, v25, v86\n"
"v_lshlrev_b32 v198, 16, v87\n"
"v_and_b32 v199, v25, v87\n"
"v_pk_add_f32 v[248:249], v[248:249], v[192:193]\n"
"v_pk_fma_f32 v[250:251], v[192:193], v[192:193], v[250:251]\n"
"v_pk_add_f32 v[252:253], v[252:253], v[194:195]\n"
"v_pk_fma_f32 v[254:255], v[194:195], v[194:195], v[254:255]\n"
"v_pk_add_f32 v[248:249], v[248:249], v[196:197]\n"
"v_pk_fma_f32 v[250:251], v[196:197], v[196:197], v[250:251]\n"
"v_pk_add_f32 v[252:253], v[252:253], v[198:199]\n"
"v_pk_fma_f32 v[254:255], v[198:199], v[198:199], v[254:255]\n"
"s_add_u32 m0, s91, 65536\n"
"s_nop 0\n"
"buffer_load_dwordx4 v28, s[40:43], s72 offen lds\n"
"s_add_u32 s72, s72, s73\n"
"s_waitcnt lgkmcnt(3)\n"
"v_lshlrev_b32 v200, 16, v232\n"
"v_and_b32 v201, v25, v232\n"
"v_lshlrev_b32 v202, 16, v233\n"
"v_and_b32 v203, v25, v233\n"
"v_lshlrev_b32 v204, 16, v234\n"
"v_and_b32 v205, v25, v234\n"
"v_lshlrev_b32 v206, 16, v235\n"
"v_and_b32 v207, v25, v235\n"
"v_pk_add_f32 v[248:249], v[248:249], v[200:201]\n"
"v_pk_fma_f32 v[250:251], v[200:201], v[200:201], v[250:251]\n"
"v_pk_add_f32 v[252:253], v[252:253], v[202:203]\n"
"v_pk_fma_f32 v[254:255], v[202:203], v[202:203], v[254:255]\n"
"v_pk_add_f32 v[248:249], v[248:249], v[204:205]\n"
"v_pk_fma_f32 v[250:251], v[204:205], v[204:205], v[250:251]\n"
"v_pk_add_f32 v[252:253], v[252:253], v[206:207]\n"
"v_pk_fma_f32 v[254:255], v[206:207], v[206:207], v[254:255]\n"
"s_waitcnt lgkmcnt(2)\n"
"v_lshlrev_b32 v208, 16, v236\n"
"v_and_b32 v209, v25, v236\n"
"v_lshlrev_b32 v210, 16, v237\n"
"v_and_b32 v211, v25, v237\n"
"v_lshlrev_b32 v212, 16, v238\n"
"v_and_b32 v213, v25, v238\n"
"v_lshlrev_b32 v214, 16, v239\n"
"v_and_b32 v215, v25, v239\n"
"v_pk_add_f32 v[248:249], v[248:249], v[208:209]\n"
"v_pk_fma_f32 v[250:251], v[208:209], v[208:209], v[250:251]\n"
"v_pk_add_f32 v[252:253], v[252:253], v[210:211]\n"
"v_pk_fma_f32 v[254:255], v[210:211], v[210:211], v[254:255]\n"
"v_pk_add_f32 v[248:249], v[248:249], v[212:213]\n"
"v_pk_fma_f32 v[250:251], v[212:213], v[212:213], v[250:251]\n"
"v_pk_add_f32 v[252:253], v[252:253], v[214:215]\n"
"v_pk_fma_f32 v[254:255], v[214:215], v[214:215], v[254:255]\n"
"s_waitcnt lgkmcnt(1)\n"
"v_lshlrev_b32 v216, 16, v240\n"
"v_and_b32 v217, v25, v240\n"
"v_lshlrev_b32 v218, 16, v241\n"
"v_and_b32 v219, v25, v241\n"
"v_lshlrev_b32 v220, 16, v242\n"
"v_and_b32 v221, v25, v242\n"
"v_lshlrev_b32 v222, 16, v243\n"
"v_and_b32 v223, v25, v243\n"
"v_pk_add_f32 v[248:249], v[248:249], v[216:217]\n"
"v_pk_fma_f32 v[250:251], v[216:217], v[216:217], v[250:251]\n"
"v_pk_add_f32 v[252:253], v[252:253], v[218:219]\n"
"v_pk_fma_f32 v[254:255], v[218:219], v[218:219], v[254:255]\n"
"v_pk_add_f32 v[248:249], v[248:249], v[220:221]\n"
"v_pk_fma_f32 v[250:251], v[220:221], v[220:221], v[250:251]\n"
"v_pk_add_f32 v[252:253], v[252:253], v[222:223]\n"
"v_pk_fma_f32 v[254:255], v[222:223], v[222:223], v[254:255]\n"
"s_waitcnt lgkmcnt(0)\n"
"v_lshlrev_b32 v224, 16, v244\n"
"v_and_b32 v225, v25, v244\n"
"v_lshlrev_b32 v226, 16, v245\n"
"v_and_b32 v227, v25, v245\n"
"v_lshlrev_b32 v228, 16, v246\n"
"v_and_b32 v229, v25, v246\n"
"v_lshlrev_b32 v230, 16, v247\n"
"v_and_b32 v231, v25, v247\n"
"v_pk_add_f32 v[248:249], v[248:249], v[224:225]\n"
"v_pk_fma_f32 v[250:251], v[224:225], v[224:225], v[250:251]\n"
"v_pk_add_f32 v[252:253], v[252:253], v[226:227]\n"
"v_pk_fma_f32 v[254:255], v[226:227], v[226:227], v[254:255]\n"
"v_pk_add_f32 v[248:249], v[248:249], v[228:229]\n"
"v_pk_fma_f32 v[250:251], v[228:229], v[228:229], v[250:251]\n"
"v_pk_add_f32 v[252:253], v[252:253], v[230:231]\n"
"v_pk_fma_f32 v[254:255], v[230:231], v[230:231], v[254:255]\n"
"v_pk_add_f32 v[248:249], v[248:249], v[252:253]\n"
"v_pk_add_f32 v[250:251], v[250:251], v[254:255]\n"
"s_nop 0\n"
"v_add_f32 v248, v248, v249\n"
"v_add_f32 v250, v250, v251\n"
"s_nop 0\n"
"v_mov_b32 v252, v248\n"
"v_mov_b32 v254, v250\n"
"s_nop 1\n"
"v_permlane32_swap_b32 v248, v252\n"
"v_permlane32_swap_b32 v250, v254\n"
"s_nop 1\n"
"v_add_f32 v248, v248, v252\n"
"v_add_f32 v249, v250, v254\n"
"ds_write_b64 v21, v[248:249]\n"
"s_waitcnt lgkmcnt(0)\n"
"s_barrier\n"
"ds_read_b64 v[252:253], v22\n"
"s_waitcnt lgkmcnt(0)\n"
"v_add_f32 v248, v248, v252\n"
"v_add_f32 v249, v249, v253\n"
"v_mul_f32 v248, 0x3ae38e39, v248\n"
"v_mul_f32 v249, 0x3ae38e39, v249\n"
"v_fma_f32 v249, -v248, v248, v249\n"
"v_max_f32 v249, 0, v249\n"
"v_add_f32 v249, 0x3727c5ac, v249\n"
"v_rsq_f32 v250, v249\n"
"s_nop 0\n"
"v_mov_b32 v251, v250\n"
"v_mul_f32 v254, v248, v250\n"
"v_sub_f32 v254, 0, v254\n"
"v_mov_b32 v255, v254\n"
"s_mul_i32 s70, s66, 20480\n"
"s_lshl_b32 s71, s67, 11\n"
"s_add_u32 s71, s71, s70\n"
"s_add_u32 s71, s71, 73728\n"
"s_add_u32 s70, s70, 73728\n"
"v_add_u32 v23, s71, v10\n"
"v_add_u32 v24, s70, v10\n"
"v_pk_fma_f32 v[88:89], v[88:89], v[250:251], v[254:255]\n"
"v_pk_fma_f32 v[90:91], v[90:91], v[250:251], v[254:255]\n"
"v_pk_fma_f32 v[92:93], v[92:93], v[250:251], v[254:255]\n"
"v_pk_fma_f32 v[94:95], v[94:95], v[250:251], v[254:255]\n"
"v_cvt_pk_bf16_f32 v32, v88, v89\n"
"v_cvt_pk_bf16_f32 v33, v90, v91\n"
"v_cvt_pk_bf16_f32 v34, v92, v93\n"
"v_cvt_pk_bf16_f32 v35, v94, v95\n"
"ds_write_b128 v23, v[32:35] offset:0\n"
"v_pk_fma_f32 v[96:97], v[96:97], v[250:251], v[254:255]\n"
"v_pk_fma_f32 v[98:99], v[98:99], v[250:251], v[254:255]\n"
"v_pk_fma_f32 v[100:101], v[100:101], v[250:251], v[254:255]\n"
"v_pk_fma_f32 v[102:103], v[102:103], v[250:251], v[254:255]\n"
"v_cvt_pk_bf16_f32 v36, v96, v97\n"
"v_cvt_pk_bf16_f32 v37, v98, v99\n"
"v_cvt_pk_bf16_f32 v38, v100, v101\n"
"v_cvt_pk_bf16_f32 v39, v102, v103\n"
"ds_write_b128 v23, v[36:39] offset:1024\n"
"v_pk_fma_f32 v[104:105], v[104:105], v[250:251], v[254:255]\n"
"v_pk_fma_f32 v[106:107], v[106:107], v[250:251], v[254:255]\n"
"v_pk_fma_f32 v[108:109], v[108:109], v[250:251], v[254:255]\n"
"v_pk_fma_f32 v[110:111], v[110:111], v[250:251], v[254:255]\n"
"v_cvt_pk_bf16_f32 v40, v104, v105\n"
"v_cvt_pk_bf16_f32 v41, v106, v107\n"
"v_cvt_pk_bf16_f32 v42, v108, v109\n"
"v_cvt_pk_bf16_f32 v43, v110, v111\n"
"ds_write_b128 v23, v[40:43] offset:4096\n"
"v_pk_fma_f32 v[112:113], v[112:113], v[250:251], v[254:255]\n"
"v_pk_fma_f32 v[114:115], v[114:115], v[250:251], v[254:255]\n"
"v_pk_fma_f32 v[116:117], v[116:117], v[250:251], v[254:255]\n"
"v_pk_fma_f32 v[118:119], v[118:119], v[250:251], v[254:255]\n"
"v_cvt_pk_bf16_f32 v44, v112, v113\n"
"v_cvt_pk_bf16_f32 v45, v114, v115\n"
"v_cvt_pk_bf16_f32 v46, v116, v117\n"
"v_cvt_pk_bf16_f32 v47, v118, v119\n"
"ds_write_b128 v23, v[44:47] offset:5120\n"
"v_pk_fma_f32 v[120:121], v[120:121], v[250:251], v[254:255]\n"
"v_pk_fma_f32 v[122:123], v[122:123], v[250:251], v[254:255]\n"
"v_pk_fma_f32 v[124:125], v[124:125], v[250:251], v[254:255]\n"
"v_pk_fma_f32 v[126:127], v[126:127], v[250:251], v[254:255]\n"
"v_cvt_pk_bf16_f32 v48, v120, v121\n"
"v_cvt_pk_bf16_f32 v49, v122, v123\n"
"v_cvt_pk_bf16_f32 v50, v124, v125\n"
"v_cvt_pk_bf16_f32 v51, v126, v127\n"
"ds_write_b128 v23, v[48:51] offset:8192\n"
"v_pk_fma_f32 v[128:129], v[128:129], v[250:251], v[254:255]\n"
"v_pk_fma_f32 v[130:131], v[130:131], v[250:251], v[254:255]\n"
"v_pk_fma_f32 v[132:133], v[132:133], v[250:251], v[254:255]\n"
"v_pk_fma_f32 v[134:135], v[134:135], v[250:251], v[254:255]\n"
"v_cvt_pk_bf16_f32 v52, v128, v129\n"
"v_cvt_pk_bf16_f32 v53, v130, v131\n"
"v_cvt_pk_bf16_f32 v54, v132, v133\n"
"v_cvt_pk_bf16_f32 v55, v134, v135\n"
"ds_write_b128 v23, v[52:55] offset:9216\n"
"v_pk_fma_f32 v[136:137], v[136:137], v[250:251], v[254:255]\n"
"v_pk_fma_f32 v[138:139], v[138:139], v[250:251], v[254:255]\n"
"v_pk_fma_f32 v[140:141], v[140:141], v[250:251], v[254:255]\n"
"v_pk_fma_f32 v[142:143], v[142:143], v[250:251], v[254:255]\n"
"v_cvt_pk_bf16_f32 v56, v136, v137\n"
"v_cvt_pk_bf16_f32 v57, v138, v139\n"
"v_cvt_pk_bf16_f32 v58, v140, v141\n"
"v_cvt_pk_bf16_f32 v59, v142, v143\n"
"ds_write_b128 v23, v[56:59] offset:12288\n"
"v_pk_fma_f32 v[144:145], v[144:145], v[250:251], v[254:255]\n"
"v_pk_fma_f32 v[146:147], v[146:147], v[250:251], v[254:255]\n"
"v_pk_fma_f32 v[148:149], v[148:149], v[250:251], v[254:255]\n"
"v_pk_fma_f32 v[150:151], v[150:151], v[250:251], v[254:255]\n"
"v_cvt_pk_bf16_f32 v60, v144, v145\n"
"v_cvt_pk_bf16_f32 v61, v146, v147\n"
"v_cvt_pk_bf16_f32 v62, v148, v149\n"
"v_cvt_pk_bf16_f32 v63, v150, v151\n"
"ds_write_b128 v23, v[60:63] offset:13312\n"
"v_pk_fma_f32 v[152:153], v[152:153], v[250:251], v[254:255]\n"
"v_pk_fma_f32 v[154:155], v[154:155], v[250:251], v[254:255]\n"
"v_pk_fma_f32 v[156:157], v[156:157], v[250:251], v[254:255]\n"
"v_pk_fma_f32 v[158:159], v[158:159], v[250:251], v[254:255]\n"
"v_cvt_pk_bf16_f32 v64, v152, v153\n"
"v_cvt_pk_bf16_f32 v65, v154, v155\n"
"v_cvt_pk_bf16_f32 v66, v156, v157\n"
"v_cvt_pk_bf16_f32 v67, v158, v159\n"
"ds_write_b128 v23, v[64:67] offset:16384\n"
"v_pk_fma_f32 v[160:161], v[160:161], v[250:251], v[254:255]\n"
"v_pk_fma_f32 v[162:163], v[162:163], v[250:251], v[254:255]\n"
"v_pk_fma_f32 v[164:165], v[164:165], v[250:251], v[254:255]\n"
"v_pk_fma_f32 v[166:167], v[166:167], v[250:251], v[254:255]\n"
"v_cvt_pk_bf16_f32 v68, v160, v161\n"
"v_cvt_pk_bf16_f32 v69, v162, v163\n"
"v_cvt_pk_bf16_f32 v70, v164, v165\n"
"v_cvt_pk_bf16_f32 v71, v166, v167\n"
"ds_write_b128 v23, v[68:71] offset:17408\n"
"s_add_u32 s76, s82, 128\n"
"s_cmp_eq_u32 s68, 0\n"
"s_cselect_b32 s77, s76, s82\n"
"buffer_load_dword v29, v11, s[52:55], s77 offen\n"
"buffer_load_dword v29, v12, s[60:63], s92 offen\n"
"s_add_u32 s92, s92, 384\n"
"s_waitcnt lgkmcnt(0)\n"
"s_barrier\n"
"ds_read_b128 v[88:91], v24 offset:0\n"
"ds_read_b128 v[92:95], v24 offset:1024\n"
"ds_read_b128 v[96:99], v24 offset:2048\n"
"ds_read_b128 v[100:103], v24 offset:3072\n"
"ds_read_b128 v[104:107], v24 offset:4096\n"
"v_pk_fma_f32 v[168:169], v[168:169], v[250:251], v[254:255]\n"
"v_pk_fma_f32 v[170:171], v[170:171], v[250:251], v[254:255]\n"
"v_pk_fma_f32 v[172:173], v[172:173], v[250:251], v[254:255]\n"
"v_pk_fma_f32 v[174:175], v[174:175], v[250:251], v[254:255]\n"
"v_cvt_pk_bf16_f32 v72, v168, v169\n"
"v_cvt_pk_bf16_f32 v73, v170, v171\n"
"v_cvt_pk_bf16_f32 v74, v172, v173\n"
"v_cvt_pk_bf16_f32 v75, v174, v175\n"
"v_pk_fma_f32 v[176:177], v[176:177], v[250:251], v[254:255]\n"
"v_pk_fma_f32 v[178:179], v[178:179], v[250:251], v[254:255]\n"
"v_pk_fma_f32 v[180:181], v[180:181], v[250:251], v[254:255]\n"
"v_pk_fma_f32 v[182:183], v[182:183], v[250:251], v[254:255]\n"
"v_cvt_pk_bf16_f32 v76, v176, v177\n"
"v_cvt_pk_bf16_f32 v77, v178, v179\n"
"v_cvt_pk_bf16_f32 v78, v180, v181\n"
"v_cvt_pk_bf16_f32 v79, v182, v183\n"
"ds_read_b128 v[108:111], v24 offset:5120\n"
"ds_read_b128 v[112:115], v24 offset:6144\n"
"ds_read_b128 v[116:119], v24 offset:7168\n"
"ds_read_b128 v[120:123], v24 offset:8192\n"
"ds_read_b128 v[124:127], v24 offset:9216\n"
"v_pk_fma_f32 v[184:185], v[184:185], v[250:251], v[254:255]\n"
"v_pk_fma_f32 v[186:187], v[186:187], v[250:251], v[254:255]\n"
"v_pk_fma_f32 v[188:189], v[188:189], v[250:251], v[254:255]\n"
"v_pk_fma_f32 v[190:191], v[190:191], v[250:251], v[254:255]\n"
"v_cvt_pk_bf16_f32 v80, v184, v185\n"
"v_cvt_pk_bf16_f32 v81, v186, v187\n"
"v_cvt_pk_bf16_f32 v82, v188, v189\n"
"v_cvt_pk_bf16_f32 v83, v190, v191\n"
"v_pk_fma_f32 v[192:193], v[192:193], v[250:251], v[254:255]\n"
"v_pk_fma_f32 v[194:195], v[194:195], v[250:251], v[254:255]\n"
"v_pk_fma_f32 v[196:197], v[196:197], v[250:251], v[254:255]\n"
"v_pk_fma_f32 v[198:199], v[198:199], v[250:251], v[254:255]\n"
"v_cvt_pk_bf16_f32 v84, v192, v193\n"
"v_cvt_pk_bf16_f32 v85, v194, v195\n"
"v_cvt_pk_bf16_f32 v86, v196, v197\n"
"v_cvt_pk_bf16_f32 v87, v198, v199\n"
"ds_read_b128 v[128:131], v24 offset:10240\n"
"ds_read_b128 v[132:135], v24 offset:11264\n"
"ds_read_b128 v[136:139], v24 offset:12288\n"
"ds_read_b128 v[140:143], v24 offset:13312\n"
"ds_read_b128 v[144:147], v24 offset:14336\n"
"v_pk_fma_f32 v[200:201], v[200:201], v[250:251], v[254:255]\n"
"v_pk_fma_f32 v[202:203], v[202:203], v[250:251], v[254:255]\n"
"v_pk_fma_f32 v[204:205], v[204:205], v[250:251], v[254:255]\n"
"v_pk_fma_f32 v[206:207], v[206:207], v[250:251], v[254:255]\n"
"v_cvt_pk_bf16_f32 v232, v200, v201\n"
"v_cvt_pk_bf16_f32 v233, v202, v203\n"
"v_cvt_pk_bf16_f32 v234, v204, v205\n"
"v_cvt_pk_bf16_f32 v235, v206, v207\n"
"v_pk_fma_f32 v[208:209], v[208:209], v[250:251], v[254:255]\n"
"v_pk_fma_f32 v[210:211], v[210:211], v[250:251], v[254:255]\n"
"v_pk_fma_f32 v[212:213], v[212:213], v[250:251], v[254:255]\n"
"v_pk_fma_f32 v[214:215], v[214:215], v[250:251], v[254:255]\n"
"v_cvt_pk_bf16_f32 v236, v208, v209\n"
"v_cvt_pk_bf16_f32 v237, v210, v211\n"
"v_cvt_pk_bf16_f32 v238, v212, v213\n"
"v_cvt_pk_bf16_f32 v239, v214, v215\n"
"ds_read_b128 v[148:151], v24 offset:15360\n"
"ds_read_b128 v[152:155], v24 offset:16384\n"
"ds_read_b128 v[156:159], v24 offset:17408\n"
"ds_read_b128 v[160:163], v24 offset:18432\n"
"ds_read_b128 v[164:167], v24 offset:19456\n"
"v_pk_fma_f32 v[216:217], v[216:217], v[250:251], v[254:255]\n"
"v_pk_fma_f32 v[218:219], v[218:219], v[250:251], v[254:255]\n"
"v_pk_fma_f32 v[220:221], v[220:221], v[250:251], v[254:255]\n"
"v_pk_fma_f32 v[222:223], v[222:223], v[250:251], v[254:255]\n"
"v_cvt_pk_bf16_f32 v240, v216, v217\n"
"v_cvt_pk_bf16_f32 v241, v218, v219\n"
"v_cvt_pk_bf16_f32 v242, v220, v221\n"
"v_cvt_pk_bf16_f32 v243, v222, v223\n"
"v_pk_fma_f32 v[224:225], v[224:225], v[250:251], v[254:255]\n"
"v_pk_fma_f32 v[226:227], v[226:227], v[250:251], v[254:255]\n"
"v_pk_fma_f32 v[228:229], v[228:229], v[250:251], v[254:255]\n"
"v_pk_fma_f32 v[230:231], v[230:231], v[250:251], v[254:255]\n"
"v_cvt_pk_bf16_f32 v244, v224, v225\n"
"v_cvt_pk_bf16_f32 v245, v226, v227\n"
"v_cvt_pk_bf16_f32 v246, v228, v229\n"
"v_cvt_pk_bf16_f32 v247, v230, v231\n"
"s_waitcnt lgkmcnt(0)\n"
"s_barrier\n"
"ds_write_b128 v23, v[72:75] offset:0\n"
"ds_write_b128 v23, v[76:79] offset:1024\n"
"ds_write_b128 v23, v[80:83] offset:4096\n"
"ds_write_b128 v23, v[84:87] offset:5120\n"
"ds_write_b128 v23, v[232:235] offset:8192\n"
"ds_write_b128 v23, v[236:239] offset:9216\n"
"ds_write_b128 v23, v[240:243] offset:12288\n"
"ds_write_b128 v23, v[244:247] offset:13312\n"
"s_add_u32 s75, s69, 128\n"
"s_cmp_eq_u32 s68, 0\n"
"s_cselect_b32 s40, s56, s52\n"
"s_cselect_b32 s41, s57, s53\n"
"s_mov_b32 s42, 5308416\n"
"s_mov_b32 s72, s82\n"
"s_mov_b32 s73, 1769472\n"
"s_cmp_eq_u32 s68, 3\n"
"s_cselect_b32 s40, s48, s40\n"
"s_cselect_b32 s41, s49, s41\n"
"s_cselect_b32 s42, s50, s42\n"
"s_cselect_b32 s72, s75, s72\n"
"s_cselect_b32 s73, 663552, s73\n"
"s_cselect_b64 vcc, 0, -1\n"
"v_cndmask_b32 v26, v7, v4, vcc\n"
"v_cndmask_b32 v27, v8, v5, vcc\n"
"v_cndmask_b32 v28, v9, v6, vcc\n"
"s_waitcnt lgkmcnt(0)\n"
"s_barrier\n"
"ds_read_b128 v[168:171], v24 offset:0\n"
"ds_read_b128 v[172:175], v24 offset:1024\n"
"ds_read_b128 v[176:179], v24 offset:2048\n"
"ds_read_b128 v[180:183], v24 offset:3072\n"
"ds_read_b128 v[184:187], v24 offset:4096\n"
"ds_read_b128 v[188:191], v24 offset:5120\n"
"ds_read_b128 v[192:195], v24 offset:6144\n"
"ds_read_b128 v[196:199], v24 offset:7168\n"
"ds_read_b128 v[200:203], v24 offset:8192\n"
"ds_read_b128 v[204:207], v24 offset:9216\n"
"ds_read_b128 v[208:211], v24 offset:10240\n"
"ds_read_b128 v[212:215], v24 offset:11264\n"
"ds_read_b128 v[216:219], v24 offset:12288\n"
"ds_read_b128 v[220:223], v24 offset:13312\n"
"ds_read_b128 v[224:227], v24 offset:14336\n"
"ds_read_b128 v[228:231], v24 offset:15360\n"
"s_waitcnt vmcnt(8) lgkmcnt(0)\n"
"s_barrier\n"
"s_add_u32 m0, s91, 73728\n"
"s_nop 0\n"
"buffer_load_dwordx4 v26, s[40:43], s72 offen lds\n"
"s_add_u32 m0, s91, 81920\n"
"s_nop 0\n"
"buffer_load_dwordx4 v27, s[40:43], s72 offen lds\n"
"s_add_u32 m0, s91, 90112\n"
"s_nop 0\n"
"buffer_load_dwordx4 v28, s[40:43], s72 offen lds\n"
"s_add_u32 s72, s72, s73\n"
"s_add_u32 m0, s91, 98304\n"
"s_nop 0\n"
"buffer_load_dwordx4 v26, s[40:43], s72 offen lds\n"
"s_add_u32 m0, s91, 106496\n"
"s_nop 0\n"
"buffer_load_dwordx4 v27, s[40:43], s72 offen lds\n"
"s_add_u32 m0, s91, 114688\n"
"s_nop 0\n"
"buffer_load_dwordx4 v28, s[40:43], s72 offen lds\n"
"s_add_u32 s72, s72, s73\n"
"s_mov_b32 s83, 0\n"
"ds_read_b64_tr_b16 v[232:233], v1 offset:0\n"
"ds_read_b64_tr_b16 v[234:235], v1 offset:512\n"
"ds_read_b64_tr_b16 v[236:237], v1 offset:2048\n"
"ds_read_b64_tr_b16 v[238:239], v1 offset:2560\n"
"ds_read_b64_tr_b16 v[240:241], v1 offset:4096\n"
"ds_read_b64_tr_b16 v[242:243], v1 offset:4608\n"
"ds_read_b64_tr_b16 v[244:245], v1 offset:6144\n"
"ds_read_b64_tr_b16 v[246:247], v1 offset:6656\n"
"ds_read_b64_tr_b16 v[248:249], v1 offset:8192\n"
"ds_read_b64_tr_b16 v[250:251], v1 offset:8704\n"
"ds_read_b64_tr_b16 v[252:253], v1 offset:10240\n"
"ds_read_b64_tr_b16 v[254:255], v1 offset:10752\n"
"s_cmp_eq_u32 s68, 0\n"
"s_cbranch_scc1 .Lkc1_GH0_11_%=\n"
"s_cmp_eq_u32 s68, 1\n"
"s_cbranch_scc1 .Lkc1_QL_4_%=\n"
"s_cmp_eq_u32 s80, 0\n"
"s_cbranch_scc0 .Lkc1_V0_5_%=\n"
".Lkc1_Q0_2_%=:\n"
"s_waitcnt lgkmcnt(8)\n"
"v_mfma_f32_32x32x16_bf16 v[48:63], v[232:235], v[88:91], 0\n"
"ds_read_b64_tr_b16 v[232:233], v1 offset:12288\n"
"ds_read_b64_tr_b16 v[234:235], v1 offset:12800\n"
"v_mfma_f32_32x32x16_bf16 v[48:63], v[236:239], v[92:95], v[48:63]\n"
"ds_read_b64_tr_b16 v[236:237], v1 offset:14336\n"
"ds_read_b64_tr_b16 v[238:239], v1 offset:14848\n"
"s_waitcnt lgkmcnt(8)\n"
"v_mfma_f32_32x32x16_bf16 v[48:63], v[240:243], v[96:99], v[48:63]\n"
"ds_read_b64_tr_b16 v[240:241], v1 offset:16384\n"
"ds_read_b64_tr_b16 v[242:243], v1 offset:16896\n"
"v_mfma_f32_32x32x16_bf16 v[48:63], v[244:247], v[100:103], v[48:63]\n"
"ds_read_b64_tr_b16 v[244:245], v1 offset:18432\n"
"ds_read_b64_tr_b16 v[246:247], v1 offset:18944\n"
"s_waitcnt lgkmcnt(8)\n"
"v_mfma_f32_32x32x16_bf16 v[48:63], v[248:251], v[104:107], v[48:63]\n"
"ds_read_b64_tr_b16 v[248:249], v1 offset:20480\n"
"ds_read_b64_tr_b16 v[250:251], v1 offset:20992\n"
"v_mfma_f32_32x32x16_bf16 v[48:63], v[252:255], v[108:111], v[48:63]\n"
"ds_read_b64_tr_b16 v[252:253], v1 offset:22528\n"
"ds_read_b64_tr_b16 v[254:255], v1 offset:23040\n"
"s_waitcnt lgkmcnt(8)\n"
"v_mfma_f32_32x32x16_bf16 v[48:63], v[232:235], v[112:115], v[48:63]\n"
"s_waitcnt vmcnt(11)\n"
"s_barrier\n"
"v_mfma_f32_32x32x16_bf16 v[48:63], v[236:239], v[116:119], v[48:63]\n"
"ds_read_b64_tr_b16 v[232:233], v1 offset:24576\n"
"ds_read_b64_tr_b16 v[234:235], v1 offset:25088\n"
"ds_read_b64_tr_b16 v[236:237], v1 offset:26624\n"
"ds_read_b64_tr_b16 v[238:239], v1 offset:27136\n"
"s_add_u32 s70, s69, 1327232\n"
"s_add_u32 m0, s91, 122880\n"
"s_waitcnt lgkmcnt(8)\n"
"v_mfma_f32_32x32x16_bf16 v[48:63], v[240:243], v[120:123], v[48:63]\n"
"ds_read_b64_tr_b16 v[240:241], v1 offset:28672\n"
"ds_read_b64_tr_b16 v[242:243], v1 offset:29184\n"
"buffer_load_dwordx4 v7, s[48:51], s70 offen lds\n"
"s_add_u32 m0, m0, 0x2000\n"
"v_mfma_f32_32x32x16_bf16 v[48:63], v[244:247], v[124:127], v[48:63]\n"
"ds_read_b64_tr_b16 v[244:245], v1 offset:30720\n"
"ds_read_b64_tr_b16 v[246:247], v1 offset:31232\n"
"buffer_load_dwordx4 v8, s[48:51], s70 offen lds\n"
"s_add_u32 m0, m0, 0x2000\n"
"s_waitcnt lgkmcnt(8)\n"
"v_mfma_f32_32x32x16_bf16 v[48:63], v[248:251], v[128:131], v[48:63]\n"
"ds_read_b64_tr_b16 v[248:249], v1 offset:32768\n"
"ds_read_b64_tr_b16 v[250:251], v1 offset:33280\n"
"buffer_load_dwordx4 v9, s[48:51], s70 offen lds\n"
"v_mfma_f32_32x32x16_bf16 v[48:63], v[252:255], v[132:135], v[48:63]\n"
"ds_read_b64_tr_b16 v[252:253], v1 offset:34816\n"
"ds_read_b64_tr_b16 v[254:255], v1 offset:35328\n"
"s_add_u32 s71, s82, 0\n"
"buffer_load_dword v29, v11, s[52:55], s71 offen\n"
"buffer_load_dword v29, v12, s[60:63], s92 offen\n"
"s_add_u32 s92, s92, 384\n"
"s_waitcnt lgkmcnt(8)\n"
"v_mfma_f32_32x32x16_bf16 v[48:63], v[232:235], v[136:139], v[48:63]\n"
"ds_read_b64_tr_b16 v[232:233], v1 offset:36864\n"
"ds_read_b64_tr_b16 v[234:235], v1 offset:37376\n"
"v_mfma_f32_32x32x16_bf16 v[48:63], v[236:239], v[140:143], v[48:63]\n"
"ds_read_b64_tr_b16 v[236:237], v1 offset:38912\n"
"ds_read_b64_tr_b16 v[238:239], v1 offset:39424\n"
"s_waitcnt lgkmcnt(8)\n"
"v_mfma_f32_32x32x16_bf16 v[48:63], v[240:243], v[144:147], v[48:63]\n"
"ds_read_b64_tr_b16 v[240:241], v1 offset:40960\n"
"ds_read_b64_tr_b16 v[242:243], v1 offset:41472\n"
"v_mfma_f32_32x32x16_bf16 v[48:63], v[244:247], v[148:151], v[48:63]\n"
"ds_read_b64_tr_b16 v[244:245], v1 offset:43008\n"
"ds_read_b64_tr_b16 v[246:247], v1 offset:43520\n"
"s_waitcnt lgkmcnt(8)\n"
"v_mfma_f32_32x32x16_bf16 v[48:63], v[248:251], v[152:155], v[48:63]\n"
"ds_read_b64_tr_b16 v[248:249], v1 offset:45056\n"
"ds_read_b64_tr_b16 v[250:251], v1 offset:45568\n"
"v_mfma_f32_32x32x16_bf16 v[48:63], v[252:255], v[156:159], v[48:63]\n"
"ds_read_b64_tr_b16 v[252:253], v1 offset:47104\n"
"ds_read_b64_tr_b16 v[254:255], v1 offset:47616\n"
"s_waitcnt lgkmcnt(8)\n"
"v_mfma_f32_32x32x16_bf16 v[48:63], v[232:235], v[160:163], v[48:63]\n"
"s_waitcnt vmcnt(11)\n"
"s_barrier\n"
"v_mfma_f32_32x32x16_bf16 v[48:63], v[236:239], v[164:167], v[48:63]\n"
"ds_read_b64_tr_b16 v[232:233], v2 offset:0\n"
"ds_read_b64_tr_b16 v[234:235], v2 offset:512\n"
"ds_read_b64_tr_b16 v[236:237], v2 offset:2048\n"
"ds_read_b64_tr_b16 v[238:239], v2 offset:2560\n"
"s_add_u32 s70, s69, 256\n"
"s_add_u32 m0, s91, 0\n"
"s_waitcnt lgkmcnt(8)\n"
"v_mfma_f32_32x32x16_bf16 v[48:63], v[240:243], v[168:171], v[48:63]\n"
"ds_read_b64_tr_b16 v[240:241], v2 offset:4096\n"
"ds_read_b64_tr_b16 v[242:243], v2 offset:4608\n"
"buffer_load_dwordx4 v7, s[48:51], s70 offen lds\n"
"s_add_u32 m0, m0, 0x2000\n"
"v_mfma_f32_32x32x16_bf16 v[48:63], v[244:247], v[172:175], v[48:63]\n"
"ds_read_b64_tr_b16 v[244:245], v2 offset:6144\n"
"ds_read_b64_tr_b16 v[246:247], v2 offset:6656\n"
"buffer_load_dwordx4 v8, s[48:51], s70 offen lds\n"
"s_add_u32 m0, m0, 0x2000\n"
"s_waitcnt lgkmcnt(8)\n"
"v_mfma_f32_32x32x16_bf16 v[48:63], v[248:251], v[176:179], v[48:63]\n"
"ds_read_b64_tr_b16 v[248:249], v2 offset:8192\n"
"ds_read_b64_tr_b16 v[250:251], v2 offset:8704\n"
"buffer_load_dwordx4 v9, s[48:51], s70 offen lds\n"
"v_mfma_f32_32x32x16_bf16 v[48:63], v[252:255], v[180:183], v[48:63]\n"
"ds_read_b64_tr_b16 v[252:253], v2 offset:10240\n"
"ds_read_b64_tr_b16 v[254:255], v2 offset:10752\n"
"s_waitcnt lgkmcnt(8)\n"
"v_mfma_f32_32x32x16_bf16 v[48:63], v[232:235], v[184:187], v[48:63]\n"
"ds_read_b64_tr_b16 v[232:233], v2 offset:12288\n"
"ds_read_b64_tr_b16 v[234:235], v2 offset:12800\n"
"v_mfma_f32_32x32x16_bf16 v[48:63], v[236:239], v[188:191], v[48:63]\n"
"ds_read_b64_tr_b16 v[236:237], v2 offset:14336\n"
"ds_read_b64_tr_b16 v[238:239], v2 offset:14848\n"
"s_waitcnt lgkmcnt(8)\n"
"v_mfma_f32_32x32x16_bf16 v[48:63], v[240:243], v[192:195], v[48:63]\n"
"ds_read_b64_tr_b16 v[240:241], v2 offset:16384\n"
"ds_read_b64_tr_b16 v[242:243], v2 offset:16896\n"
"v_mfma_f32_32x32x16_bf16 v[48:63], v[244:247], v[196:199], v[48:63]\n"
"ds_read_b64_tr_b16 v[244:245], v2 offset:18432\n"
"ds_read_b64_tr_b16 v[246:247], v2 offset:18944\n"
"s_waitcnt lgkmcnt(8)\n"
"v_mfma_f32_32x32x16_bf16 v[48:63], v[248:251], v[200:203], v[48:63]\n"
"ds_read_b64_tr_b16 v[248:249], v2 offset:20480\n"
"ds_read_b64_tr_b16 v[250:251], v2 offset:20992\n"
"v_mfma_f32_32x32x16_bf16 v[48:63], v[252:255], v[204:207], v[48:63]\n"
"ds_read_b64_tr_b16 v[252:253], v2 offset:22528\n"
"ds_read_b64_tr_b16 v[254:255], v2 offset:23040\n"
"s_waitcnt lgkmcnt(8)\n"
"v_mfma_f32_32x32x16_bf16 v[48:63], v[232:235], v[208:211], v[48:63]\n"
"s_waitcnt vmcnt(11)\n"
"s_barrier\n"
"v_mfma_f32_32x32x16_bf16 v[48:63], v[236:239], v[212:215], v[48:63]\n"
"ds_read_b64_tr_b16 v[232:233], v2 offset:24576\n"
"ds_read_b64_tr_b16 v[234:235], v2 offset:25088\n"
"ds_read_b64_tr_b16 v[236:237], v2 offset:26624\n"
"ds_read_b64_tr_b16 v[238:239], v2 offset:27136\n"
"s_add_u32 s70, s69, 663808\n"
"s_add_u32 m0, s91, 24576\n"
"s_waitcnt lgkmcnt(8)\n"
"v_mfma_f32_32x32x16_bf16 v[48:63], v[240:243], v[216:219], v[48:63]\n"
"ds_read_b64_tr_b16 v[240:241], v2 offset:28672\n"
"ds_read_b64_tr_b16 v[242:243], v2 offset:29184\n"
"buffer_load_dwordx4 v7, s[48:51], s70 offen lds\n"
"s_add_u32 m0, m0, 0x2000\n"
"v_mfma_f32_32x32x16_bf16 v[48:63], v[244:247], v[220:223], v[48:63]\n"
"ds_read_b64_tr_b16 v[244:245], v2 offset:30720\n"
"ds_read_b64_tr_b16 v[246:247], v2 offset:31232\n"
"buffer_load_dwordx4 v8, s[48:51], s70 offen lds\n"
"s_add_u32 m0, m0, 0x2000\n"
"s_waitcnt lgkmcnt(8)\n"
"v_mfma_f32_32x32x16_bf16 v[48:63], v[248:251], v[224:227], v[48:63]\n"
"ds_read_b64_tr_b16 v[248:249], v2 offset:32768\n"
"ds_read_b64_tr_b16 v[250:251], v2 offset:33280\n"
"buffer_load_dwordx4 v9, s[48:51], s70 offen lds\n"
"v_mfma_f32_32x32x16_bf16 v[48:63], v[252:255], v[228:231], v[48:63]\n"
"ds_read_b64_tr_b16 v[252:253], v2 offset:34816\n"
"ds_read_b64_tr_b16 v[254:255], v2 offset:35328\n"
".Lkc1_Q1_3_%=:\n"
"s_waitcnt lgkmcnt(8)\n"
"v_mfma_f32_32x32x16_bf16 v[32:47], v[232:235], v[88:91], 0\n"
"ds_read_b64_tr_b16 v[232:233], v2 offset:36864\n"
"ds_read_b64_tr_b16 v[234:235], v2 offset:37376\n"
"s_mov_b32 s84, s85\n"
"v_mfma_f32_32x32x16_bf16 v[32:47], v[236:239], v[92:95], v[32:47]\n"
"ds_read_b64_tr_b16 v[236:237], v2 offset:38912\n"
"ds_read_b64_tr_b16 v[238:239], v2 offset:39424\n"
"s_waitcnt lgkmcnt(8)\n"
"v_mfma_f32_32x32x16_bf16 v[32:47], v[240:243], v[96:99], v[32:47]\n"
"ds_read_b64_tr_b16 v[240:241], v2 offset:40960\n"
"ds_read_b64_tr_b16 v[242:243], v2 offset:41472\n"
"v_mfma_f32_32x32x16_bf16 v[32:47], v[244:247], v[100:103], v[32:47]\n"
"ds_read_b64_tr_b16 v[244:245], v2 offset:43008\n"
"ds_read_b64_tr_b16 v[246:247], v2 offset:43520\n"
"s_waitcnt lgkmcnt(8)\n"
"v_mfma_f32_32x32x16_bf16 v[32:47], v[248:251], v[104:107], v[32:47]\n"
"ds_read_b64_tr_b16 v[248:249], v2 offset:45056\n"
"ds_read_b64_tr_b16 v[250:251], v2 offset:45568\n"
"v_cvt_pk_bf16_f32 v80, v48, v49\n"
"v_mfma_f32_32x32x16_bf16 v[32:47], v[252:255], v[108:111], v[32:47]\n"
"ds_read_b64_tr_b16 v[252:253], v2 offset:47104\n"
"ds_read_b64_tr_b16 v[254:255], v2 offset:47616\n"
"s_waitcnt lgkmcnt(8)\n"
"v_mfma_f32_32x32x16_bf16 v[32:47], v[232:235], v[112:115], v[32:47]\n"
"s_waitcnt vmcnt(11)\n"
"s_barrier\n"
"v_mfma_f32_32x32x16_bf16 v[32:47], v[236:239], v[116:119], v[32:47]\n"
"ds_read_b64_tr_b16 v[232:233], v3 offset:0\n"
"ds_read_b64_tr_b16 v[234:235], v3 offset:512\n"
"ds_read_b64_tr_b16 v[236:237], v3 offset:2048\n"
"ds_read_b64_tr_b16 v[238:239], v3 offset:2560\n"
"s_add_u32 s70, s69, 1327360\n"
"s_add_u32 m0, s91, 49152\n"
"v_cvt_pk_bf16_f32 v81, v50, v51\n"
"s_waitcnt lgkmcnt(8)\n"
"v_mfma_f32_32x32x16_bf16 v[32:47], v[240:243], v[120:123], v[32:47]\n"
"ds_read_b64_tr_b16 v[240:241], v3 offset:4096\n"
"ds_read_b64_tr_b16 v[242:243], v3 offset:4608\n"
"buffer_load_dwordx4 v7, s[48:51], s70 offen lds\n"
"s_add_u32 m0, m0, 0x2000\n"
"v_mfma_f32_32x32x16_bf16 v[32:47], v[244:247], v[124:127], v[32:47]\n"
"ds_read_b64_tr_b16 v[244:245], v3 offset:6144\n"
"ds_read_b64_tr_b16 v[246:247], v3 offset:6656\n"
"buffer_load_dwordx4 v8, s[48:51], s70 offen lds\n"
"s_add_u32 m0, m0, 0x2000\n"
"s_waitcnt lgkmcnt(8)\n"
"v_mfma_f32_32x32x16_bf16 v[32:47], v[248:251], v[128:131], v[32:47]\n"
"ds_read_b64_tr_b16 v[248:249], v3 offset:8192\n"
"ds_read_b64_tr_b16 v[250:251], v3 offset:8704\n"
"buffer_load_dwordx4 v9, s[48:51], s70 offen lds\n"
"v_cvt_pk_bf16_f32 v82, v52, v53\n"
"v_mfma_f32_32x32x16_bf16 v[32:47], v[252:255], v[132:135], v[32:47]\n"
"ds_read_b64_tr_b16 v[252:253], v3 offset:10240\n"
"ds_read_b64_tr_b16 v[254:255], v3 offset:10752\n"
"s_add_u32 s71, s82, 0\n"
"buffer_load_dword v29, v11, s[56:59], s71 offen\n"
"buffer_load_dword v29, v12, s[60:63], s92 offen\n"
"s_add_u32 s92, s92, 384\n"
"s_waitcnt lgkmcnt(8)\n"
"v_mfma_f32_32x32x16_bf16 v[32:47], v[232:235], v[136:139], v[32:47]\n"
"ds_read_b64_tr_b16 v[232:233], v3 offset:12288\n"
"ds_read_b64_tr_b16 v[234:235], v3 offset:12800\n"
"v_mfma_f32_32x32x16_bf16 v[32:47], v[236:239], v[140:143], v[32:47]\n"
"ds_read_b64_tr_b16 v[236:237], v3 offset:14336\n"
"ds_read_b64_tr_b16 v[238:239], v3 offset:14848\n"
"v_cvt_pk_bf16_f32 v83, v54, v55\n"
"s_waitcnt lgkmcnt(8)\n"
"v_mfma_f32_32x32x16_bf16 v[32:47], v[240:243], v[144:147], v[32:47]\n"
"ds_read_b64_tr_b16 v[240:241], v3 offset:16384\n"
"ds_read_b64_tr_b16 v[242:243], v3 offset:16896\n"
"v_mfma_f32_32x32x16_bf16 v[32:47], v[244:247], v[148:151], v[32:47]\n"
"ds_read_b64_tr_b16 v[244:245], v3 offset:18432\n"
"ds_read_b64_tr_b16 v[246:247], v3 offset:18944\n"
"s_waitcnt lgkmcnt(8)\n"
"v_mfma_f32_32x32x16_bf16 v[32:47], v[248:251], v[152:155], v[32:47]\n"
"ds_read_b64_tr_b16 v[248:249], v3 offset:20480\n"
"ds_read_b64_tr_b16 v[250:251], v3 offset:20992\n"
"v_cvt_pk_bf16_f32 v84, v56, v57\n"
"v_mfma_f32_32x32x16_bf16 v[32:47], v[252:255], v[156:159], v[32:47]\n"
"ds_read_b64_tr_b16 v[252:253], v3 offset:22528\n"
"ds_read_b64_tr_b16 v[254:255], v3 offset:23040\n"
"s_waitcnt lgkmcnt(8)\n"
"v_mfma_f32_32x32x16_bf16 v[32:47], v[232:235], v[160:163], v[32:47]\n"
"s_waitcnt vmcnt(11)\n"
"s_barrier\n"
"v_mfma_f32_32x32x16_bf16 v[32:47], v[236:239], v[164:167], v[32:47]\n"
"ds_read_b64_tr_b16 v[232:233], v3 offset:24576\n"
"ds_read_b64_tr_b16 v[234:235], v3 offset:25088\n"
"ds_read_b64_tr_b16 v[236:237], v3 offset:26624\n"
"ds_read_b64_tr_b16 v[238:239], v3 offset:27136\n"
"s_add_u32 s70, s82, 0\n"
"s_add_u32 m0, s91, 73728\n"
"v_cvt_pk_bf16_f32 v85, v58, v59\n"
"s_waitcnt lgkmcnt(8)\n"
"v_mfma_f32_32x32x16_bf16 v[32:47], v[240:243], v[168:171], v[32:47]\n"
"ds_read_b64_tr_b16 v[240:241], v3 offset:28672\n"
"ds_read_b64_tr_b16 v[242:243], v3 offset:29184\n"
"buffer_load_dwordx4 v4, s[52:55], s70 offen lds\n"
"s_add_u32 m0, m0, 0x2000\n"
"v_mfma_f32_32x32x16_bf16 v[32:47], v[244:247], v[172:175], v[32:47]\n"
"ds_read_b64_tr_b16 v[244:245], v3 offset:30720\n"
"ds_read_b64_tr_b16 v[246:247], v3 offset:31232\n"
"buffer_load_dwordx4 v5, s[52:55], s70 offen lds\n"
"s_add_u32 m0, m0, 0x2000\n"
"s_waitcnt lgkmcnt(8)\n"
"v_mfma_f32_32x32x16_bf16 v[32:47], v[248:251], v[176:179], v[32:47]\n"
"ds_read_b64_tr_b16 v[248:249], v3 offset:32768\n"
"ds_read_b64_tr_b16 v[250:251], v3 offset:33280\n"
"buffer_load_dwordx4 v6, s[52:55], s70 offen lds\n"
"v_cvt_pk_bf16_f32 v86, v60, v61\n"
"v_mfma_f32_32x32x16_bf16 v[32:47], v[252:255], v[180:183], v[32:47]\n"
"ds_read_b64_tr_b16 v[252:253], v3 offset:34816\n"
"ds_read_b64_tr_b16 v[254:255], v3 offset:35328\n"
"s_waitcnt lgkmcnt(8)\n"
"v_mfma_f32_32x32x16_bf16 v[32:47], v[232:235], v[184:187], v[32:47]\n"
"ds_read_b64_tr_b16 v[232:233], v3 offset:36864\n"
"ds_read_b64_tr_b16 v[234:235], v3 offset:37376\n"
"v_mfma_f32_32x32x16_bf16 v[32:47], v[236:239], v[188:191], v[32:47]\n"
"ds_read_b64_tr_b16 v[236:237], v3 offset:38912\n"
"ds_read_b64_tr_b16 v[238:239], v3 offset:39424\n"
"v_cvt_pk_bf16_f32 v87, v62, v63\n"
"s_waitcnt lgkmcnt(8)\n"
"v_mfma_f32_32x32x16_bf16 v[32:47], v[240:243], v[192:195], v[32:47]\n"
"ds_read_b64_tr_b16 v[240:241], v3 offset:40960\n"
"ds_read_b64_tr_b16 v[242:243], v3 offset:41472\n"
"v_mfma_f32_32x32x16_bf16 v[32:47], v[244:247], v[196:199], v[32:47]\n"
"ds_read_b64_tr_b16 v[244:245], v3 offset:43008\n"
"ds_read_b64_tr_b16 v[246:247], v3 offset:43520\n"
"s_waitcnt lgkmcnt(8)\n"
"v_mfma_f32_32x32x16_bf16 v[32:47], v[248:251], v[200:203], v[32:47]\n"
"ds_read_b64_tr_b16 v[248:249], v3 offset:45056\n"
"ds_read_b64_tr_b16 v[250:251], v3 offset:45568\n"
"buffer_store_dwordx4 v[80:83], v10, s[44:47], s84 offen sc1\n"
"v_mfma_f32_32x32x16_bf16 v[32:47], v[252:255], v[204:207], v[32:47]\n"
"ds_read_b64_tr_b16 v[252:253], v3 offset:47104\n"
"ds_read_b64_tr_b16 v[254:255], v3 offset:47616\n"
"s_waitcnt lgkmcnt(8)\n"
"v_mfma_f32_32x32x16_bf16 v[32:47], v[232:235], v[208:211], v[32:47]\n"
"s_waitcnt vmcnt(11)\n"
"s_barrier\n"
"v_mfma_f32_32x32x16_bf16 v[32:47], v[236:239], v[212:215], v[32:47]\n"
"ds_read_b64_tr_b16 v[232:233], v1 offset:0\n"
"ds_read_b64_tr_b16 v[234:235], v1 offset:512\n"
"ds_read_b64_tr_b16 v[236:237], v1 offset:2048\n"
"ds_read_b64_tr_b16 v[238:239], v1 offset:2560\n"
"s_add_u32 s70, s82, 1769472\n"
"s_add_u32 m0, s91, 98304\n"
"buffer_store_dwordx4 v[84:87], v10, s[44:47], s84 offen offset:1024 sc1\n"
"s_waitcnt lgkmcnt(8)\n"
"v_mfma_f32_32x32x16_bf16 v[32:47], v[240:243], v[216:219], v[32:47]\n"
"ds_read_b64_tr_b16 v[240:241], v1 offset:4096\n"
"ds_read_b64_tr_b16 v[242:243], v1 offset:4608\n"
"buffer_load_dwordx4 v4, s[52:55], s70 offen lds\n"
"s_add_u32 m0, m0, 0x2000\n"
"v_mfma_f32_32x32x16_bf16 v[32:47], v[244:247], v[220:223], v[32:47]\n"
"ds_read_b64_tr_b16 v[244:245], v1 offset:6144\n"
"ds_read_b64_tr_b16 v[246:247], v1 offset:6656\n"
"buffer_load_dwordx4 v5, s[52:55], s70 offen lds\n"
"s_add_u32 m0, m0, 0x2000\n"
"s_waitcnt lgkmcnt(8)\n"
"v_mfma_f32_32x32x16_bf16 v[32:47], v[248:251], v[224:227], v[32:47]\n"
"ds_read_b64_tr_b16 v[248:249], v1 offset:8192\n"
"ds_read_b64_tr_b16 v[250:251], v1 offset:8704\n"
"buffer_load_dwordx4 v6, s[52:55], s70 offen lds\n"
"v_mfma_f32_32x32x16_bf16 v[32:47], v[252:255], v[228:231], v[32:47]\n"
"ds_read_b64_tr_b16 v[252:253], v1 offset:10240\n"
"ds_read_b64_tr_b16 v[254:255], v1 offset:10752\n"
".Lkc1_QL_4_%=:\n"
"s_waitcnt lgkmcnt(8)\n"
"v_mfma_f32_32x32x16_bf16 v[48:63], v[232:235], v[88:91], 0\n"
"ds_read_b64_tr_b16 v[232:233], v1 offset:12288\n"
"ds_read_b64_tr_b16 v[234:235], v1 offset:12800\n"
"s_add_u32 s72, s85, 4096\n"
"s_cmp_eq_u32 s68, 3\n"
"s_cselect_b32 s84, s72, 1073741824\n"
"v_mfma_f32_32x32x16_bf16 v[48:63], v[236:239], v[92:95], v[48:63]\n"
"ds_read_b64_tr_b16 v[236:237], v1 offset:14336\n"
"ds_read_b64_tr_b16 v[238:239], v1 offset:14848\n"
"s_waitcnt lgkmcnt(8)\n"
"v_mfma_f32_32x32x16_bf16 v[48:63], v[240:243], v[96:99], v[48:63]\n"
"ds_read_b64_tr_b16 v[240:241], v1 offset:16384\n"
"ds_read_b64_tr_b16 v[242:243], v1 offset:16896\n"
"v_mfma_f32_32x32x16_bf16 v[48:63], v[244:247], v[100:103], v[48:63]\n"
"ds_read_b64_tr_b16 v[244:245], v1 offset:18432\n"
"ds_read_b64_tr_b16 v[246:247], v1 offset:18944\n"
"s_waitcnt lgkmcnt(8)\n"
"v_mfma_f32_32x32x16_bf16 v[48:63], v[248:251], v[104:107], v[48:63]\n"
"ds_read_b64_tr_b16 v[248:249], v1 offset:20480\n"
"ds_read_b64_tr_b16 v[250:251], v1 offset:20992\n"
"v_cvt_pk_bf16_f32 v80, v32, v33\n"
"v_mfma_f32_32x32x16_bf16 v[48:63], v[252:255], v[108:111], v[48:63]\n"
"ds_read_b64_tr_b16 v[252:253], v1 offset:22528\n"
"ds_read_b64_tr_b16 v[254:255], v1 offset:23040\n"
"s_waitcnt lgkmcnt(8)\n"
"v_mfma_f32_32x32x16_bf16 v[48:63], v[232:235], v[112:115], v[48:63]\n"
"s_waitcnt vmcnt(11)\n"
"s_barrier\n"
"v_mfma_f32_32x32x16_bf16 v[48:63], v[236:239], v[116:119], v[48:63]\n"
"ds_read_b64_tr_b16 v[232:233], v1 offset:24576\n"
"ds_read_b64_tr_b16 v[234:235], v1 offset:25088\n"
"ds_read_b64_tr_b16 v[236:237], v1 offset:26624\n"
"ds_read_b64_tr_b16 v[238:239], v1 offset:27136\n"
"s_add_u32 s70, s82, 3538944\n"
"s_add_u32 m0, s91, 122880\n"
"v_cvt_pk_bf16_f32 v81, v34, v35\n"
"s_waitcnt lgkmcnt(8)\n"
"v_mfma_f32_32x32x16_bf16 v[48:63], v[240:243], v[120:123], v[48:63]\n"
"ds_read_b64_tr_b16 v[240:241], v1 offset:28672\n"
"ds_read_b64_tr_b16 v[242:243], v1 offset:29184\n"
"buffer_load_dwordx4 v4, s[52:55], s70 offen lds\n"
"s_add_u32 m0, m0, 0x2000\n"
"v_mfma_f32_32x32x16_bf16 v[48:63], v[244:247], v[124:127], v[48:63]\n"
"ds_read_b64_tr_b16 v[244:245], v1 offset:30720\n"
"ds_read_b64_tr_b16 v[246:247], v1 offset:31232\n"
"buffer_load_dwordx4 v5, s[52:55], s70 offen lds\n"
"s_add_u32 m0, m0, 0x2000\n"
"s_waitcnt lgkmcnt(8)\n"
"v_mfma_f32_32x32x16_bf16 v[48:63], v[248:251], v[128:131], v[48:63]\n"
"ds_read_b64_tr_b16 v[248:249], v1 offset:32768\n"
"ds_read_b64_tr_b16 v[250:251], v1 offset:33280\n"
"buffer_load_dwordx4 v6, s[52:55], s70 offen lds\n"
"v_cvt_pk_bf16_f32 v82, v36, v37\n"
"v_mfma_f32_32x32x16_bf16 v[48:63], v[252:255], v[132:135], v[48:63]\n"
"ds_read_b64_tr_b16 v[252:253], v1 offset:34816\n"
"ds_read_b64_tr_b16 v[254:255], v1 offset:35328\n"
"s_add_u32 s71, s82, 128\n"
"buffer_load_dword v29, v11, s[52:55], s71 offen\n"
"buffer_load_dword v29, v12, s[60:63], s92 offen\n"
"s_add_u32 s92, s92, 384\n"
"s_waitcnt lgkmcnt(8)\n"
"v_mfma_f32_32x32x16_bf16 v[48:63], v[232:235], v[136:139], v[48:63]\n"
"ds_read_b64_tr_b16 v[232:233], v1 offset:36864\n"
"ds_read_b64_tr_b16 v[234:235], v1 offset:37376\n"
"v_mfma_f32_32x32x16_bf16 v[48:63], v[236:239], v[140:143], v[48:63]\n"
"ds_read_b64_tr_b16 v[236:237], v1 offset:38912\n"
"ds_read_b64_tr_b16 v[238:239], v1 offset:39424\n"
"v_cvt_pk_bf16_f32 v83, v38, v39\n"
"s_waitcnt lgkmcnt(8)\n"
"v_mfma_f32_32x32x16_bf16 v[48:63], v[240:243], v[144:147], v[48:63]\n"
"ds_read_b64_tr_b16 v[240:241], v1 offset:40960\n"
"ds_read_b64_tr_b16 v[242:243], v1 offset:41472\n"
"v_mfma_f32_32x32x16_bf16 v[48:63], v[244:247], v[148:151], v[48:63]\n"
"ds_read_b64_tr_b16 v[244:245], v1 offset:43008\n"
"ds_read_b64_tr_b16 v[246:247], v1 offset:43520\n"
"s_waitcnt lgkmcnt(8)\n"
"v_mfma_f32_32x32x16_bf16 v[48:63], v[248:251], v[152:155], v[48:63]\n"
"ds_read_b64_tr_b16 v[248:249], v1 offset:45056\n"
"ds_read_b64_tr_b16 v[250:251], v1 offset:45568\n"
"v_cvt_pk_bf16_f32 v84, v40, v41\n"
"v_mfma_f32_32x32x16_bf16 v[48:63], v[252:255], v[156:159], v[48:63]\n"
"ds_read_b64_tr_b16 v[252:253], v1 offset:47104\n"
"ds_read_b64_tr_b16 v[254:255], v1 offset:47616\n"
"s_waitcnt lgkmcnt(8)\n"
"v_mfma_f32_32x32x16_bf16 v[48:63], v[232:235], v[160:163], v[48:63]\n"
"s_waitcnt vmcnt(11)\n"
"s_barrier\n"
"v_mfma_f32_32x32x16_bf16 v[48:63], v[236:239], v[164:167], v[48:63]\n"
"ds_read_b64_tr_b16 v[232:233], v2 offset:0\n"
"ds_read_b64_tr_b16 v[234:235], v2 offset:512\n"
"ds_read_b64_tr_b16 v[236:237], v2 offset:2048\n"
"ds_read_b64_tr_b16 v[238:239], v2 offset:2560\n"
"s_add_u32 s70, s82, 0\n"
"s_add_u32 m0, s91, 0\n"
"v_cvt_pk_bf16_f32 v85, v42, v43\n"
"s_waitcnt lgkmcnt(8)\n"
"v_mfma_f32_32x32x16_bf16 v[48:63], v[240:243], v[168:171], v[48:63]\n"
"ds_read_b64_tr_b16 v[240:241], v2 offset:4096\n"
"ds_read_b64_tr_b16 v[242:243], v2 offset:4608\n"
"buffer_load_dwordx4 v4, s[56:59], s70 offen lds\n"
"s_add_u32 m0, m0, 0x2000\n"
"v_mfma_f32_32x32x16_bf16 v[48:63], v[244:247], v[172:175], v[48:63]\n"
"ds_read_b64_tr_b16 v[244:245], v2 offset:6144\n"
"ds_read_b64_tr_b16 v[246:247], v2 offset:6656\n"
"buffer_load_dwordx4 v5, s[56:59], s70 offen lds\n"
"s_add_u32 m0, m0, 0x2000\n"
"s_waitcnt lgkmcnt(8)\n"
"v_mfma_f32_32x32x16_bf16 v[48:63], v[248:251], v[176:179], v[48:63]\n"
"ds_read_b64_tr_b16 v[248:249], v2 offset:8192\n"
"ds_read_b64_tr_b16 v[250:251], v2 offset:8704\n"
"buffer_load_dwordx4 v6, s[56:59], s70 offen lds\n"
"v_cvt_pk_bf16_f32 v86, v44, v45\n"
"v_mfma_f32_32x32x16_bf16 v[48:63], v[252:255], v[180:183], v[48:63]\n"
"ds_read_b64_tr_b16 v[252:253], v2 offset:10240\n"
"ds_read_b64_tr_b16 v[254:255], v2 offset:10752\n"
"s_waitcnt lgkmcnt(8)\n"
"v_mfma_f32_32x32x16_bf16 v[48:63], v[232:235], v[184:187], v[48:63]\n"
"ds_read_b64_tr_b16 v[232:233], v2 offset:12288\n"
"ds_read_b64_tr_b16 v[234:235], v2 offset:12800\n"
"v_mfma_f32_32x32x16_bf16 v[48:63], v[236:239], v[188:191], v[48:63]\n"
"ds_read_b64_tr_b16 v[236:237], v2 offset:14336\n"
"ds_read_b64_tr_b16 v[238:239], v2 offset:14848\n"
"v_cvt_pk_bf16_f32 v87, v46, v47\n"
"s_waitcnt lgkmcnt(8)\n"
"v_mfma_f32_32x32x16_bf16 v[48:63], v[240:243], v[192:195], v[48:63]\n"
"ds_read_b64_tr_b16 v[240:241], v2 offset:16384\n"
"ds_read_b64_tr_b16 v[242:243], v2 offset:16896\n"
"v_mfma_f32_32x32x16_bf16 v[48:63], v[244:247], v[196:199], v[48:63]\n"
"ds_read_b64_tr_b16 v[244:245], v2 offset:18432\n"
"ds_read_b64_tr_b16 v[246:247], v2 offset:18944\n"
"s_waitcnt lgkmcnt(8)\n"
"v_mfma_f32_32x32x16_bf16 v[48:63], v[248:251], v[200:203], v[48:63]\n"
"ds_read_b64_tr_b16 v[248:249], v2 offset:20480\n"
"ds_read_b64_tr_b16 v[250:251], v2 offset:20992\n"
"buffer_store_dwordx4 v[80:83], v10, s[44:47], s84 offen sc1\n"
"v_mfma_f32_32x32x16_bf16 v[48:63], v[252:255], v[204:207], v[48:63]\n"
"ds_read_b64_tr_b16 v[252:253], v2 offset:22528\n"
"ds_read_b64_tr_b16 v[254:255], v2 offset:23040\n"
"s_waitcnt lgkmcnt(8)\n"
"v_mfma_f32_32x32x16_bf16 v[48:63], v[232:235], v[208:211], v[48:63]\n"
"s_waitcnt vmcnt(11)\n"
"s_barrier\n"
"v_mfma_f32_32x32x16_bf16 v[48:63], v[236:239], v[212:215], v[48:63]\n"
"ds_read_b64_tr_b16 v[232:233], v2 offset:24576\n"
"ds_read_b64_tr_b16 v[234:235], v2 offset:25088\n"
"ds_read_b64_tr_b16 v[236:237], v2 offset:26624\n"
"ds_read_b64_tr_b16 v[238:239], v2 offset:27136\n"
"s_add_u32 s70, s82, 1769472\n"
"s_add_u32 m0, s91, 24576\n"
"buffer_store_dwordx4 v[84:87], v10, s[44:47], s84 offen offset:1024 sc1\n"
"s_waitcnt lgkmcnt(8)\n"
"v_mfma_f32_32x32x16_bf16 v[48:63], v[240:243], v[216:219], v[48:63]\n"
"ds_read_b64_tr_b16 v[240:241], v2 offset:28672\n"
"ds_read_b64_tr_b16 v[242:243], v2 offset:29184\n"
"buffer_load_dwordx4 v4, s[56:59], s70 offen lds\n"
"s_add_u32 m0, m0, 0x2000\n"
"v_mfma_f32_32x32x16_bf16 v[48:63], v[244:247], v[220:223], v[48:63]\n"
"ds_read_b64_tr_b16 v[244:245], v2 offset:30720\n"
"ds_read_b64_tr_b16 v[246:247], v2 offset:31232\n"
"buffer_load_dwordx4 v5, s[56:59], s70 offen lds\n"
"s_add_u32 m0, m0, 0x2000\n"
"s_waitcnt lgkmcnt(8)\n"
"v_mfma_f32_32x32x16_bf16 v[48:63], v[248:251], v[224:227], v[48:63]\n"
"ds_read_b64_tr_b16 v[248:249], v2 offset:32768\n"
"ds_read_b64_tr_b16 v[250:251], v2 offset:33280\n"
"buffer_load_dwordx4 v6, s[56:59], s70 offen lds\n"
"v_mfma_f32_32x32x16_bf16 v[48:63], v[252:255], v[228:231], v[48:63]\n"
"ds_read_b64_tr_b16 v[252:253], v2 offset:34816\n"
"ds_read_b64_tr_b16 v[254:255], v2 offset:35328\n"
"s_branch .Lkc1_GQ_8_%=\n"
".Lkc1_V0_5_%=:\n"
"s_waitcnt lgkmcnt(8)\n"
"v_mfma_f32_32x32x16_bf16 v[48:63], v[88:91], v[232:235], 0\n"
"ds_read_b64_tr_b16 v[232:233], v1 offset:12288\n"
"ds_read_b64_tr_b16 v[234:235], v1 offset:12800\n"
"v_mfma_f32_32x32x16_bf16 v[48:63], v[92:95], v[236:239], v[48:63]\n"
"ds_read_b64_tr_b16 v[236:237], v1 offset:14336\n"
"ds_read_b64_tr_b16 v[238:239], v1 offset:14848\n"
"s_waitcnt lgkmcnt(8)\n"
"v_mfma_f32_32x32x16_bf16 v[48:63], v[96:99], v[240:243], v[48:63]\n"
"ds_read_b64_tr_b16 v[240:241], v1 offset:16384\n"
"ds_read_b64_tr_b16 v[242:243], v1 offset:16896\n"
"v_mfma_f32_32x32x16_bf16 v[48:63], v[100:103], v[244:247], v[48:63]\n"
"ds_read_b64_tr_b16 v[244:245], v1 offset:18432\n"
"ds_read_b64_tr_b16 v[246:247], v1 offset:18944\n"
"s_waitcnt lgkmcnt(8)\n"
"v_mfma_f32_32x32x16_bf16 v[48:63], v[104:107], v[248:251], v[48:63]\n"
"ds_read_b64_tr_b16 v[248:249], v1 offset:20480\n"
"ds_read_b64_tr_b16 v[250:251], v1 offset:20992\n"
"v_mfma_f32_32x32x16_bf16 v[48:63], v[108:111], v[252:255], v[48:63]\n"
"ds_read_b64_tr_b16 v[252:253], v1 offset:22528\n"
"ds_read_b64_tr_b16 v[254:255], v1 offset:23040\n"
"s_waitcnt lgkmcnt(8)\n"
"v_mfma_f32_32x32x16_bf16 v[48:63], v[112:115], v[232:235], v[48:63]\n"
"s_waitcnt vmcnt(11)\n"
"s_barrier\n"
"v_mfma_f32_32x32x16_bf16 v[48:63], v[116:119], v[236:239], v[48:63]\n"
"ds_read_b64_tr_b16 v[232:233], v1 offset:24576\n"
"ds_read_b64_tr_b16 v[234:235], v1 offset:25088\n"
"ds_read_b64_tr_b16 v[236:237], v1 offset:26624\n"
"ds_read_b64_tr_b16 v[238:239], v1 offset:27136\n"
"s_add_u32 s70, s69, 1327232\n"
"s_add_u32 m0, s91, 122880\n"
"s_waitcnt lgkmcnt(8)\n"
"v_mfma_f32_32x32x16_bf16 v[48:63], v[120:123], v[240:243], v[48:63]\n"
"ds_read_b64_tr_b16 v[240:241], v1 offset:28672\n"
"ds_read_b64_tr_b16 v[242:243], v1 offset:29184\n"
"buffer_load_dwordx4 v7, s[48:51], s70 offen lds\n"
"s_add_u32 m0, m0, 0x2000\n"
"v_mfma_f32_32x32x16_bf16 v[48:63], v[124:127], v[244:247], v[48:63]\n"
"ds_read_b64_tr_b16 v[244:245], v1 offset:30720\n"
"ds_read_b64_tr_b16 v[246:247], v1 offset:31232\n"
"buffer_load_dwordx4 v8, s[48:51], s70 offen lds\n"
"s_add_u32 m0, m0, 0x2000\n"
"s_waitcnt lgkmcnt(8)\n"
"v_mfma_f32_32x32x16_bf16 v[48:63], v[128:131], v[248:251], v[48:63]\n"
"ds_read_b64_tr_b16 v[248:249], v1 offset:32768\n"
"ds_read_b64_tr_b16 v[250:251], v1 offset:33280\n"
"buffer_load_dwordx4 v9, s[48:51], s70 offen lds\n"
"v_mfma_f32_32x32x16_bf16 v[48:63], v[132:135], v[252:255], v[48:63]\n"
"ds_read_b64_tr_b16 v[252:253], v1 offset:34816\n"
"ds_read_b64_tr_b16 v[254:255], v1 offset:35328\n"
"s_add_u32 s71, s82, 0\n"
"buffer_load_dword v29, v11, s[52:55], s71 offen\n"
"buffer_load_dword v29, v12, s[60:63], s92 offen\n"
"s_add_u32 s92, s92, 384\n"
"s_waitcnt lgkmcnt(8)\n"
"v_mfma_f32_32x32x16_bf16 v[48:63], v[136:139], v[232:235], v[48:63]\n"
"ds_read_b64_tr_b16 v[232:233], v1 offset:36864\n"
"ds_read_b64_tr_b16 v[234:235], v1 offset:37376\n"
"v_mfma_f32_32x32x16_bf16 v[48:63], v[140:143], v[236:239], v[48:63]\n"
"ds_read_b64_tr_b16 v[236:237], v1 offset:38912\n"
"ds_read_b64_tr_b16 v[238:239], v1 offset:39424\n"
"s_waitcnt lgkmcnt(8)\n"
"v_mfma_f32_32x32x16_bf16 v[48:63], v[144:147], v[240:243], v[48:63]\n"
"ds_read_b64_tr_b16 v[240:241], v1 offset:40960\n"
"ds_read_b64_tr_b16 v[242:243], v1 offset:41472\n"
"v_mfma_f32_32x32x16_bf16 v[48:63], v[148:151], v[244:247], v[48:63]\n"
"ds_read_b64_tr_b16 v[244:245], v1 offset:43008\n"
"ds_read_b64_tr_b16 v[246:247], v1 offset:43520\n"
"s_waitcnt lgkmcnt(8)\n"
"v_mfma_f32_32x32x16_bf16 v[48:63], v[152:155], v[248:251], v[48:63]\n"
"ds_read_b64_tr_b16 v[248:249], v1 offset:45056\n"
"ds_read_b64_tr_b16 v[250:251], v1 offset:45568\n"
"v_mfma_f32_32x32x16_bf16 v[48:63], v[156:159], v[252:255], v[48:63]\n"
"ds_read_b64_tr_b16 v[252:253], v1 offset:47104\n"
"ds_read_b64_tr_b16 v[254:255], v1 offset:47616\n"
"s_waitcnt lgkmcnt(8)\n"
"v_mfma_f32_32x32x16_bf16 v[48:63], v[160:163], v[232:235], v[48:63]\n"
"s_waitcnt vmcnt(11)\n"
"s_barrier\n"
"v_mfma_f32_32x32x16_bf16 v[48:63], v[164:167], v[236:239], v[48:63]\n"
"ds_read_b64_tr_b16 v[232:233], v2 offset:0\n"
"ds_read_b64_tr_b16 v[234:235], v2 offset:512\n"
"ds_read_b64_tr_b16 v[236:237], v2 offset:2048\n"
"ds_read_b64_tr_b16 v[238:239], v2 offset:2560\n"
"s_add_u32 s70, s69, 256\n"
"s_add_u32 m0, s91, 0\n"
"s_waitcnt lgkmcnt(8)\n"
"v_mfma_f32_32x32x16_bf16 v[48:63], v[168:171], v[240:243], v[48:63]\n"
"ds_read_b64_tr_b16 v[240:241], v2 offset:4096\n"
"ds_read_b64_tr_b16 v[242:243], v2 offset:4608\n"
"buffer_load_dwordx4 v7, s[48:51], s70 offen lds\n"
"s_add_u32 m0, m0, 0x2000\n"
"v_mfma_f32_32x32x16_bf16 v[48:63], v[172:175], v[244:247], v[48:63]\n"
"ds_read_b64_tr_b16 v[244:245], v2 offset:6144\n"
"ds_read_b64_tr_b16 v[246:247], v2 offset:6656\n"
"buffer_load_dwordx4 v8, s[48:51], s70 offen lds\n"
"s_add_u32 m0, m0, 0x2000\n"
"s_waitcnt lgkmcnt(8)\n"
"v_mfma_f32_32x32x16_bf16 v[48:63], v[176:179], v[248:251], v[48:63]\n"
"ds_read_b64_tr_b16 v[248:249], v2 offset:8192\n"
"ds_read_b64_tr_b16 v[250:251], v2 offset:8704\n"
"buffer_load_dwordx4 v9, s[48:51], s70 offen lds\n"
"v_mfma_f32_32x32x16_bf16 v[48:63], v[180:183], v[252:255], v[48:63]\n"
"ds_read_b64_tr_b16 v[252:253], v2 offset:10240\n"
"ds_read_b64_tr_b16 v[254:255], v2 offset:10752\n"
"s_waitcnt lgkmcnt(8)\n"
"v_mfma_f32_32x32x16_bf16 v[48:63], v[184:187], v[232:235], v[48:63]\n"
"ds_read_b64_tr_b16 v[232:233], v2 offset:12288\n"
"ds_read_b64_tr_b16 v[234:235], v2 offset:12800\n"
"v_mfma_f32_32x32x16_bf16 v[48:63], v[188:191], v[236:239], v[48:63]\n"
"ds_read_b64_tr_b16 v[236:237], v2 offset:14336\n"
"ds_read_b64_tr_b16 v[238:239], v2 offset:14848\n"
"s_waitcnt lgkmcnt(8)\n"
"v_mfma_f32_32x32x16_bf16 v[48:63], v[192:195], v[240:243], v[48:63]\n"
"ds_read_b64_tr_b16 v[240:241], v2 offset:16384\n"
"ds_read_b64_tr_b16 v[242:243], v2 offset:16896\n"
"v_mfma_f32_32x32x16_bf16 v[48:63], v[196:199], v[244:247], v[48:63]\n"
"ds_read_b64_tr_b16 v[244:245], v2 offset:18432\n"
"ds_read_b64_tr_b16 v[246:247], v2 offset:18944\n"
"s_waitcnt lgkmcnt(8)\n"
"v_mfma_f32_32x32x16_bf16 v[48:63], v[200:203], v[248:251], v[48:63]\n"
"ds_read_b64_tr_b16 v[248:249], v2 offset:20480\n"
"ds_read_b64_tr_b16 v[250:251], v2 offset:20992\n"
"v_mfma_f32_32x32x16_bf16 v[48:63], v[204:207], v[252:255], v[48:63]\n"
"ds_read_b64_tr_b16 v[252:253], v2 offset:22528\n"
"ds_read_b64_tr_b16 v[254:255], v2 offset:23040\n"
"s_waitcnt lgkmcnt(8)\n"
"v_mfma_f32_32x32x16_bf16 v[48:63], v[208:211], v[232:235], v[48:63]\n"
"s_waitcnt vmcnt(11)\n"
"s_barrier\n"
"v_mfma_f32_32x32x16_bf16 v[48:63], v[212:215], v[236:239], v[48:63]\n"
"ds_read_b64_tr_b16 v[232:233], v2 offset:24576\n"
"ds_read_b64_tr_b16 v[234:235], v2 offset:25088\n"
"ds_read_b64_tr_b16 v[236:237], v2 offset:26624\n"
"ds_read_b64_tr_b16 v[238:239], v2 offset:27136\n"
"s_add_u32 s70, s69, 663808\n"
"s_add_u32 m0, s91, 24576\n"
"s_waitcnt lgkmcnt(8)\n"
"v_mfma_f32_32x32x16_bf16 v[48:63], v[216:219], v[240:243], v[48:63]\n"
"ds_read_b64_tr_b16 v[240:241], v2 offset:28672\n"
"ds_read_b64_tr_b16 v[242:243], v2 offset:29184\n"
"buffer_load_dwordx4 v7, s[48:51], s70 offen lds\n"
"s_add_u32 m0, m0, 0x2000\n"
"v_mfma_f32_32x32x16_bf16 v[48:63], v[220:223], v[244:247], v[48:63]\n"
"ds_read_b64_tr_b16 v[244:245], v2 offset:30720\n"
"ds_read_b64_tr_b16 v[246:247], v2 offset:31232\n"
"buffer_load_dwordx4 v8, s[48:51], s70 offen lds\n"
"s_add_u32 m0, m0, 0x2000\n"
"s_waitcnt lgkmcnt(8)\n"
"v_mfma_f32_32x32x16_bf16 v[48:63], v[224:227], v[248:251], v[48:63]\n"
"ds_read_b64_tr_b16 v[248:249], v2 offset:32768\n"
"ds_read_b64_tr_b16 v[250:251], v2 offset:33280\n"
"buffer_load_dwordx4 v9, s[48:51], s70 offen lds\n"
"v_mfma_f32_32x32x16_bf16 v[48:63], v[228:231], v[252:255], v[48:63]\n"
"ds_read_b64_tr_b16 v[252:253], v2 offset:34816\n"
"ds_read_b64_tr_b16 v[254:255], v2 offset:35328\n"
".Lkc1_V1_6_%=:\n"
"s_waitcnt lgkmcnt(8)\n"
"v_mfma_f32_32x32x16_bf16 v[32:47], v[88:91], v[232:235], 0\n"
"ds_read_b64_tr_b16 v[232:233], v2 offset:36864\n"
"ds_read_b64_tr_b16 v[234:235], v2 offset:37376\n"
"s_mov_b32 s84, s85\n"
"v_mfma_f32_32x32x16_bf16 v[32:47], v[92:95], v[236:239], v[32:47]\n"
"ds_read_b64_tr_b16 v[236:237], v2 offset:38912\n"
"ds_read_b64_tr_b16 v[238:239], v2 offset:39424\n"
"s_waitcnt lgkmcnt(8)\n"
"v_mfma_f32_32x32x16_bf16 v[32:47], v[96:99], v[240:243], v[32:47]\n"
"ds_read_b64_tr_b16 v[240:241], v2 offset:40960\n"
"ds_read_b64_tr_b16 v[242:243], v2 offset:41472\n"
"v_mfma_f32_32x32x16_bf16 v[32:47], v[100:103], v[244:247], v[32:47]\n"
"ds_read_b64_tr_b16 v[244:245], v2 offset:43008\n"
"ds_read_b64_tr_b16 v[246:247], v2 offset:43520\n"
"s_waitcnt lgkmcnt(8)\n"
"v_mfma_f32_32x32x16_bf16 v[32:47], v[104:107], v[248:251], v[32:47]\n"
"ds_read_b64_tr_b16 v[248:249], v2 offset:45056\n"
"ds_read_b64_tr_b16 v[250:251], v2 offset:45568\n"
"v_cvt_pk_bf16_f32 v80, v48, v49\n"
"v_mfma_f32_32x32x16_bf16 v[32:47], v[108:111], v[252:255], v[32:47]\n"
"ds_read_b64_tr_b16 v[252:253], v2 offset:47104\n"
"ds_read_b64_tr_b16 v[254:255], v2 offset:47616\n"
"s_waitcnt lgkmcnt(8)\n"
"v_mfma_f32_32x32x16_bf16 v[32:47], v[112:115], v[232:235], v[32:47]\n"
"s_waitcnt vmcnt(11)\n"
"s_barrier\n"
"v_mfma_f32_32x32x16_bf16 v[32:47], v[116:119], v[236:239], v[32:47]\n"
"ds_read_b64_tr_b16 v[232:233], v3 offset:0\n"
"ds_read_b64_tr_b16 v[234:235], v3 offset:512\n"
"ds_read_b64_tr_b16 v[236:237], v3 offset:2048\n"
"ds_read_b64_tr_b16 v[238:239], v3 offset:2560\n"
"s_add_u32 s70, s69, 1327360\n"
"s_add_u32 m0, s91, 49152\n"
"v_cvt_pk_bf16_f32 v81, v50, v51\n"
"s_waitcnt lgkmcnt(8)\n"
"v_mfma_f32_32x32x16_bf16 v[32:47], v[120:123], v[240:243], v[32:47]\n"
"ds_read_b64_tr_b16 v[240:241], v3 offset:4096\n"
"ds_read_b64_tr_b16 v[242:243], v3 offset:4608\n"
"buffer_load_dwordx4 v7, s[48:51], s70 offen lds\n"
"s_add_u32 m0, m0, 0x2000\n"
"v_mfma_f32_32x32x16_bf16 v[32:47], v[124:127], v[244:247], v[32:47]\n"
"ds_read_b64_tr_b16 v[244:245], v3 offset:6144\n"
"ds_read_b64_tr_b16 v[246:247], v3 offset:6656\n"
"buffer_load_dwordx4 v8, s[48:51], s70 offen lds\n"
"s_add_u32 m0, m0, 0x2000\n"
"s_waitcnt lgkmcnt(8)\n"
"v_mfma_f32_32x32x16_bf16 v[32:47], v[128:131], v[248:251], v[32:47]\n"
"ds_read_b64_tr_b16 v[248:249], v3 offset:8192\n"
"ds_read_b64_tr_b16 v[250:251], v3 offset:8704\n"
"buffer_load_dwordx4 v9, s[48:51], s70 offen lds\n"
"v_cvt_pk_bf16_f32 v82, v52, v53\n"
"v_mfma_f32_32x32x16_bf16 v[32:47], v[132:135], v[252:255], v[32:47]\n"
"ds_read_b64_tr_b16 v[252:253], v3 offset:10240\n"
"ds_read_b64_tr_b16 v[254:255], v3 offset:10752\n"
"s_add_u32 s71, s82, 0\n"
"buffer_load_dword v29, v11, s[56:59], s71 offen\n"
"buffer_load_dword v29, v12, s[60:63], s92 offen\n"
"s_add_u32 s92, s92, 384\n"
"s_waitcnt lgkmcnt(8)\n"
"v_mfma_f32_32x32x16_bf16 v[32:47], v[136:139], v[232:235], v[32:47]\n"
"ds_read_b64_tr_b16 v[232:233], v3 offset:12288\n"
"ds_read_b64_tr_b16 v[234:235], v3 offset:12800\n"
"v_mfma_f32_32x32x16_bf16 v[32:47], v[140:143], v[236:239], v[32:47]\n"
"ds_read_b64_tr_b16 v[236:237], v3 offset:14336\n"
"ds_read_b64_tr_b16 v[238:239], v3 offset:14848\n"
"v_cvt_pk_bf16_f32 v83, v54, v55\n"
"s_waitcnt lgkmcnt(8)\n"
"v_mfma_f32_32x32x16_bf16 v[32:47], v[144:147], v[240:243], v[32:47]\n"
"ds_read_b64_tr_b16 v[240:241], v3 offset:16384\n"
"ds_read_b64_tr_b16 v[242:243], v3 offset:16896\n"
"v_mfma_f32_32x32x16_bf16 v[32:47], v[148:151], v[244:247], v[32:47]\n"
"ds_read_b64_tr_b16 v[244:245], v3 offset:18432\n"
"ds_read_b64_tr_b16 v[246:247], v3 offset:18944\n"
"s_waitcnt lgkmcnt(8)\n"
"v_mfma_f32_32x32x16_bf16 v[32:47], v[152:155], v[248:251], v[32:47]\n"
"ds_read_b64_tr_b16 v[248:249], v3 offset:20480\n"
"ds_read_b64_tr_b16 v[250:251], v3 offset:20992\n"
"v_cvt_pk_bf16_f32 v84, v56, v57\n"
"v_mfma_f32_32x32x16_bf16 v[32:47], v[156:159], v[252:255], v[32:47]\n"
"ds_read_b64_tr_b16 v[252:253], v3 offset:22528\n"
"ds_read_b64_tr_b16 v[254:255], v3 offset:23040\n"
"s_waitcnt lgkmcnt(8)\n"
"v_mfma_f32_32x32x16_bf16 v[32:47], v[160:163], v[232:235], v[32:47]\n"
"s_waitcnt vmcnt(11)\n"
"s_barrier\n"
"v_mfma_f32_32x32x16_bf16 v[32:47], v[164:167], v[236:239], v[32:47]\n"
"ds_read_b64_tr_b16 v[232:233], v3 offset:24576\n"
"ds_read_b64_tr_b16 v[234:235], v3 offset:25088\n"
"ds_read_b64_tr_b16 v[236:237], v3 offset:26624\n"
"ds_read_b64_tr_b16 v[238:239], v3 offset:27136\n"
"s_add_u32 s70, s82, 0\n"
"s_add_u32 m0, s91, 73728\n"
"v_cvt_pk_bf16_f32 v85, v58, v59\n"
"s_waitcnt lgkmcnt(8)\n"
"v_mfma_f32_32x32x16_bf16 v[32:47], v[168:171], v[240:243], v[32:47]\n"
"ds_read_b64_tr_b16 v[240:241], v3 offset:28672\n"
"ds_read_b64_tr_b16 v[242:243], v3 offset:29184\n"
"buffer_load_dwordx4 v4, s[52:55], s70 offen lds\n"
"s_add_u32 m0, m0, 0x2000\n"
"v_mfma_f32_32x32x16_bf16 v[32:47], v[172:175], v[244:247], v[32:47]\n"
"ds_read_b64_tr_b16 v[244:245], v3 offset:30720\n"
"ds_read_b64_tr_b16 v[246:247], v3 offset:31232\n"
"buffer_load_dwordx4 v5, s[52:55], s70 offen lds\n"
"s_add_u32 m0, m0, 0x2000\n"
"s_waitcnt lgkmcnt(8)\n"
"v_mfma_f32_32x32x16_bf16 v[32:47], v[176:179], v[248:251], v[32:47]\n"
"ds_read_b64_tr_b16 v[248:249], v3 offset:32768\n"
"ds_read_b64_tr_b16 v[250:251], v3 offset:33280\n"
"buffer_load_dwordx4 v6, s[52:55], s70 offen lds\n"
"v_cvt_pk_bf16_f32 v86, v60, v61\n"
"v_mfma_f32_32x32x16_bf16 v[32:47], v[180:183], v[252:255], v[32:47]\n"
"ds_read_b64_tr_b16 v[252:253], v3 offset:34816\n"
"ds_read_b64_tr_b16 v[254:255], v3 offset:35328\n"
"s_waitcnt lgkmcnt(8)\n"
"v_mfma_f32_32x32x16_bf16 v[32:47], v[184:187], v[232:235], v[32:47]\n"
"ds_read_b64_tr_b16 v[232:233], v3 offset:36864\n"
"ds_read_b64_tr_b16 v[234:235], v3 offset:37376\n"
"v_mfma_f32_32x32x16_bf16 v[32:47], v[188:191], v[236:239], v[32:47]\n"
"ds_read_b64_tr_b16 v[236:237], v3 offset:38912\n"
"ds_read_b64_tr_b16 v[238:239], v3 offset:39424\n"
"v_cvt_pk_bf16_f32 v87, v62, v63\n"
"s_waitcnt lgkmcnt(8)\n"
"v_mfma_f32_32x32x16_bf16 v[32:47], v[192:195], v[240:243], v[32:47]\n"
"ds_read_b64_tr_b16 v[240:241], v3 offset:40960\n"
"ds_read_b64_tr_b16 v[242:243], v3 offset:41472\n"
"v_mfma_f32_32x32x16_bf16 v[32:47], v[196:199], v[244:247], v[32:47]\n"
"ds_read_b64_tr_b16 v[244:245], v3 offset:43008\n"
"ds_read_b64_tr_b16 v[246:247], v3 offset:43520\n"
"s_waitcnt lgkmcnt(8)\n"
"v_mfma_f32_32x32x16_bf16 v[32:47], v[200:203], v[248:251], v[32:47]\n"
"ds_read_b64_tr_b16 v[248:249], v3 offset:45056\n"
"ds_read_b64_tr_b16 v[250:251], v3 offset:45568\n"
"buffer_store_dwordx4 v[80:83], v10, s[44:47], s84 offen sc1\n"
"v_mfma_f32_32x32x16_bf16 v[32:47], v[204:207], v[252:255], v[32:47]\n"
"ds_read_b64_tr_b16 v[252:253], v3 offset:47104\n"
"ds_read_b64_tr_b16 v[254:255], v3 offset:47616\n"
"s_waitcnt lgkmcnt(8)\n"
"v_mfma_f32_32x32x16_bf16 v[32:47], v[208:211], v[232:235], v[32:47]\n"
"s_waitcnt vmcnt(11)\n"
"s_barrier\n"
"v_mfma_f32_32x32x16_bf16 v[32:47], v[212:215], v[236:239], v[32:47]\n"
"ds_read_b64_tr_b16 v[232:233], v1 offset:0\n"
"ds_read_b64_tr_b16 v[234:235], v1 offset:512\n"
"ds_read_b64_tr_b16 v[236:237], v1 offset:2048\n"
"ds_read_b64_tr_b16 v[238:239], v1 offset:2560\n"
"s_add_u32 s70, s82, 1769472\n"
"s_add_u32 m0, s91, 98304\n"
"buffer_store_dwordx4 v[84:87], v10, s[44:47], s84 offen offset:1024 sc1\n"
"s_waitcnt lgkmcnt(8)\n"
"v_mfma_f32_32x32x16_bf16 v[32:47], v[216:219], v[240:243], v[32:47]\n"
"ds_read_b64_tr_b16 v[240:241], v1 offset:4096\n"
"ds_read_b64_tr_b16 v[242:243], v1 offset:4608\n"
"buffer_load_dwordx4 v4, s[52:55], s70 offen lds\n"
"s_add_u32 m0, m0, 0x2000\n"
"v_mfma_f32_32x32x16_bf16 v[32:47], v[220:223], v[244:247], v[32:47]\n"
"ds_read_b64_tr_b16 v[244:245], v1 offset:6144\n"
"ds_read_b64_tr_b16 v[246:247], v1 offset:6656\n"
"buffer_load_dwordx4 v5, s[52:55], s70 offen lds\n"
"s_add_u32 m0, m0, 0x2000\n"
"s_waitcnt lgkmcnt(8)\n"
"v_mfma_f32_32x32x16_bf16 v[32:47], v[224:227], v[248:251], v[32:47]\n"
"ds_read_b64_tr_b16 v[248:249], v1 offset:8192\n"
"ds_read_b64_tr_b16 v[250:251], v1 offset:8704\n"
"buffer_load_dwordx4 v6, s[52:55], s70 offen lds\n"
"v_mfma_f32_32x32x16_bf16 v[32:47], v[228:231], v[252:255], v[32:47]\n"
"ds_read_b64_tr_b16 v[252:253], v1 offset:10240\n"
"ds_read_b64_tr_b16 v[254:255], v1 offset:10752\n"
".Lkc1_VL_7_%=:\n"
"s_waitcnt lgkmcnt(8)\n"
"v_mfma_f32_32x32x16_bf16 v[48:63], v[88:91], v[232:235], 0\n"
"ds_read_b64_tr_b16 v[232:233], v1 offset:12288\n"
"ds_read_b64_tr_b16 v[234:235], v1 offset:12800\n"
"s_add_u32 s72, s85, 4096\n"
"s_cmp_eq_u32 s68, 3\n"
"s_cselect_b32 s84, s72, 1073741824\n"
"v_mfma_f32_32x32x16_bf16 v[48:63], v[92:95], v[236:239], v[48:63]\n"
"ds_read_b64_tr_b16 v[236:237], v1 offset:14336\n"
"ds_read_b64_tr_b16 v[238:239], v1 offset:14848\n"
"s_waitcnt lgkmcnt(8)\n"
"v_mfma_f32_32x32x16_bf16 v[48:63], v[96:99], v[240:243], v[48:63]\n"
"ds_read_b64_tr_b16 v[240:241], v1 offset:16384\n"
"ds_read_b64_tr_b16 v[242:243], v1 offset:16896\n"
"v_mfma_f32_32x32x16_bf16 v[48:63], v[100:103], v[244:247], v[48:63]\n"
"ds_read_b64_tr_b16 v[244:245], v1 offset:18432\n"
"ds_read_b64_tr_b16 v[246:247], v1 offset:18944\n"
"s_waitcnt lgkmcnt(8)\n"
"v_mfma_f32_32x32x16_bf16 v[48:63], v[104:107], v[248:251], v[48:63]\n"
"ds_read_b64_tr_b16 v[248:249], v1 offset:20480\n"
"ds_read_b64_tr_b16 v[250:251], v1 offset:20992\n"
"v_cvt_pk_bf16_f32 v80, v32, v33\n"
"v_mfma_f32_32x32x16_bf16 v[48:63], v[108:111], v[252:255], v[48:63]\n"
"ds_read_b64_tr_b16 v[252:253], v1 offset:22528\n"
"ds_read_b64_tr_b16 v[254:255], v1 offset:23040\n"
"s_waitcnt lgkmcnt(8)\n"
"v_mfma_f32_32x32x16_bf16 v[48:63], v[112:115], v[232:235], v[48:63]\n"
"s_waitcnt vmcnt(11)\n"
"s_barrier\n"
"v_mfma_f32_32x32x16_bf16 v[48:63], v[116:119], v[236:239], v[48:63]\n"
"ds_read_b64_tr_b16 v[232:233], v1 offset:24576\n"
"ds_read_b64_tr_b16 v[234:235], v1 offset:25088\n"
"ds_read_b64_tr_b16 v[236:237], v1 offset:26624\n"
"ds_read_b64_tr_b16 v[238:239], v1 offset:27136\n"
"s_add_u32 s70, s82, 3538944\n"
"s_add_u32 m0, s91, 122880\n"
"v_cvt_pk_bf16_f32 v81, v34, v35\n"
"s_waitcnt lgkmcnt(8)\n"
"v_mfma_f32_32x32x16_bf16 v[48:63], v[120:123], v[240:243], v[48:63]\n"
"ds_read_b64_tr_b16 v[240:241], v1 offset:28672\n"
"ds_read_b64_tr_b16 v[242:243], v1 offset:29184\n"
"buffer_load_dwordx4 v4, s[52:55], s70 offen lds\n"
"s_add_u32 m0, m0, 0x2000\n"
"v_mfma_f32_32x32x16_bf16 v[48:63], v[124:127], v[244:247], v[48:63]\n"
"ds_read_b64_tr_b16 v[244:245], v1 offset:30720\n"
"ds_read_b64_tr_b16 v[246:247], v1 offset:31232\n"
"buffer_load_dwordx4 v5, s[52:55], s70 offen lds\n"
"s_add_u32 m0, m0, 0x2000\n"
"s_waitcnt lgkmcnt(8)\n"
"v_mfma_f32_32x32x16_bf16 v[48:63], v[128:131], v[248:251], v[48:63]\n"
"ds_read_b64_tr_b16 v[248:249], v1 offset:32768\n"
"ds_read_b64_tr_b16 v[250:251], v1 offset:33280\n"
"buffer_load_dwordx4 v6, s[52:55], s70 offen lds\n"
"v_cvt_pk_bf16_f32 v82, v36, v37\n"
"v_mfma_f32_32x32x16_bf16 v[48:63], v[132:135], v[252:255], v[48:63]\n"
"ds_read_b64_tr_b16 v[252:253], v1 offset:34816\n"
"ds_read_b64_tr_b16 v[254:255], v1 offset:35328\n"
"s_add_u32 s71, s82, 128\n"
"buffer_load_dword v29, v11, s[52:55], s71 offen\n"
"buffer_load_dword v29, v12, s[60:63], s92 offen\n"
"s_add_u32 s92, s92, 384\n"
"s_waitcnt lgkmcnt(8)\n"
"v_mfma_f32_32x32x16_bf16 v[48:63], v[136:139], v[232:235], v[48:63]\n"
"ds_read_b64_tr_b16 v[232:233], v1 offset:36864\n"
"ds_read_b64_tr_b16 v[234:235], v1 offset:37376\n"
"v_mfma_f32_32x32x16_bf16 v[48:63], v[140:143], v[236:239], v[48:63]\n"
"ds_read_b64_tr_b16 v[236:237], v1 offset:38912\n"
"ds_read_b64_tr_b16 v[238:239], v1 offset:39424\n"
"v_cvt_pk_bf16_f32 v83, v38, v39\n"
"s_waitcnt lgkmcnt(8)\n"
"v_mfma_f32_32x32x16_bf16 v[48:63], v[144:147], v[240:243], v[48:63]\n"
"ds_read_b64_tr_b16 v[240:241], v1 offset:40960\n"
"ds_read_b64_tr_b16 v[242:243], v1 offset:41472\n"
"v_mfma_f32_32x32x16_bf16 v[48:63], v[148:151], v[244:247], v[48:63]\n"
"ds_read_b64_tr_b16 v[244:245], v1 offset:43008\n"
"ds_read_b64_tr_b16 v[246:247], v1 offset:43520\n"
"s_waitcnt lgkmcnt(8)\n"
"v_mfma_f32_32x32x16_bf16 v[48:63], v[152:155], v[248:251], v[48:63]\n"
"ds_read_b64_tr_b16 v[248:249], v1 offset:45056\n"
"ds_read_b64_tr_b16 v[250:251], v1 offset:45568\n"
"v_cvt_pk_bf16_f32 v84, v40, v41\n"
"v_mfma_f32_32x32x16_bf16 v[48:63], v[156:159], v[252:255], v[48:63]\n"
"ds_read_b64_tr_b16 v[252:253], v1 offset:47104\n"
"ds_read_b64_tr_b16 v[254:255], v1 offset:47616\n"
"s_waitcnt lgkmcnt(8)\n"
"v_mfma_f32_32x32x16_bf16 v[48:63], v[160:163], v[232:235], v[48:63]\n"
"s_waitcnt vmcnt(11)\n"
"s_barrier\n"
"v_mfma_f32_32x32x16_bf16 v[48:63], v[164:167], v[236:239], v[48:63]\n"
"ds_read_b64_tr_b16 v[232:233], v2 offset:0\n"
"ds_read_b64_tr_b16 v[234:235], v2 offset:512\n"
"ds_read_b64_tr_b16 v[236:237], v2 offset:2048\n"
"ds_read_b64_tr_b16 v[238:239], v2 offset:2560\n"
"s_add_u32 s70, s82, 0\n"
"s_add_u32 m0, s91, 0\n"
"v_cvt_pk_bf16_f32 v85, v42, v43\n"
"s_waitcnt lgkmcnt(8)\n"
"v_mfma_f32_32x32x16_bf16 v[48:63], v[168:171], v[240:243], v[48:63]\n"
"ds_read_b64_tr_b16 v[240:241], v2 offset:4096\n"
"ds_read_b64_tr_b16 v[242:243], v2 offset:4608\n"
"buffer_load_dwordx4 v4, s[56:59], s70 offen lds\n"
"s_add_u32 m0, m0, 0x2000\n"
"v_mfma_f32_32x32x16_bf16 v[48:63], v[172:175], v[244:247], v[48:63]\n"
"ds_read_b64_tr_b16 v[244:245], v2 offset:6144\n"
"ds_read_b64_tr_b16 v[246:247], v2 offset:6656\n"
"buffer_load_dwordx4 v5, s[56:59], s70 offen lds\n"
"s_add_u32 m0, m0, 0x2000\n"
"s_waitcnt lgkmcnt(8)\n"
"v_mfma_f32_32x32x16_bf16 v[48:63], v[176:179], v[248:251], v[48:63]\n"
"ds_read_b64_tr_b16 v[248:249], v2 offset:8192\n"
"ds_read_b64_tr_b16 v[250:251], v2 offset:8704\n"
"buffer_load_dwordx4 v6, s[56:59], s70 offen lds\n"
"v_cvt_pk_bf16_f32 v86, v44, v45\n"
"v_mfma_f32_32x32x16_bf16 v[48:63], v[180:183], v[252:255], v[48:63]\n"
"ds_read_b64_tr_b16 v[252:253], v2 offset:10240\n"
"ds_read_b64_tr_b16 v[254:255], v2 offset:10752\n"
"s_waitcnt lgkmcnt(8)\n"
"v_mfma_f32_32x32x16_bf16 v[48:63], v[184:187], v[232:235], v[48:63]\n"
"ds_read_b64_tr_b16 v[232:233], v2 offset:12288\n"
"ds_read_b64_tr_b16 v[234:235], v2 offset:12800\n"
"v_mfma_f32_32x32x16_bf16 v[48:63], v[188:191], v[236:239], v[48:63]\n"
"ds_read_b64_tr_b16 v[236:237], v2 offset:14336\n"
"ds_read_b64_tr_b16 v[238:239], v2 offset:14848\n"
"v_cvt_pk_bf16_f32 v87, v46, v47\n"
"s_waitcnt lgkmcnt(8)\n"
"v_mfma_f32_32x32x16_bf16 v[48:63], v[192:195], v[240:243], v[48:63]\n"
"ds_read_b64_tr_b16 v[240:241], v2 offset:16384\n"
"ds_read_b64_tr_b16 v[242:243], v2 offset:16896\n"
"v_mfma_f32_32x32x16_bf16 v[48:63], v[196:199], v[244:247], v[48:63]\n"
"ds_read_b64_tr_b16 v[244:245], v2 offset:18432\n"
"ds_read_b64_tr_b16 v[246:247], v2 offset:18944\n"
"s_waitcnt lgkmcnt(8)\n"
"v_mfma_f32_32x32x16_bf16 v[48:63], v[200:203], v[248:251], v[48:63]\n"
"ds_read_b64_tr_b16 v[248:249], v2 offset:20480\n"
"ds_read_b64_tr_b16 v[250:251], v2 offset:20992\n"
"buffer_store_dwordx4 v[80:83], v10, s[44:47], s84 offen sc1\n"
"v_mfma_f32_32x32x16_bf16 v[48:63], v[204:207], v[252:255], v[48:63]\n"
"ds_read_b64_tr_b16 v[252:253], v2 offset:22528\n"
"ds_read_b64_tr_b16 v[254:255], v2 offset:23040\n"
"s_waitcnt lgkmcnt(8)\n"
"v_mfma_f32_32x32x16_bf16 v[48:63], v[208:211], v[232:235], v[48:63]\n"
"s_waitcnt vmcnt(11)\n"
"s_barrier\n"
"v_mfma_f32_32x32x16_bf16 v[48:63], v[212:215], v[236:239], v[48:63]\n"
"ds_read_b64_tr_b16 v[232:233], v2 offset:24576\n"
"ds_read_b64_tr_b16 v[234:235], v2 offset:25088\n"
"ds_read_b64_tr_b16 v[236:237], v2 offset:26624\n"
"ds_read_b64_tr_b16 v[238:239], v2 offset:27136\n"
"s_add_u32 s70, s82, 1769472\n"
"s_add_u32 m0, s91, 24576\n"
"buffer_store_dwordx4 v[84:87], v10, s[44:47], s84 offen offset:1024 sc1\n"
"s_waitcnt lgkmcnt(8)\n"
"v_mfma_f32_32x32x16_bf16 v[48:63], v[216:219], v[240:243], v[48:63]\n"
"ds_read_b64_tr_b16 v[240:241], v2 offset:28672\n"
"ds_read_b64_tr_b16 v[242:243], v2 offset:29184\n"
"buffer_load_dwordx4 v4, s[56:59], s70 offen lds\n"
"s_add_u32 m0, m0, 0x2000\n"
"v_mfma_f32_32x32x16_bf16 v[48:63], v[220:223], v[244:247], v[48:63]\n"
"ds_read_b64_tr_b16 v[244:245], v2 offset:30720\n"
"ds_read_b64_tr_b16 v[246:247], v2 offset:31232\n"
"buffer_load_dwordx4 v5, s[56:59], s70 offen lds\n"
"s_add_u32 m0, m0, 0x2000\n"
"s_waitcnt lgkmcnt(8)\n"
"v_mfma_f32_32x32x16_bf16 v[48:63], v[224:227], v[248:251], v[48:63]\n"
"ds_read_b64_tr_b16 v[248:249], v2 offset:32768\n"
"ds_read_b64_tr_b16 v[250:251], v2 offset:33280\n"
"buffer_load_dwordx4 v6, s[56:59], s70 offen lds\n"
"v_mfma_f32_32x32x16_bf16 v[48:63], v[228:231], v[252:255], v[48:63]\n"
"ds_read_b64_tr_b16 v[252:253], v2 offset:34816\n"
"ds_read_b64_tr_b16 v[254:255], v2 offset:35328\n"
"s_branch .Lkc1_GQ_8_%=\n"
".Lkc1_GQ_8_%=:\n"
"s_waitcnt lgkmcnt(8)\n"
"v_mfma_f32_32x32x16_bf16 v[32:47], v[232:235], v[88:91], 0\n"
"ds_read_b64_tr_b16 v[232:233], v2 offset:36864\n"
"ds_read_b64_tr_b16 v[234:235], v2 offset:37376\n"
"s_sub_u32 s72, s68, 1\n"
"s_lshl_b32 s72, s72, 12\n"
"s_add_u32 s84, s85, s72\n"
"v_mfma_f32_32x32x16_bf16 v[32:47], v[236:239], v[92:95], v[32:47]\n"
"ds_read_b64_tr_b16 v[236:237], v2 offset:38912\n"
"ds_read_b64_tr_b16 v[238:239], v2 offset:39424\n"
"s_add_u32 s72, s83, 1\n"
"s_cmp_lt_u32 s72, s81\n"
"s_cselect_b32 s54, 5308416, 0\n"
"s_waitcnt lgkmcnt(8)\n"
"v_mfma_f32_32x32x16_bf16 v[32:47], v[240:243], v[96:99], v[32:47]\n"
"ds_read_b64_tr_b16 v[240:241], v2 offset:40960\n"
"ds_read_b64_tr_b16 v[242:243], v2 offset:41472\n"
"v_mfma_f32_32x32x16_bf16 v[32:47], v[244:247], v[100:103], v[32:47]\n"
"ds_read_b64_tr_b16 v[244:245], v2 offset:43008\n"
"ds_read_b64_tr_b16 v[246:247], v2 offset:43520\n"
"s_waitcnt lgkmcnt(8)\n"
"v_mfma_f32_32x32x16_bf16 v[32:47], v[248:251], v[104:107], v[32:47]\n"
"ds_read_b64_tr_b16 v[248:249], v2 offset:45056\n"
"ds_read_b64_tr_b16 v[250:251], v2 offset:45568\n"
"v_cvt_pk_bf16_f32 v80, v48, v49\n"
"v_mfma_f32_32x32x16_bf16 v[32:47], v[252:255], v[108:111], v[32:47]\n"
"ds_read_b64_tr_b16 v[252:253], v2 offset:47104\n"
"ds_read_b64_tr_b16 v[254:255], v2 offset:47616\n"
"s_waitcnt lgkmcnt(8)\n"
"v_mfma_f32_32x32x16_bf16 v[32:47], v[232:235], v[112:115], v[32:47]\n"
"s_waitcnt vmcnt(11)\n"
"s_barrier\n"
"v_mfma_f32_32x32x16_bf16 v[32:47], v[236:239], v[116:119], v[32:47]\n"
"ds_read_b64_tr_b16 v[232:233], v3 offset:0\n"
"ds_read_b64_tr_b16 v[234:235], v3 offset:512\n"
"ds_read_b64_tr_b16 v[236:237], v3 offset:2048\n"
"ds_read_b64_tr_b16 v[238:239], v3 offset:2560\n"
"s_add_u32 s70, s82, 3538944\n"
"s_add_u32 m0, s91, 49152\n"
"v_cvt_pk_bf16_f32 v81, v50, v51\n"
"s_waitcnt lgkmcnt(8)\n"
"v_mfma_f32_32x32x16_bf16 v[32:47], v[240:243], v[120:123], v[32:47]\n"
"ds_read_b64_tr_b16 v[240:241], v3 offset:4096\n"
"ds_read_b64_tr_b16 v[242:243], v3 offset:4608\n"
"buffer_load_dwordx4 v4, s[56:59], s70 offen lds\n"
"s_add_u32 m0, m0, 0x2000\n"
"v_mfma_f32_32x32x16_bf16 v[32:47], v[244:247], v[124:127], v[32:47]\n"
"ds_read_b64_tr_b16 v[244:245], v3 offset:6144\n"
"ds_read_b64_tr_b16 v[246:247], v3 offset:6656\n"
"buffer_load_dwordx4 v5, s[56:59], s70 offen lds\n"
"s_add_u32 m0, m0, 0x2000\n"
"s_waitcnt lgkmcnt(8)\n"
"v_mfma_f32_32x32x16_bf16 v[32:47], v[248:251], v[128:131], v[32:47]\n"
"ds_read_b64_tr_b16 v[248:249], v3 offset:8192\n"
"ds_read_b64_tr_b16 v[250:251], v3 offset:8704\n"
"buffer_load_dwordx4 v6, s[56:59], s70 offen lds\n"
"v_cvt_pk_bf16_f32 v82, v52, v53\n"
"v_mfma_f32_32x32x16_bf16 v[32:47], v[252:255], v[132:135], v[32:47]\n"
"ds_read_b64_tr_b16 v[252:253], v3 offset:10240\n"
"ds_read_b64_tr_b16 v[254:255], v3 offset:10752\n"
"s_add_u32 s71, s82, 128\n"
"buffer_load_dword v29, v11, s[56:59], s71 offen\n"
"buffer_load_dword v29, v12, s[60:63], s92 offen\n"
"s_add_u32 s92, s92, 384\n"
"s_waitcnt lgkmcnt(8)\n"
"v_mfma_f32_32x32x16_bf16 v[32:47], v[232:235], v[136:139], v[32:47]\n"
"ds_read_b64_tr_b16 v[232:233], v3 offset:12288\n"
"ds_read_b64_tr_b16 v[234:235], v3 offset:12800\n"
"v_mfma_f32_32x32x16_bf16 v[32:47], v[236:239], v[140:143], v[32:47]\n"
"ds_read_b64_tr_b16 v[236:237], v3 offset:14336\n"
"ds_read_b64_tr_b16 v[238:239], v3 offset:14848\n"
"v_cvt_pk_bf16_f32 v83, v54, v55\n"
"s_waitcnt lgkmcnt(8)\n"
"v_mfma_f32_32x32x16_bf16 v[32:47], v[240:243], v[144:147], v[32:47]\n"
"ds_read_b64_tr_b16 v[240:241], v3 offset:16384\n"
"ds_read_b64_tr_b16 v[242:243], v3 offset:16896\n"
"v_mfma_f32_32x32x16_bf16 v[32:47], v[244:247], v[148:151], v[32:47]\n"
"ds_read_b64_tr_b16 v[244:245], v3 offset:18432\n"
"ds_read_b64_tr_b16 v[246:247], v3 offset:18944\n"
"s_waitcnt lgkmcnt(8)\n"
"v_mfma_f32_32x32x16_bf16 v[32:47], v[248:251], v[152:155], v[32:47]\n"
"ds_read_b64_tr_b16 v[248:249], v3 offset:20480\n"
"ds_read_b64_tr_b16 v[250:251], v3 offset:20992\n"
"v_cvt_pk_bf16_f32 v84, v56, v57\n"
"v_mfma_f32_32x32x16_bf16 v[32:47], v[252:255], v[156:159], v[32:47]\n"
"ds_read_b64_tr_b16 v[252:253], v3 offset:22528\n"
"ds_read_b64_tr_b16 v[254:255], v3 offset:23040\n"
"s_waitcnt lgkmcnt(8)\n"
"v_mfma_f32_32x32x16_bf16 v[32:47], v[232:235], v[160:163], v[32:47]\n"
"s_waitcnt vmcnt(11)\n"
"s_barrier\n"
"v_mfma_f32_32x32x16_bf16 v[32:47], v[236:239], v[164:167], v[32:47]\n"
"ds_read_b64_tr_b16 v[232:233], v3 offset:24576\n"
"ds_read_b64_tr_b16 v[234:235], v3 offset:25088\n"
"ds_read_b64_tr_b16 v[236:237], v3 offset:26624\n"
"ds_read_b64_tr_b16 v[238:239], v3 offset:27136\n"
"s_add_u32 s70, s82, 128\n"
"s_add_u32 m0, s91, 73728\n"
"v_cvt_pk_bf16_f32 v85, v58, v59\n"
"s_waitcnt lgkmcnt(8)\n"
"v_mfma_f32_32x32x16_bf16 v[32:47], v[240:243], v[168:171], v[32:47]\n"
"ds_read_b64_tr_b16 v[240:241], v3 offset:28672\n"
"ds_read_b64_tr_b16 v[242:243], v3 offset:29184\n"
"buffer_load_dwordx4 v4, s[52:55], s70 offen lds\n"
"s_add_u32 m0, m0, 0x2000\n"
"v_mfma_f32_32x32x16_bf16 v[32:47], v[244:247], v[172:175], v[32:47]\n"
"ds_read_b64_tr_b16 v[244:245], v3 offset:30720\n"
"ds_read_b64_tr_b16 v[246:247], v3 offset:31232\n"
"buffer_load_dwordx4 v5, s[52:55], s70 offen lds\n"
"s_add_u32 m0, m0, 0x2000\n"
"s_waitcnt lgkmcnt(8)\n"
"v_mfma_f32_32x32x16_bf16 v[32:47], v[248:251], v[176:179], v[32:47]\n"
"ds_read_b64_tr_b16 v[248:249], v3 offset:32768\n"
"ds_read_b64_tr_b16 v[250:251], v3 offset:33280\n"
"buffer_load_dwordx4 v6, s[52:55], s70 offen lds\n"
"v_cvt_pk_bf16_f32 v86, v60, v61\n"
"v_mfma_f32_32x32x16_bf16 v[32:47], v[252:255], v[180:183], v[32:47]\n"
"ds_read_b64_tr_b16 v[252:253], v3 offset:34816\n"
"ds_read_b64_tr_b16 v[254:255], v3 offset:35328\n"
"s_waitcnt lgkmcnt(8)\n"
"v_mfma_f32_32x32x16_bf16 v[32:47], v[232:235], v[184:187], v[32:47]\n"
"ds_read_b64_tr_b16 v[232:233], v3 offset:36864\n"
"ds_read_b64_tr_b16 v[234:235], v3 offset:37376\n"
"v_mfma_f32_32x32x16_bf16 v[32:47], v[236:239], v[188:191], v[32:47]\n"
"ds_read_b64_tr_b16 v[236:237], v3 offset:38912\n"
"ds_read_b64_tr_b16 v[238:239], v3 offset:39424\n"
"v_cvt_pk_bf16_f32 v87, v62, v63\n"
"s_waitcnt lgkmcnt(8)\n"
"v_mfma_f32_32x32x16_bf16 v[32:47], v[240:243], v[192:195], v[32:47]\n"
"ds_read_b64_tr_b16 v[240:241], v3 offset:40960\n"
"ds_read_b64_tr_b16 v[242:243], v3 offset:41472\n"
"v_mfma_f32_32x32x16_bf16 v[32:47], v[244:247], v[196:199], v[32:47]\n"
"ds_read_b64_tr_b16 v[244:245], v3 offset:43008\n"
"ds_read_b64_tr_b16 v[246:247], v3 offset:43520\n"
"s_waitcnt lgkmcnt(8)\n"
"v_mfma_f32_32x32x16_bf16 v[32:47], v[248:251], v[200:203], v[32:47]\n"
"ds_read_b64_tr_b16 v[248:249], v3 offset:45056\n"
"ds_read_b64_tr_b16 v[250:251], v3 offset:45568\n"
"buffer_store_dwordx4 v[80:83], v10, s[44:47], s84 offen sc1\n"
"v_mfma_f32_32x32x16_bf16 v[32:47], v[252:255], v[204:207], v[32:47]\n"
"ds_read_b64_tr_b16 v[252:253], v3 offset:47104\n"
"ds_read_b64_tr_b16 v[254:255], v3 offset:47616\n"
"s_waitcnt lgkmcnt(8)\n"
"v_mfma_f32_32x32x16_bf16 v[32:47], v[232:235], v[208:211], v[32:47]\n"
"s_waitcnt vmcnt(11)\n"
"s_barrier\n"
"v_mfma_f32_32x32x16_bf16 v[32:47], v[236:239], v[212:215], v[32:47]\n"
"ds_read_b64_tr_b16 v[232:233], v1 offset:0\n"
"ds_read_b64_tr_b16 v[234:235], v1 offset:512\n"
"ds_read_b64_tr_b16 v[236:237], v1 offset:2048\n"
"ds_read_b64_tr_b16 v[238:239], v1 offset:2560\n"
"s_add_u32 s70, s82, 1769600\n"
"s_add_u32 m0, s91, 98304\n"
"buffer_store_dwordx4 v[84:87], v10, s[44:47], s84 offen offset:1024 sc1\n"
"s_waitcnt lgkmcnt(8)\n"
"v_mfma_f32_32x32x16_bf16 v[32:47], v[240:243], v[216:219], v[32:47]\n"
"ds_read_b64_tr_b16 v[240:241], v1 offset:4096\n"
"ds_read_b64_tr_b16 v[242:243], v1 offset:4608\n"
"buffer_load_dwordx4 v4, s[52:55], s70 offen lds\n"
"s_add_u32 m0, m0, 0x2000\n"
"v_mfma_f32_32x32x16_bf16 v[32:47], v[244:247], v[220:223], v[32:47]\n"
"ds_read_b64_tr_b16 v[244:245], v1 offset:6144\n"
"ds_read_b64_tr_b16 v[246:247], v1 offset:6656\n"
"buffer_load_dwordx4 v5, s[52:55], s70 offen lds\n"
"s_add_u32 m0, m0, 0x2000\n"
"s_waitcnt lgkmcnt(8)\n"
"v_mfma_f32_32x32x16_bf16 v[32:47], v[248:251], v[224:227], v[32:47]\n"
"ds_read_b64_tr_b16 v[248:249], v1 offset:8192\n"
"ds_read_b64_tr_b16 v[250:251], v1 offset:8704\n"
"buffer_load_dwordx4 v6, s[52:55], s70 offen lds\n"
"v_mfma_f32_32x32x16_bf16 v[32:47], v[252:255], v[228:231], v[32:47]\n"
"ds_read_b64_tr_b16 v[252:253], v1 offset:10240\n"
"ds_read_b64_tr_b16 v[254:255], v1 offset:10752\n"
".Lkc1_U0_9_%=:\n"
"s_waitcnt lgkmcnt(8)\n"
"v_mfma_f32_32x32x16_bf16 v[48:63], v[232:235], v[88:91], 0\n"
"ds_read_b64_tr_b16 v[232:233], v1 offset:12288\n"
"ds_read_b64_tr_b16 v[234:235], v1 offset:12800\n"
"s_add_u32 s72, s83, 1\n"
"s_cmp_lt_u32 s72, s81\n"
"s_cselect_b32 s58, 5308416, 0\n"
"v_mfma_f32_32x32x16_bf16 v[48:63], v[236:239], v[92:95], v[48:63]\n"
"ds_read_b64_tr_b16 v[236:237], v1 offset:14336\n"
"ds_read_b64_tr_b16 v[238:239], v1 offset:14848\n"
"s_waitcnt lgkmcnt(8)\n"
"v_mfma_f32_32x32x16_bf16 v[48:63], v[240:243], v[96:99], v[48:63]\n"
"ds_read_b64_tr_b16 v[240:241], v1 offset:16384\n"
"ds_read_b64_tr_b16 v[242:243], v1 offset:16896\n"
"v_mfma_f32_32x32x16_bf16 v[48:63], v[244:247], v[100:103], v[48:63]\n"
"ds_read_b64_tr_b16 v[244:245], v1 offset:18432\n"
"ds_read_b64_tr_b16 v[246:247], v1 offset:18944\n"
"s_waitcnt lgkmcnt(8)\n"
"v_mfma_f32_32x32x16_bf16 v[48:63], v[248:251], v[104:107], v[48:63]\n"
"ds_read_b64_tr_b16 v[248:249], v1 offset:20480\n"
"ds_read_b64_tr_b16 v[250:251], v1 offset:20992\n"
"v_mul_f32 v32, 0xbfb8aa3b, v32\n"
"v_mul_f32 v33, 0xbfb8aa3b, v33\n"
"v_mul_f32 v34, 0xbfb8aa3b, v34\n"
"v_mfma_f32_32x32x16_bf16 v[48:63], v[252:255], v[108:111], v[48:63]\n"
"ds_read_b64_tr_b16 v[252:253], v1 offset:22528\n"
"ds_read_b64_tr_b16 v[254:255], v1 offset:23040\n"
"v_mul_f32 v35, 0xbfb8aa3b, v35\n"
"v_mul_f32 v36, 0xbfb8aa3b, v36\n"
"s_waitcnt lgkmcnt(8)\n"
"v_mfma_f32_32x32x16_bf16 v[48:63], v[232:235], v[112:115], v[48:63]\n"
"s_waitcnt vmcnt(11)\n"
"s_barrier\n"
"v_mul_f32 v37, 0xbfb8aa3b, v37\n"
"v_mul_f32 v38, 0xbfb8aa3b, v38\n"
"v_mfma_f32_32x32x16_bf16 v[48:63], v[236:239], v[116:119], v[48:63]\n"
"ds_read_b64_tr_b16 v[232:233], v1 offset:24576\n"
"ds_read_b64_tr_b16 v[234:235], v1 offset:25088\n"
"ds_read_b64_tr_b16 v[236:237], v1 offset:26624\n"
"ds_read_b64_tr_b16 v[238:239], v1 offset:27136\n"
"s_add_u32 s70, s82, 3539072\n"
"s_add_u32 m0, s91, 122880\n"
"v_mul_f32 v39, 0xbfb8aa3b, v39\n"
"v_mul_f32 v40, 0xbfb8aa3b, v40\n"
"s_waitcnt lgkmcnt(8)\n"
"v_mfma_f32_32x32x16_bf16 v[48:63], v[240:243], v[120:123], v[48:63]\n"
"ds_read_b64_tr_b16 v[240:241], v1 offset:28672\n"
"ds_read_b64_tr_b16 v[242:243], v1 offset:29184\n"
"buffer_load_dwordx4 v4, s[52:55], s70 offen lds\n"
"s_add_u32 m0, m0, 0x2000\n"
"v_mul_f32 v41, 0xbfb8aa3b, v41\n"
"v_mul_f32 v42, 0xbfb8aa3b, v42\n"
"v_mfma_f32_32x32x16_bf16 v[48:63], v[244:247], v[124:127], v[48:63]\n"
"ds_read_b64_tr_b16 v[244:245], v1 offset:30720\n"
"ds_read_b64_tr_b16 v[246:247], v1 offset:31232\n"
"buffer_load_dwordx4 v5, s[52:55], s70 offen lds\n"
"s_add_u32 m0, m0, 0x2000\n"
"v_mul_f32 v43, 0xbfb8aa3b, v43\n"
"v_mul_f32 v44, 0xbfb8aa3b, v44\n"
"s_waitcnt lgkmcnt(8)\n"
"v_mfma_f32_32x32x16_bf16 v[48:63], v[248:251], v[128:131], v[48:63]\n"
"ds_read_b64_tr_b16 v[248:249], v1 offset:32768\n"
"ds_read_b64_tr_b16 v[250:251], v1 offset:33280\n"
"buffer_load_dwordx4 v6, s[52:55], s70 offen lds\n"
"v_mul_f32 v45, 0xbfb8aa3b, v45\n"
"v_mul_f32 v46, 0xbfb8aa3b, v46\n"
"v_mfma_f32_32x32x16_bf16 v[48:63], v[252:255], v[132:135], v[48:63]\n"
"ds_read_b64_tr_b16 v[252:253], v1 offset:34816\n"
"ds_read_b64_tr_b16 v[254:255], v1 offset:35328\n"
"s_add_u32 s71, s82, 256\n"
"buffer_load_dword v29, v11, s[52:55], s71 offen\n"
"buffer_load_dword v29, v12, s[60:63], s92 offen\n"
"s_add_u32 s92, s92, 384\n"
"v_mul_f32 v47, 0xbfb8aa3b, v47\n"
"v_exp_f32 v32, v32\n"
"s_waitcnt lgkmcnt(8)\n"
"v_mfma_f32_32x32x16_bf16 v[48:63], v[232:235], v[136:139], v[48:63]\n"
"ds_read_b64_tr_b16 v[232:233], v1 offset:36864\n"
"ds_read_b64_tr_b16 v[234:235], v1 offset:37376\n"
"v_exp_f32 v33, v33\n"
"v_exp_f32 v34, v34\n"
"v_mfma_f32_32x32x16_bf16 v[48:63], v[236:239], v[140:143], v[48:63]\n"
"ds_read_b64_tr_b16 v[236:237], v1 offset:38912\n"
"ds_read_b64_tr_b16 v[238:239], v1 offset:39424\n"
"v_exp_f32 v35, v35\n"
"v_exp_f32 v36, v36\n"
"s_waitcnt lgkmcnt(8)\n"
"v_mfma_f32_32x32x16_bf16 v[48:63], v[240:243], v[144:147], v[48:63]\n"
"ds_read_b64_tr_b16 v[240:241], v1 offset:40960\n"
"ds_read_b64_tr_b16 v[242:243], v1 offset:41472\n"
"v_exp_f32 v37, v37\n"
"v_exp_f32 v38, v38\n"
"v_mfma_f32_32x32x16_bf16 v[48:63], v[244:247], v[148:151], v[48:63]\n"
"ds_read_b64_tr_b16 v[244:245], v1 offset:43008\n"
"ds_read_b64_tr_b16 v[246:247], v1 offset:43520\n"
"v_exp_f32 v39, v39\n"
"v_exp_f32 v40, v40\n"
"s_waitcnt lgkmcnt(8)\n"
"v_mfma_f32_32x32x16_bf16 v[48:63], v[248:251], v[152:155], v[48:63]\n"
"ds_read_b64_tr_b16 v[248:249], v1 offset:45056\n"
"ds_read_b64_tr_b16 v[250:251], v1 offset:45568\n"
"v_exp_f32 v41, v41\n"
"v_exp_f32 v42, v42\n"
"v_mfma_f32_32x32x16_bf16 v[48:63], v[252:255], v[156:159], v[48:63]\n"
"ds_read_b64_tr_b16 v[252:253], v1 offset:47104\n"
"ds_read_b64_tr_b16 v[254:255], v1 offset:47616\n"
"v_exp_f32 v43, v43\n"
"v_exp_f32 v44, v44\n"
"s_waitcnt lgkmcnt(8)\n"
"v_mfma_f32_32x32x16_bf16 v[48:63], v[232:235], v[160:163], v[48:63]\n"
"s_waitcnt vmcnt(11)\n"
"s_barrier\n"
"v_exp_f32 v45, v45\n"
"v_exp_f32 v46, v46\n"
"v_mfma_f32_32x32x16_bf16 v[48:63], v[236:239], v[164:167], v[48:63]\n"
"ds_read_b64_tr_b16 v[232:233], v2 offset:0\n"
"ds_read_b64_tr_b16 v[234:235], v2 offset:512\n"
"ds_read_b64_tr_b16 v[236:237], v2 offset:2048\n"
"ds_read_b64_tr_b16 v[238:239], v2 offset:2560\n"
"s_add_u32 s70, s82, 128\n"
"s_add_u32 m0, s91, 0\n"
"v_exp_f32 v47, v47\n"
"v_add_f32 v32, 1.0, v32\n"
"v_add_f32 v33, 1.0, v33\n"
"s_waitcnt lgkmcnt(8)\n"
"v_mfma_f32_32x32x16_bf16 v[48:63], v[240:243], v[168:171], v[48:63]\n"
"ds_read_b64_tr_b16 v[240:241], v2 offset:4096\n"
"ds_read_b64_tr_b16 v[242:243], v2 offset:4608\n"
"buffer_load_dwordx4 v4, s[56:59], s70 offen lds\n"
"s_add_u32 m0, m0, 0x2000\n"
"v_add_f32 v34, 1.0, v34\n"
"v_add_f32 v35, 1.0, v35\n"
"v_mfma_f32_32x32x16_bf16 v[48:63], v[244:247], v[172:175], v[48:63]\n"
"ds_read_b64_tr_b16 v[244:245], v2 offset:6144\n"
"ds_read_b64_tr_b16 v[246:247], v2 offset:6656\n"
"buffer_load_dwordx4 v5, s[56:59], s70 offen lds\n"
"s_add_u32 m0, m0, 0x2000\n"
"v_add_f32 v36, 1.0, v36\n"
"v_add_f32 v37, 1.0, v37\n"
"s_waitcnt lgkmcnt(8)\n"
"v_mfma_f32_32x32x16_bf16 v[48:63], v[248:251], v[176:179], v[48:63]\n"
"ds_read_b64_tr_b16 v[248:249], v2 offset:8192\n"
"ds_read_b64_tr_b16 v[250:251], v2 offset:8704\n"
"buffer_load_dwordx4 v6, s[56:59], s70 offen lds\n"
"v_add_f32 v38, 1.0, v38\n"
"v_add_f32 v39, 1.0, v39\n"
"v_mfma_f32_32x32x16_bf16 v[48:63], v[252:255], v[180:183], v[48:63]\n"
"ds_read_b64_tr_b16 v[252:253], v2 offset:10240\n"
"ds_read_b64_tr_b16 v[254:255], v2 offset:10752\n"
"v_add_f32 v40, 1.0, v40\n"
"v_add_f32 v41, 1.0, v41\n"
"s_waitcnt lgkmcnt(8)\n"
"v_mfma_f32_32x32x16_bf16 v[48:63], v[232:235], v[184:187], v[48:63]\n"
"ds_read_b64_tr_b16 v[232:233], v2 offset:12288\n"
"ds_read_b64_tr_b16 v[234:235], v2 offset:12800\n"
"v_add_f32 v42, 1.0, v42\n"
"v_add_f32 v43, 1.0, v43\n"
"v_mfma_f32_32x32x16_bf16 v[48:63], v[236:239], v[188:191], v[48:63]\n"
"ds_read_b64_tr_b16 v[236:237], v2 offset:14336\n"
"ds_read_b64_tr_b16 v[238:239], v2 offset:14848\n"
"v_add_f32 v44, 1.0, v44\n"
"v_add_f32 v45, 1.0, v45\n"
"s_waitcnt lgkmcnt(8)\n"
"v_mfma_f32_32x32x16_bf16 v[48:63], v[240:243], v[192:195], v[48:63]\n"
"ds_read_b64_tr_b16 v[240:241], v2 offset:16384\n"
"ds_read_b64_tr_b16 v[242:243], v2 offset:16896\n"
"v_add_f32 v46, 1.0, v46\n"
"v_add_f32 v47, 1.0, v47\n"
"v_mfma_f32_32x32x16_bf16 v[48:63], v[244:247], v[196:199], v[48:63]\n"
"ds_read_b64_tr_b16 v[244:245], v2 offset:18432\n"
"ds_read_b64_tr_b16 v[246:247], v2 offset:18944\n"
"v_rcp_f32 v64, v32\n"
"v_rcp_f32 v65, v33\n"
"s_waitcnt lgkmcnt(8)\n"
"v_mfma_f32_32x32x16_bf16 v[48:63], v[248:251], v[200:203], v[48:63]\n"
"ds_read_b64_tr_b16 v[248:249], v2 offset:20480\n"
"ds_read_b64_tr_b16 v[250:251], v2 offset:20992\n"
"v_rcp_f32 v66, v34\n"
"v_rcp_f32 v67, v35\n"
"v_mfma_f32_32x32x16_bf16 v[48:63], v[252:255], v[204:207], v[48:63]\n"
"ds_read_b64_tr_b16 v[252:253], v2 offset:22528\n"
"ds_read_b64_tr_b16 v[254:255], v2 offset:23040\n"
"v_rcp_f32 v68, v36\n"
"v_rcp_f32 v69, v37\n"
"s_waitcnt lgkmcnt(8)\n"
"v_mfma_f32_32x32x16_bf16 v[48:63], v[232:235], v[208:211], v[48:63]\n"
"s_waitcnt vmcnt(11)\n"
"s_barrier\n"
"v_rcp_f32 v70, v38\n"
"v_rcp_f32 v71, v39\n"
"v_mfma_f32_32x32x16_bf16 v[48:63], v[236:239], v[212:215], v[48:63]\n"
"ds_read_b64_tr_b16 v[232:233], v2 offset:24576\n"
"ds_read_b64_tr_b16 v[234:235], v2 offset:25088\n"
"ds_read_b64_tr_b16 v[236:237], v2 offset:26624\n"
"ds_read_b64_tr_b16 v[238:239], v2 offset:27136\n"
"s_add_u32 s70, s82, 1769600\n"
"s_add_u32 m0, s91, 24576\n"
"v_rcp_f32 v72, v40\n"
"v_rcp_f32 v73, v41\n"
"s_waitcnt lgkmcnt(8)\n"
"v_mfma_f32_32x32x16_bf16 v[48:63], v[240:243], v[216:219], v[48:63]\n"
"ds_read_b64_tr_b16 v[240:241], v2 offset:28672\n"
"ds_read_b64_tr_b16 v[242:243], v2 offset:29184\n"
"buffer_load_dwordx4 v4, s[56:59], s70 offen lds\n"
"s_add_u32 m0, m0, 0x2000\n"
"v_rcp_f32 v74, v42\n"
"v_rcp_f32 v75, v43\n"
"v_mfma_f32_32x32x16_bf16 v[48:63], v[244:247], v[220:223], v[48:63]\n"
"ds_read_b64_tr_b16 v[244:245], v2 offset:30720\n"
"ds_read_b64_tr_b16 v[246:247], v2 offset:31232\n"
"buffer_load_dwordx4 v5, s[56:59], s70 offen lds\n"
"s_add_u32 m0, m0, 0x2000\n"
"v_rcp_f32 v76, v44\n"
"v_rcp_f32 v77, v45\n"
"s_waitcnt lgkmcnt(8)\n"
"v_mfma_f32_32x32x16_bf16 v[48:63], v[248:251], v[224:227], v[48:63]\n"
"ds_read_b64_tr_b16 v[248:249], v2 offset:32768\n"
"ds_read_b64_tr_b16 v[250:251], v2 offset:33280\n"
"buffer_load_dwordx4 v6, s[56:59], s70 offen lds\n"
"v_rcp_f32 v78, v46\n"
"v_rcp_f32 v79, v47\n"
"v_mfma_f32_32x32x16_bf16 v[48:63], v[252:255], v[228:231], v[48:63]\n"
"ds_read_b64_tr_b16 v[252:253], v2 offset:34816\n"
"ds_read_b64_tr_b16 v[254:255], v2 offset:35328\n"
"s_add_u32 s83, s83, 1\n"
"s_add_u32 s82, s82, 128\n"
"s_cmp_lt_u32 s83, s81\n"
"s_cbranch_scc0 .Lkc1_END_13_%=\n"
".Lkc1_GH3_10_%=:\n"
"s_waitcnt lgkmcnt(8)\n"
"v_mfma_f32_32x32x16_bf16 v[32:47], v[232:235], v[88:91], 0\n"
"ds_read_b64_tr_b16 v[232:233], v2 offset:36864\n"
"ds_read_b64_tr_b16 v[234:235], v2 offset:37376\n"
"s_lshl_b32 s72, s83, 12\n"
"s_add_u32 s72, s72, s90\n"
"s_sub_u32 s72, s72, 4096\n"
"s_cmp_eq_u32 s83, 0\n"
"s_cselect_b32 s84, 1073741824, s72\n"
"v_mfma_f32_32x32x16_bf16 v[32:47], v[236:239], v[92:95], v[32:47]\n"
"ds_read_b64_tr_b16 v[236:237], v2 offset:38912\n"
"ds_read_b64_tr_b16 v[238:239], v2 offset:39424\n"
"s_add_u32 s72, s83, 1\n"
"s_cmp_lt_u32 s72, s81\n"
"s_cselect_b32 s54, 5308416, 0\n"
"s_waitcnt lgkmcnt(8)\n"
"v_mfma_f32_32x32x16_bf16 v[32:47], v[240:243], v[96:99], v[32:47]\n"
"ds_read_b64_tr_b16 v[240:241], v2 offset:40960\n"
"ds_read_b64_tr_b16 v[242:243], v2 offset:41472\n"
"v_mfma_f32_32x32x16_bf16 v[32:47], v[244:247], v[100:103], v[32:47]\n"
"ds_read_b64_tr_b16 v[244:245], v2 offset:43008\n"
"ds_read_b64_tr_b16 v[246:247], v2 offset:43520\n"
"s_waitcnt lgkmcnt(8)\n"
"v_mfma_f32_32x32x16_bf16 v[32:47], v[248:251], v[104:107], v[32:47]\n"
"ds_read_b64_tr_b16 v[248:249], v2 offset:45056\n"
"ds_read_b64_tr_b16 v[250:251], v2 offset:45568\n"
"v_mul_f32 v48, v48, v64\n"
"v_mfma_f32_32x32x16_bf16 v[32:47], v[252:255], v[108:111], v[32:47]\n"
"ds_read_b64_tr_b16 v[252:253], v2 offset:47104\n"
"ds_read_b64_tr_b16 v[254:255], v2 offset:47616\n"
"v_mul_f32 v49, v49, v65\n"
"s_waitcnt lgkmcnt(8)\n"
"v_mfma_f32_32x32x16_bf16 v[32:47], v[232:235], v[112:115], v[32:47]\n"
"s_waitcnt vmcnt(11)\n"
"s_barrier\n"
"v_mul_f32 v50, v50, v66\n"
"v_mfma_f32_32x32x16_bf16 v[32:47], v[236:239], v[116:119], v[32:47]\n"
"ds_read_b64_tr_b16 v[232:233], v3 offset:0\n"
"ds_read_b64_tr_b16 v[234:235], v3 offset:512\n"
"ds_read_b64_tr_b16 v[236:237], v3 offset:2048\n"
"ds_read_b64_tr_b16 v[238:239], v3 offset:2560\n"
"s_add_u32 s70, s82, 3538944\n"
"s_add_u32 m0, s91, 49152\n"
"v_mul_f32 v51, v51, v67\n"
"s_waitcnt lgkmcnt(8)\n"
"v_mfma_f32_32x32x16_bf16 v[32:47], v[240:243], v[120:123], v[32:47]\n"
"ds_read_b64_tr_b16 v[240:241], v3 offset:4096\n"
"ds_read_b64_tr_b16 v[242:243], v3 offset:4608\n"
"buffer_load_dwordx4 v4, s[56:59], s70 offen lds\n"
"s_add_u32 m0, m0, 0x2000\n"
"v_mul_f32 v52, v52, v68\n"
"v_mfma_f32_32x32x16_bf16 v[32:47], v[244:247], v[124:127], v[32:47]\n"
"ds_read_b64_tr_b16 v[244:245], v3 offset:6144\n"
"ds_read_b64_tr_b16 v[246:247], v3 offset:6656\n"
"buffer_load_dwordx4 v5, s[56:59], s70 offen lds\n"
"s_add_u32 m0, m0, 0x2000\n"
"v_mul_f32 v53, v53, v69\n"
"s_waitcnt lgkmcnt(8)\n"
"v_mfma_f32_32x32x16_bf16 v[32:47], v[248:251], v[128:131], v[32:47]\n"
"ds_read_b64_tr_b16 v[248:249], v3 offset:8192\n"
"ds_read_b64_tr_b16 v[250:251], v3 offset:8704\n"
"buffer_load_dwordx4 v6, s[56:59], s70 offen lds\n"
"v_mfma_f32_32x32x16_bf16 v[32:47], v[252:255], v[132:135], v[32:47]\n"
"ds_read_b64_tr_b16 v[252:253], v3 offset:10240\n"
"ds_read_b64_tr_b16 v[254:255], v3 offset:10752\n"
"s_add_u32 s71, s82, 128\n"
"buffer_load_dword v29, v11, s[56:59], s71 offen\n"
"buffer_load_dword v29, v12, s[60:63], s92 offen\n"
"s_add_u32 s92, s92, 384\n"
"v_mul_f32 v54, v54, v70\n"
"s_waitcnt lgkmcnt(8)\n"
"v_mfma_f32_32x32x16_bf16 v[32:47], v[232:235], v[136:139], v[32:47]\n"
"ds_read_b64_tr_b16 v[232:233], v3 offset:12288\n"
"ds_read_b64_tr_b16 v[234:235], v3 offset:12800\n"
"v_mul_f32 v55, v55, v71\n"
"v_mfma_f32_32x32x16_bf16 v[32:47], v[236:239], v[140:143], v[32:47]\n"
"ds_read_b64_tr_b16 v[236:237], v3 offset:14336\n"
"ds_read_b64_tr_b16 v[238:239], v3 offset:14848\n"
"v_mul_f32 v56, v56, v72\n"
"s_waitcnt lgkmcnt(8)\n"
"v_mfma_f32_32x32x16_bf16 v[32:47], v[240:243], v[144:147], v[32:47]\n"
"ds_read_b64_tr_b16 v[240:241], v3 offset:16384\n"
"ds_read_b64_tr_b16 v[242:243], v3 offset:16896\n"
"v_mul_f32 v57, v57, v73\n"
"v_mfma_f32_32x32x16_bf16 v[32:47], v[244:247], v[148:151], v[32:47]\n"
"ds_read_b64_tr_b16 v[244:245], v3 offset:18432\n"
"ds_read_b64_tr_b16 v[246:247], v3 offset:18944\n"
"v_mul_f32 v58, v58, v74\n"
"s_waitcnt lgkmcnt(8)\n"
"v_mfma_f32_32x32x16_bf16 v[32:47], v[248:251], v[152:155], v[32:47]\n"
"ds_read_b64_tr_b16 v[248:249], v3 offset:20480\n"
"ds_read_b64_tr_b16 v[250:251], v3 offset:20992\n"
"v_mfma_f32_32x32x16_bf16 v[32:47], v[252:255], v[156:159], v[32:47]\n"
"ds_read_b64_tr_b16 v[252:253], v3 offset:22528\n"
"ds_read_b64_tr_b16 v[254:255], v3 offset:23040\n"
"v_mul_f32 v59, v59, v75\n"
"s_waitcnt lgkmcnt(8)\n"
"v_mfma_f32_32x32x16_bf16 v[32:47], v[232:235], v[160:163], v[32:47]\n"
"s_waitcnt vmcnt(11)\n"
"s_barrier\n"
"v_mul_f32 v60, v60, v76\n"
"v_mfma_f32_32x32x16_bf16 v[32:47], v[236:239], v[164:167], v[32:47]\n"
"ds_read_b64_tr_b16 v[232:233], v3 offset:24576\n"
"ds_read_b64_tr_b16 v[234:235], v3 offset:25088\n"
"ds_read_b64_tr_b16 v[236:237], v3 offset:26624\n"
"ds_read_b64_tr_b16 v[238:239], v3 offset:27136\n"
"s_add_u32 s70, s82, 128\n"
"s_add_u32 m0, s91, 73728\n"
"v_mul_f32 v61, v61, v77\n"
"s_waitcnt lgkmcnt(8)\n"
"v_mfma_f32_32x32x16_bf16 v[32:47], v[240:243], v[168:171], v[32:47]\n"
"ds_read_b64_tr_b16 v[240:241], v3 offset:28672\n"
"ds_read_b64_tr_b16 v[242:243], v3 offset:29184\n"
"buffer_load_dwordx4 v4, s[52:55], s70 offen lds\n"
"s_add_u32 m0, m0, 0x2000\n"
"v_mul_f32 v62, v62, v78\n"
"v_mfma_f32_32x32x16_bf16 v[32:47], v[244:247], v[172:175], v[32:47]\n"
"ds_read_b64_tr_b16 v[244:245], v3 offset:30720\n"
"ds_read_b64_tr_b16 v[246:247], v3 offset:31232\n"
"buffer_load_dwordx4 v5, s[52:55], s70 offen lds\n"
"s_add_u32 m0, m0, 0x2000\n"
"v_mul_f32 v63, v63, v79\n"
"s_waitcnt lgkmcnt(8)\n"
"v_mfma_f32_32x32x16_bf16 v[32:47], v[248:251], v[176:179], v[32:47]\n"
"ds_read_b64_tr_b16 v[248:249], v3 offset:32768\n"
"ds_read_b64_tr_b16 v[250:251], v3 offset:33280\n"
"buffer_load_dwordx4 v6, s[52:55], s70 offen lds\n"
"v_mfma_f32_32x32x16_bf16 v[32:47], v[252:255], v[180:183], v[32:47]\n"
"ds_read_b64_tr_b16 v[252:253], v3 offset:34816\n"
"ds_read_b64_tr_b16 v[254:255], v3 offset:35328\n"
"v_cvt_pk_bf16_f32 v80, v48, v49\n"
"s_waitcnt lgkmcnt(8)\n"
"v_mfma_f32_32x32x16_bf16 v[32:47], v[232:235], v[184:187], v[32:47]\n"
"ds_read_b64_tr_b16 v[232:233], v3 offset:36864\n"
"ds_read_b64_tr_b16 v[234:235], v3 offset:37376\n"
"v_cvt_pk_bf16_f32 v81, v50, v51\n"
"v_mfma_f32_32x32x16_bf16 v[32:47], v[236:239], v[188:191], v[32:47]\n"
"ds_read_b64_tr_b16 v[236:237], v3 offset:38912\n"
"ds_read_b64_tr_b16 v[238:239], v3 offset:39424\n"
"v_cvt_pk_bf16_f32 v82, v52, v53\n"
"s_waitcnt lgkmcnt(8)\n"
"v_mfma_f32_32x32x16_bf16 v[32:47], v[240:243], v[192:195], v[32:47]\n"
"ds_read_b64_tr_b16 v[240:241], v3 offset:40960\n"
"ds_read_b64_tr_b16 v[242:243], v3 offset:41472\n"
"v_cvt_pk_bf16_f32 v83, v54, v55\n"
"v_mfma_f32_32x32x16_bf16 v[32:47], v[244:247], v[196:199], v[32:47]\n"
"ds_read_b64_tr_b16 v[244:245], v3 offset:43008\n"
"ds_read_b64_tr_b16 v[246:247], v3 offset:43520\n"
"v_cvt_pk_bf16_f32 v84, v56, v57\n"
"s_waitcnt lgkmcnt(8)\n"
"v_mfma_f32_32x32x16_bf16 v[32:47], v[248:251], v[200:203], v[32:47]\n"
"ds_read_b64_tr_b16 v[248:249], v3 offset:45056\n"
"ds_read_b64_tr_b16 v[250:251], v3 offset:45568\n"
"v_mfma_f32_32x32x16_bf16 v[32:47], v[252:255], v[204:207], v[32:47]\n"
"ds_read_b64_tr_b16 v[252:253], v3 offset:47104\n"
"ds_read_b64_tr_b16 v[254:255], v3 offset:47616\n"
"v_cvt_pk_bf16_f32 v85, v58, v59\n"
"s_waitcnt lgkmcnt(8)\n"
"v_mfma_f32_32x32x16_bf16 v[32:47], v[232:235], v[208:211], v[32:47]\n"
"s_waitcnt vmcnt(11)\n"
"s_barrier\n"
"v_cvt_pk_bf16_f32 v86, v60, v61\n"
"v_mfma_f32_32x32x16_bf16 v[32:47], v[236:239], v[212:215], v[32:47]\n"
"ds_read_b64_tr_b16 v[232:233], v1 offset:0\n"
"ds_read_b64_tr_b16 v[234:235], v1 offset:512\n"
"ds_read_b64_tr_b16 v[236:237], v1 offset:2048\n"
"ds_read_b64_tr_b16 v[238:239], v1 offset:2560\n"
"s_add_u32 s70, s82, 1769600\n"
"s_add_u32 m0, s91, 98304\n"
"v_cvt_pk_bf16_f32 v87, v62, v63\n"
"s_waitcnt lgkmcnt(8)\n"
"v_mfma_f32_32x32x16_bf16 v[32:47], v[240:243], v[216:219], v[32:47]\n"
"ds_read_b64_tr_b16 v[240:241], v1 offset:4096\n"
"ds_read_b64_tr_b16 v[242:243], v1 offset:4608\n"
"buffer_load_dwordx4 v4, s[52:55], s70 offen lds\n"
"s_add_u32 m0, m0, 0x2000\n"
"buffer_store_dwordx4 v[80:83], v10, s[44:47], s84 offen sc1\n"
"v_mfma_f32_32x32x16_bf16 v[32:47], v[244:247], v[220:223], v[32:47]\n"
"ds_read_b64_tr_b16 v[244:245], v1 offset:6144\n"
"ds_read_b64_tr_b16 v[246:247], v1 offset:6656\n"
"buffer_load_dwordx4 v5, s[52:55], s70 offen lds\n"
"s_add_u32 m0, m0, 0x2000\n"
"buffer_store_dwordx4 v[84:87], v10, s[44:47], s84 offen offset:1024 sc1\n"
"s_waitcnt lgkmcnt(8)\n"
"v_mfma_f32_32x32x16_bf16 v[32:47], v[248:251], v[224:227], v[32:47]\n"
"ds_read_b64_tr_b16 v[248:249], v1 offset:8192\n"
"ds_read_b64_tr_b16 v[250:251], v1 offset:8704\n"
"buffer_load_dwordx4 v6, s[52:55], s70 offen lds\n"
"v_mfma_f32_32x32x16_bf16 v[32:47], v[252:255], v[228:231], v[32:47]\n"
"ds_read_b64_tr_b16 v[252:253], v1 offset:10240\n"
"ds_read_b64_tr_b16 v[254:255], v1 offset:10752\n"
"s_branch .Lkc1_U0_9_%=\n"
".Lkc1_GH0_11_%=:\n"
"s_waitcnt lgkmcnt(8)\n"
"v_mfma_f32_32x32x16_bf16 v[32:47], v[232:235], v[88:91], 0\n"
"ds_read_b64_tr_b16 v[232:233], v1 offset:12288\n"
"ds_read_b64_tr_b16 v[234:235], v1 offset:12800\n"
"s_lshl_b32 s72, s83, 12\n"
"s_add_u32 s72, s72, s90\n"
"s_sub_u32 s72, s72, 4096\n"
"s_cmp_eq_u32 s83, 0\n"
"s_cselect_b32 s84, 1073741824, s72\n"
"v_mfma_f32_32x32x16_bf16 v[32:47], v[236:239], v[92:95], v[32:47]\n"
"ds_read_b64_tr_b16 v[236:237], v1 offset:14336\n"
"ds_read_b64_tr_b16 v[238:239], v1 offset:14848\n"
"s_add_u32 s72, s83, 1\n"
"s_cmp_lt_u32 s72, s81\n"
"s_cselect_b32 s54, 5308416, 0\n"
"s_waitcnt lgkmcnt(8)\n"
"v_mfma_f32_32x32x16_bf16 v[32:47], v[240:243], v[96:99], v[32:47]\n"
"ds_read_b64_tr_b16 v[240:241], v1 offset:16384\n"
"ds_read_b64_tr_b16 v[242:243], v1 offset:16896\n"
"v_mfma_f32_32x32x16_bf16 v[32:47], v[244:247], v[100:103], v[32:47]\n"
"ds_read_b64_tr_b16 v[244:245], v1 offset:18432\n"
"ds_read_b64_tr_b16 v[246:247], v1 offset:18944\n"
"s_waitcnt lgkmcnt(8)\n"
"v_mfma_f32_32x32x16_bf16 v[32:47], v[248:251], v[104:107], v[32:47]\n"
"ds_read_b64_tr_b16 v[248:249], v1 offset:20480\n"
"ds_read_b64_tr_b16 v[250:251], v1 offset:20992\n"
"v_mul_f32 v48, v48, v64\n"
"v_mfma_f32_32x32x16_bf16 v[32:47], v[252:255], v[108:111], v[32:47]\n"
"ds_read_b64_tr_b16 v[252:253], v1 offset:22528\n"
"ds_read_b64_tr_b16 v[254:255], v1 offset:23040\n"
"v_mul_f32 v49, v49, v65\n"
"s_waitcnt lgkmcnt(8)\n"
"v_mfma_f32_32x32x16_bf16 v[32:47], v[232:235], v[112:115], v[32:47]\n"
"s_waitcnt vmcnt(11)\n"
"s_barrier\n"
"v_mul_f32 v50, v50, v66\n"
"v_mfma_f32_32x32x16_bf16 v[32:47], v[236:239], v[116:119], v[32:47]\n"
"ds_read_b64_tr_b16 v[232:233], v1 offset:24576\n"
"ds_read_b64_tr_b16 v[234:235], v1 offset:25088\n"
"ds_read_b64_tr_b16 v[236:237], v1 offset:26624\n"
"ds_read_b64_tr_b16 v[238:239], v1 offset:27136\n"
"s_add_u32 s70, s82, 3538944\n"
"s_add_u32 m0, s91, 122880\n"
"v_mul_f32 v51, v51, v67\n"
"s_waitcnt lgkmcnt(8)\n"
"v_mfma_f32_32x32x16_bf16 v[32:47], v[240:243], v[120:123], v[32:47]\n"
"ds_read_b64_tr_b16 v[240:241], v1 offset:28672\n"
"ds_read_b64_tr_b16 v[242:243], v1 offset:29184\n"
"buffer_load_dwordx4 v4, s[56:59], s70 offen lds\n"
"s_add_u32 m0, m0, 0x2000\n"
"v_mul_f32 v52, v52, v68\n"
"v_mfma_f32_32x32x16_bf16 v[32:47], v[244:247], v[124:127], v[32:47]\n"
"ds_read_b64_tr_b16 v[244:245], v1 offset:30720\n"
"ds_read_b64_tr_b16 v[246:247], v1 offset:31232\n"
"buffer_load_dwordx4 v5, s[56:59], s70 offen lds\n"
"s_add_u32 m0, m0, 0x2000\n"
"v_mul_f32 v53, v53, v69\n"
"s_waitcnt lgkmcnt(8)\n"
"v_mfma_f32_32x32x16_bf16 v[32:47], v[248:251], v[128:131], v[32:47]\n"
"ds_read_b64_tr_b16 v[248:249], v1 offset:32768\n"
"ds_read_b64_tr_b16 v[250:251], v1 offset:33280\n"
"buffer_load_dwordx4 v6, s[56:59], s70 offen lds\n"
"v_mfma_f32_32x32x16_bf16 v[32:47], v[252:255], v[132:135], v[32:47]\n"
"ds_read_b64_tr_b16 v[252:253], v1 offset:34816\n"
"ds_read_b64_tr_b16 v[254:255], v1 offset:35328\n"
"s_add_u32 s71, s82, 128\n"
"buffer_load_dword v29, v11, s[56:59], s71 offen\n"
"buffer_load_dword v29, v12, s[60:63], s92 offen\n"
"s_add_u32 s92, s92, 384\n"
"v_mul_f32 v54, v54, v70\n"
"s_waitcnt lgkmcnt(8)\n"
"v_mfma_f32_32x32x16_bf16 v[32:47], v[232:235], v[136:139], v[32:47]\n"
"ds_read_b64_tr_b16 v[232:233], v1 offset:36864\n"
"ds_read_b64_tr_b16 v[234:235], v1 offset:37376\n"
"v_mul_f32 v55, v55, v71\n"
"v_mfma_f32_32x32x16_bf16 v[32:47], v[236:239], v[140:143], v[32:47]\n"
"ds_read_b64_tr_b16 v[236:237], v1 offset:38912\n"
"ds_read_b64_tr_b16 v[238:239], v1 offset:39424\n"
"v_mul_f32 v56, v56, v72\n"
"s_waitcnt lgkmcnt(8)\n"
"v_mfma_f32_32x32x16_bf16 v[32:47], v[240:243], v[144:147], v[32:47]\n"
"ds_read_b64_tr_b16 v[240:241], v1 offset:40960\n"
"ds_read_b64_tr_b16 v[242:243], v1 offset:41472\n"
"v_mul_f32 v57, v57, v73\n"
"v_mfma_f32_32x32x16_bf16 v[32:47], v[244:247], v[148:151], v[32:47]\n"
"ds_read_b64_tr_b16 v[244:245], v1 offset:43008\n"
"ds_read_b64_tr_b16 v[246:247], v1 offset:43520\n"
"v_mul_f32 v58, v58, v74\n"
"s_waitcnt lgkmcnt(8)\n"
"v_mfma_f32_32x32x16_bf16 v[32:47], v[248:251], v[152:155], v[32:47]\n"
"ds_read_b64_tr_b16 v[248:249], v1 offset:45056\n"
"ds_read_b64_tr_b16 v[250:251], v1 offset:45568\n"
"v_mfma_f32_32x32x16_bf16 v[32:47], v[252:255], v[156:159], v[32:47]\n"
"ds_read_b64_tr_b16 v[252:253], v1 offset:47104\n"
"ds_read_b64_tr_b16 v[254:255], v1 offset:47616\n"
"v_mul_f32 v59, v59, v75\n"
"s_waitcnt lgkmcnt(8)\n"
"v_mfma_f32_32x32x16_bf16 v[32:47], v[232:235], v[160:163], v[32:47]\n"
"s_waitcnt vmcnt(11)\n"
"s_barrier\n"
"v_mul_f32 v60, v60, v76\n"
"v_mfma_f32_32x32x16_bf16 v[32:47], v[236:239], v[164:167], v[32:47]\n"
"ds_read_b64_tr_b16 v[232:233], v2 offset:0\n"
"ds_read_b64_tr_b16 v[234:235], v2 offset:512\n"
"ds_read_b64_tr_b16 v[236:237], v2 offset:2048\n"
"ds_read_b64_tr_b16 v[238:239], v2 offset:2560\n"
"s_add_u32 s70, s82, 128\n"
"s_add_u32 m0, s91, 0\n"
"v_mul_f32 v61, v61, v77\n"
"s_waitcnt lgkmcnt(8)\n"
"v_mfma_f32_32x32x16_bf16 v[32:47], v[240:243], v[168:171], v[32:47]\n"
"ds_read_b64_tr_b16 v[240:241], v2 offset:4096\n"
"ds_read_b64_tr_b16 v[242:243], v2 offset:4608\n"
"buffer_load_dwordx4 v4, s[52:55], s70 offen lds\n"
"s_add_u32 m0, m0, 0x2000\n"
"v_mul_f32 v62, v62, v78\n"
"v_mfma_f32_32x32x16_bf16 v[32:47], v[244:247], v[172:175], v[32:47]\n"
"ds_read_b64_tr_b16 v[244:245], v2 offset:6144\n"
"ds_read_b64_tr_b16 v[246:247], v2 offset:6656\n"
"buffer_load_dwordx4 v5, s[52:55], s70 offen lds\n"
"s_add_u32 m0, m0, 0x2000\n"
"v_mul_f32 v63, v63, v79\n"
"s_waitcnt lgkmcnt(8)\n"
"v_mfma_f32_32x32x16_bf16 v[32:47], v[248:251], v[176:179], v[32:47]\n"
"ds_read_b64_tr_b16 v[248:249], v2 offset:8192\n"
"ds_read_b64_tr_b16 v[250:251], v2 offset:8704\n"
"buffer_load_dwordx4 v6, s[52:55], s70 offen lds\n"
"v_mfma_f32_32x32x16_bf16 v[32:47], v[252:255], v[180:183], v[32:47]\n"
"ds_read_b64_tr_b16 v[252:253], v2 offset:10240\n"
"ds_read_b64_tr_b16 v[254:255], v2 offset:10752\n"
"v_cvt_pk_bf16_f32 v80, v48, v49\n"
"s_waitcnt lgkmcnt(8)\n"
"v_mfma_f32_32x32x16_bf16 v[32:47], v[232:235], v[184:187], v[32:47]\n"
"ds_read_b64_tr_b16 v[232:233], v2 offset:12288\n"
"ds_read_b64_tr_b16 v[234:235], v2 offset:12800\n"
"v_cvt_pk_bf16_f32 v81, v50, v51\n"
"v_mfma_f32_32x32x16_bf16 v[32:47], v[236:239], v[188:191], v[32:47]\n"
"ds_read_b64_tr_b16 v[236:237], v2 offset:14336\n"
"ds_read_b64_tr_b16 v[238:239], v2 offset:14848\n"
"v_cvt_pk_bf16_f32 v82, v52, v53\n"
"s_waitcnt lgkmcnt(8)\n"
"v_mfma_f32_32x32x16_bf16 v[32:47], v[240:243], v[192:195], v[32:47]\n"
"ds_read_b64_tr_b16 v[240:241], v2 offset:16384\n"
"ds_read_b64_tr_b16 v[242:243], v2 offset:16896\n"
"v_cvt_pk_bf16_f32 v83, v54, v55\n"
"v_mfma_f32_32x32x16_bf16 v[32:47], v[244:247], v[196:199], v[32:47]\n"
"ds_read_b64_tr_b16 v[244:245], v2 offset:18432\n"
"ds_read_b64_tr_b16 v[246:247], v2 offset:18944\n"
"v_cvt_pk_bf16_f32 v84, v56, v57\n"
"s_waitcnt lgkmcnt(8)\n"
"v_mfma_f32_32x32x16_bf16 v[32:47], v[248:251], v[200:203], v[32:47]\n"
"ds_read_b64_tr_b16 v[248:249], v2 offset:20480\n"
"ds_read_b64_tr_b16 v[250:251], v2 offset:20992\n"
"v_mfma_f32_32x32x16_bf16 v[32:47], v[252:255], v[204:207], v[32:47]\n"
"ds_read_b64_tr_b16 v[252:253], v2 offset:22528\n"
"ds_read_b64_tr_b16 v[254:255], v2 offset:23040\n"
"v_cvt_pk_bf16_f32 v85, v58, v59\n"
"s_waitcnt lgkmcnt(8)\n"
"v_mfma_f32_32x32x16_bf16 v[32:47], v[232:235], v[208:211], v[32:47]\n"
"s_waitcnt vmcnt(11)\n"
"s_barrier\n"
"v_cvt_pk_bf16_f32 v86, v60, v61\n"
"v_mfma_f32_32x32x16_bf16 v[32:47], v[236:239], v[212:215], v[32:47]\n"
"ds_read_b64_tr_b16 v[232:233], v2 offset:24576\n"
"ds_read_b64_tr_b16 v[234:235], v2 offset:25088\n"
"ds_read_b64_tr_b16 v[236:237], v2 offset:26624\n"
"ds_read_b64_tr_b16 v[238:239], v2 offset:27136\n"
"s_add_u32 s70, s82, 1769600\n"
"s_add_u32 m0, s91, 24576\n"
"v_cvt_pk_bf16_f32 v87, v62, v63\n"
"s_waitcnt lgkmcnt(8)\n"
"v_mfma_f32_32x32x16_bf16 v[32:47], v[240:243], v[216:219], v[32:47]\n"
"ds_read_b64_tr_b16 v[240:241], v2 offset:28672\n"
"ds_read_b64_tr_b16 v[242:243], v2 offset:29184\n"
"buffer_load_dwordx4 v4, s[52:55], s70 offen lds\n"
"s_add_u32 m0, m0, 0x2000\n"
"buffer_store_dwordx4 v[80:83], v10, s[44:47], s84 offen sc1\n"
"v_mfma_f32_32x32x16_bf16 v[32:47], v[244:247], v[220:223], v[32:47]\n"
"ds_read_b64_tr_b16 v[244:245], v2 offset:30720\n"
"ds_read_b64_tr_b16 v[246:247], v2 offset:31232\n"
"buffer_load_dwordx4 v5, s[52:55], s70 offen lds\n"
"s_add_u32 m0, m0, 0x2000\n"
"buffer_store_dwordx4 v[84:87], v10, s[44:47], s84 offen offset:1024 sc1\n"
"s_waitcnt lgkmcnt(8)\n"
"v_mfma_f32_32x32x16_bf16 v[32:47], v[248:251], v[224:227], v[32:47]\n"
"ds_read_b64_tr_b16 v[248:249], v2 offset:32768\n"
"ds_read_b64_tr_b16 v[250:251], v2 offset:33280\n"
"buffer_load_dwordx4 v6, s[52:55], s70 offen lds\n"
"v_mfma_f32_32x32x16_bf16 v[32:47], v[252:255], v[228:231], v[32:47]\n"
"ds_read_b64_tr_b16 v[252:253], v2 offset:34816\n"
"ds_read_b64_tr_b16 v[254:255], v2 offset:35328\n"
".Lkc1_U3_12_%=:\n"
"s_waitcnt lgkmcnt(8)\n"
"v_mfma_f32_32x32x16_bf16 v[48:63], v[232:235], v[88:91], 0\n"
"ds_read_b64_tr_b16 v[232:233], v2 offset:36864\n"
"ds_read_b64_tr_b16 v[234:235], v2 offset:37376\n"
"s_add_u32 s72, s83, 1\n"
"s_cmp_lt_u32 s72, s81\n"
"s_cselect_b32 s58, 5308416, 0\n"
"v_mfma_f32_32x32x16_bf16 v[48:63], v[236:239], v[92:95], v[48:63]\n"
"ds_read_b64_tr_b16 v[236:237], v2 offset:38912\n"
"ds_read_b64_tr_b16 v[238:239], v2 offset:39424\n"
"s_waitcnt lgkmcnt(8)\n"
"v_mfma_f32_32x32x16_bf16 v[48:63], v[240:243], v[96:99], v[48:63]\n"
"ds_read_b64_tr_b16 v[240:241], v2 offset:40960\n"
"ds_read_b64_tr_b16 v[242:243], v2 offset:41472\n"
"v_mfma_f32_32x32x16_bf16 v[48:63], v[244:247], v[100:103], v[48:63]\n"
"ds_read_b64_tr_b16 v[244:245], v2 offset:43008\n"
"ds_read_b64_tr_b16 v[246:247], v2 offset:43520\n"
"s_waitcnt lgkmcnt(8)\n"
"v_mfma_f32_32x32x16_bf16 v[48:63], v[248:251], v[104:107], v[48:63]\n"
"ds_read_b64_tr_b16 v[248:249], v2 offset:45056\n"
"ds_read_b64_tr_b16 v[250:251], v2 offset:45568\n"
"v_mul_f32 v32, 0xbfb8aa3b, v32\n"
"v_mul_f32 v33, 0xbfb8aa3b, v33\n"
"v_mul_f32 v34, 0xbfb8aa3b, v34\n"
"v_mfma_f32_32x32x16_bf16 v[48:63], v[252:255], v[108:111], v[48:63]\n"
"ds_read_b64_tr_b16 v[252:253], v2 offset:47104\n"
"ds_read_b64_tr_b16 v[254:255], v2 offset:47616\n"
"v_mul_f32 v35, 0xbfb8aa3b, v35\n"
"v_mul_f32 v36, 0xbfb8aa3b, v36\n"
"s_waitcnt lgkmcnt(8)\n"
"v_mfma_f32_32x32x16_bf16 v[48:63], v[232:235], v[112:115], v[48:63]\n"
"s_waitcnt vmcnt(11)\n"
"s_barrier\n"
"v_mul_f32 v37, 0xbfb8aa3b, v37\n"
"v_mul_f32 v38, 0xbfb8aa3b, v38\n"
"v_mfma_f32_32x32x16_bf16 v[48:63], v[236:239], v[116:119], v[48:63]\n"
"ds_read_b64_tr_b16 v[232:233], v3 offset:0\n"
"ds_read_b64_tr_b16 v[234:235], v3 offset:512\n"
"ds_read_b64_tr_b16 v[236:237], v3 offset:2048\n"
"ds_read_b64_tr_b16 v[238:239], v3 offset:2560\n"
"s_add_u32 s70, s82, 3539072\n"
"s_add_u32 m0, s91, 49152\n"
"v_mul_f32 v39, 0xbfb8aa3b, v39\n"
"v_mul_f32 v40, 0xbfb8aa3b, v40\n"
"s_waitcnt lgkmcnt(8)\n"
"v_mfma_f32_32x32x16_bf16 v[48:63], v[240:243], v[120:123], v[48:63]\n"
"ds_read_b64_tr_b16 v[240:241], v3 offset:4096\n"
"ds_read_b64_tr_b16 v[242:243], v3 offset:4608\n"
"buffer_load_dwordx4 v4, s[52:55], s70 offen lds\n"
"s_add_u32 m0, m0, 0x2000\n"
"v_mul_f32 v41, 0xbfb8aa3b, v41\n"
"v_mul_f32 v42, 0xbfb8aa3b, v42\n"
"v_mfma_f32_32x32x16_bf16 v[48:63], v[244:247], v[124:127], v[48:63]\n"
"ds_read_b64_tr_b16 v[244:245], v3 offset:6144\n"
"ds_read_b64_tr_b16 v[246:247], v3 offset:6656\n"
"buffer_load_dwordx4 v5, s[52:55], s70 offen lds\n"
"s_add_u32 m0, m0, 0x2000\n"
"v_mul_f32 v43, 0xbfb8aa3b, v43\n"
"v_mul_f32 v44, 0xbfb8aa3b, v44\n"
"s_waitcnt lgkmcnt(8)\n"
"v_mfma_f32_32x32x16_bf16 v[48:63], v[248:251], v[128:131], v[48:63]\n"
"ds_read_b64_tr_b16 v[248:249], v3 offset:8192\n"
"ds_read_b64_tr_b16 v[250:251], v3 offset:8704\n"
"buffer_load_dwordx4 v6, s[52:55], s70 offen lds\n"
"v_mul_f32 v45, 0xbfb8aa3b, v45\n"
"v_mul_f32 v46, 0xbfb8aa3b, v46\n"
"v_mfma_f32_32x32x16_bf16 v[48:63], v[252:255], v[132:135], v[48:63]\n"
"ds_read_b64_tr_b16 v[252:253], v3 offset:10240\n"
"ds_read_b64_tr_b16 v[254:255], v3 offset:10752\n"
"s_add_u32 s71, s82, 256\n"
"buffer_load_dword v29, v11, s[52:55], s71 offen\n"
"buffer_load_dword v29, v12, s[60:63], s92 offen\n"
"s_add_u32 s92, s92, 384\n"
"v_mul_f32 v47, 0xbfb8aa3b, v47\n"
"v_exp_f32 v32, v32\n"
"s_waitcnt lgkmcnt(8)\n"
"v_mfma_f32_32x32x16_bf16 v[48:63], v[232:235], v[136:139], v[48:63]\n"
"ds_read_b64_tr_b16 v[232:233], v3 offset:12288\n"
"ds_read_b64_tr_b16 v[234:235], v3 offset:12800\n"
"v_exp_f32 v33, v33\n"
"v_exp_f32 v34, v34\n"
"v_mfma_f32_32x32x16_bf16 v[48:63], v[236:239], v[140:143], v[48:63]\n"
"ds_read_b64_tr_b16 v[236:237], v3 offset:14336\n"
"ds_read_b64_tr_b16 v[238:239], v3 offset:14848\n"
"v_exp_f32 v35, v35\n"
"v_exp_f32 v36, v36\n"
"s_waitcnt lgkmcnt(8)\n"
"v_mfma_f32_32x32x16_bf16 v[48:63], v[240:243], v[144:147], v[48:63]\n"
"ds_read_b64_tr_b16 v[240:241], v3 offset:16384\n"
"ds_read_b64_tr_b16 v[242:243], v3 offset:16896\n"
"v_exp_f32 v37, v37\n"
"v_exp_f32 v38, v38\n"
"v_mfma_f32_32x32x16_bf16 v[48:63], v[244:247], v[148:151], v[48:63]\n"
"ds_read_b64_tr_b16 v[244:245], v3 offset:18432\n"
"ds_read_b64_tr_b16 v[246:247], v3 offset:18944\n"
"v_exp_f32 v39, v39\n"
"v_exp_f32 v40, v40\n"
"s_waitcnt lgkmcnt(8)\n"
"v_mfma_f32_32x32x16_bf16 v[48:63], v[248:251], v[152:155], v[48:63]\n"
"ds_read_b64_tr_b16 v[248:249], v3 offset:20480\n"
"ds_read_b64_tr_b16 v[250:251], v3 offset:20992\n"
"v_exp_f32 v41, v41\n"
"v_exp_f32 v42, v42\n"
"v_mfma_f32_32x32x16_bf16 v[48:63], v[252:255], v[156:159], v[48:63]\n"
"ds_read_b64_tr_b16 v[252:253], v3 offset:22528\n"
"ds_read_b64_tr_b16 v[254:255], v3 offset:23040\n"
"v_exp_f32 v43, v43\n"
"v_exp_f32 v44, v44\n"
"s_waitcnt lgkmcnt(8)\n"
"v_mfma_f32_32x32x16_bf16 v[48:63], v[232:235], v[160:163], v[48:63]\n"
"s_waitcnt vmcnt(11)\n"
"s_barrier\n"
"v_exp_f32 v45, v45\n"
"v_exp_f32 v46, v46\n"
"v_mfma_f32_32x32x16_bf16 v[48:63], v[236:239], v[164:167], v[48:63]\n"
"ds_read_b64_tr_b16 v[232:233], v3 offset:24576\n"
"ds_read_b64_tr_b16 v[234:235], v3 offset:25088\n"
"ds_read_b64_tr_b16 v[236:237], v3 offset:26624\n"
"ds_read_b64_tr_b16 v[238:239], v3 offset:27136\n"
"s_add_u32 s70, s82, 128\n"
"s_add_u32 m0, s91, 73728\n"
"v_exp_f32 v47, v47\n"
"v_add_f32 v32, 1.0, v32\n"
"v_add_f32 v33, 1.0, v33\n"
"s_waitcnt lgkmcnt(8)\n"
"v_mfma_f32_32x32x16_bf16 v[48:63], v[240:243], v[168:171], v[48:63]\n"
"ds_read_b64_tr_b16 v[240:241], v3 offset:28672\n"
"ds_read_b64_tr_b16 v[242:243], v3 offset:29184\n"
"buffer_load_dwordx4 v4, s[56:59], s70 offen lds\n"
"s_add_u32 m0, m0, 0x2000\n"
"v_add_f32 v34, 1.0, v34\n"
"v_add_f32 v35, 1.0, v35\n"
"v_mfma_f32_32x32x16_bf16 v[48:63], v[244:247], v[172:175], v[48:63]\n"
"ds_read_b64_tr_b16 v[244:245], v3 offset:30720\n"
"ds_read_b64_tr_b16 v[246:247], v3 offset:31232\n"
"buffer_load_dwordx4 v5, s[56:59], s70 offen lds\n"
"s_add_u32 m0, m0, 0x2000\n"
"v_add_f32 v36, 1.0, v36\n"
"v_add_f32 v37, 1.0, v37\n"
"s_waitcnt lgkmcnt(8)\n"
"v_mfma_f32_32x32x16_bf16 v[48:63], v[248:251], v[176:179], v[48:63]\n"
"ds_read_b64_tr_b16 v[248:249], v3 offset:32768\n"
"ds_read_b64_tr_b16 v[250:251], v3 offset:33280\n"
"buffer_load_dwordx4 v6, s[56:59], s70 offen lds\n"
"v_add_f32 v38, 1.0, v38\n"
"v_add_f32 v39, 1.0, v39\n"
"v_mfma_f32_32x32x16_bf16 v[48:63], v[252:255], v[180:183], v[48:63]\n"
"ds_read_b64_tr_b16 v[252:253], v3 offset:34816\n"
"ds_read_b64_tr_b16 v[254:255], v3 offset:35328\n"
"v_add_f32 v40, 1.0, v40\n"
"v_add_f32 v41, 1.0, v41\n"
"s_waitcnt lgkmcnt(8)\n"
"v_mfma_f32_32x32x16_bf16 v[48:63], v[232:235], v[184:187], v[48:63]\n"
"ds_read_b64_tr_b16 v[232:233], v3 offset:36864\n"
"ds_read_b64_tr_b16 v[234:235], v3 offset:37376\n"
"v_add_f32 v42, 1.0, v42\n"
"v_add_f32 v43, 1.0, v43\n"
"v_mfma_f32_32x32x16_bf16 v[48:63], v[236:239], v[188:191], v[48:63]\n"
"ds_read_b64_tr_b16 v[236:237], v3 offset:38912\n"
"ds_read_b64_tr_b16 v[238:239], v3 offset:39424\n"
"v_add_f32 v44, 1.0, v44\n"
"v_add_f32 v45, 1.0, v45\n"
"s_waitcnt lgkmcnt(8)\n"
"v_mfma_f32_32x32x16_bf16 v[48:63], v[240:243], v[192:195], v[48:63]\n"
"ds_read_b64_tr_b16 v[240:241], v3 offset:40960\n"
"ds_read_b64_tr_b16 v[242:243], v3 offset:41472\n"
"v_add_f32 v46, 1.0, v46\n"
"v_add_f32 v47, 1.0, v47\n"
"v_mfma_f32_32x32x16_bf16 v[48:63], v[244:247], v[196:199], v[48:63]\n"
"ds_read_b64_tr_b16 v[244:245], v3 offset:43008\n"
"ds_read_b64_tr_b16 v[246:247], v3 offset:43520\n"
"v_rcp_f32 v64, v32\n"
"v_rcp_f32 v65, v33\n"
"s_waitcnt lgkmcnt(8)\n"
"v_mfma_f32_32x32x16_bf16 v[48:63], v[248:251], v[200:203], v[48:63]\n"
"ds_read_b64_tr_b16 v[248:249], v3 offset:45056\n"
"ds_read_b64_tr_b16 v[250:251], v3 offset:45568\n"
"v_rcp_f32 v66, v34\n"
"v_rcp_f32 v67, v35\n"
"v_mfma_f32_32x32x16_bf16 v[48:63], v[252:255], v[204:207], v[48:63]\n"
"ds_read_b64_tr_b16 v[252:253], v3 offset:47104\n"
"ds_read_b64_tr_b16 v[254:255], v3 offset:47616\n"
"v_rcp_f32 v68, v36\n"
"v_rcp_f32 v69, v37\n"
"s_waitcnt lgkmcnt(8)\n"
"v_mfma_f32_32x32x16_bf16 v[48:63], v[232:235], v[208:211], v[48:63]\n"
"s_waitcnt vmcnt(11)\n"
"s_barrier\n"
"v_rcp_f32 v70, v38\n"
"v_rcp_f32 v71, v39\n"
"v_mfma_f32_32x32x16_bf16 v[48:63], v[236:239], v[212:215], v[48:63]\n"
"ds_read_b64_tr_b16 v[232:233], v1 offset:0\n"
"ds_read_b64_tr_b16 v[234:235], v1 offset:512\n"
"ds_read_b64_tr_b16 v[236:237], v1 offset:2048\n"
"ds_read_b64_tr_b16 v[238:239], v1 offset:2560\n"
"s_add_u32 s70, s82, 1769600\n"
"s_add_u32 m0, s91, 98304\n"
"v_rcp_f32 v72, v40\n"
"v_rcp_f32 v73, v41\n"
"s_waitcnt lgkmcnt(8)\n"
"v_mfma_f32_32x32x16_bf16 v[48:63], v[240:243], v[216:219], v[48:63]\n"
"ds_read_b64_tr_b16 v[240:241], v1 offset:4096\n"
"ds_read_b64_tr_b16 v[242:243], v1 offset:4608\n"
"buffer_load_dwordx4 v4, s[56:59], s70 offen lds\n"
"s_add_u32 m0, m0, 0x2000\n"
"v_rcp_f32 v74, v42\n"
"v_rcp_f32 v75, v43\n"
"v_mfma_f32_32x32x16_bf16 v[48:63], v[244:247], v[220:223], v[48:63]\n"
"ds_read_b64_tr_b16 v[244:245], v1 offset:6144\n"
"ds_read_b64_tr_b16 v[246:247], v1 offset:6656\n"
"buffer_load_dwordx4 v5, s[56:59], s70 offen lds\n"
"s_add_u32 m0, m0, 0x2000\n"
"v_rcp_f32 v76, v44\n"
"v_rcp_f32 v77, v45\n"
"s_waitcnt lgkmcnt(8)\n"
"v_mfma_f32_32x32x16_bf16 v[48:63], v[248:251], v[224:227], v[48:63]\n"
"ds_read_b64_tr_b16 v[248:249], v1 offset:8192\n"
"ds_read_b64_tr_b16 v[250:251], v1 offset:8704\n"
"buffer_load_dwordx4 v6, s[56:59], s70 offen lds\n"
"v_rcp_f32 v78, v46\n"
"v_rcp_f32 v79, v47\n"
"v_mfma_f32_32x32x16_bf16 v[48:63], v[252:255], v[228:231], v[48:63]\n"
"ds_read_b64_tr_b16 v[252:253], v1 offset:10240\n"
"ds_read_b64_tr_b16 v[254:255], v1 offset:10752\n"
"s_add_u32 s83, s83, 1\n"
"s_add_u32 s82, s82, 128\n"
"s_cmp_lt_u32 s83, s81\n"
"s_cbranch_scc1 .Lkc1_GH0_11_%=\n"
".Lkc1_END_13_%=:\n"
"s_lshl_b32 s72, s83, 12\n"
"s_add_u32 s72, s72, s90\n"
"s_sub_u32 s84, s72, 4096\n"
"s_nop 15\n"
"v_mul_f32 v48, v48, v64\n"
"v_mul_f32 v49, v49, v65\n"
"v_mul_f32 v50, v50, v66\n"
"v_mul_f32 v51, v51, v67\n"
"v_mul_f32 v52, v52, v68\n"
"v_mul_f32 v53, v53, v69\n"
"v_mul_f32 v54, v54, v70\n"
"v_mul_f32 v55, v55, v71\n"
"v_mul_f32 v56, v56, v72\n"
"v_mul_f32 v57, v57, v73\n"
"v_mul_f32 v58, v58, v74\n"
"v_mul_f32 v59, v59, v75\n"
"v_mul_f32 v60, v60, v76\n"
"v_mul_f32 v61, v61, v77\n"
"v_mul_f32 v62, v62, v78\n"
"v_mul_f32 v63, v63, v79\n"
"v_cvt_pk_bf16_f32 v80, v48, v49\n"
"v_cvt_pk_bf16_f32 v81, v50, v51\n"
"v_cvt_pk_bf16_f32 v82, v52, v53\n"
"v_cvt_pk_bf16_f32 v83, v54, v55\n"
"v_cvt_pk_bf16_f32 v84, v56, v57\n"
"v_cvt_pk_bf16_f32 v85, v58, v59\n"
"v_cvt_pk_bf16_f32 v86, v60, v61\n"
"v_cvt_pk_bf16_f32 v87, v62, v63\n"
"buffer_store_dwordx4 v[80:83], v10, s[44:47], s84 offen sc1\n"
"buffer_store_dwordx4 v[84:87], v10, s[44:47], s84 offen offset:1024 sc1\n"
"s_waitcnt vmcnt(0) lgkmcnt(0)\n"
  :
  : [x_lo] "s"((u32)px), [x_hi] "s"((u32)(px >> 32)), [wqkv_lo] "s"((u32)pq), [wqkv_hi] "s"((u32)(pq >> 32)),
    [wg_lo] "s"((u32)pg), [wg_hi] "s"((u32)(pg >> 32)), [wu_lo] "s"((u32)pu), [wu_hi] "s"((u32)(pu >> 32)),
    [ws_lo] "s"((u32)pw), [ws_hi] "s"((u32)(pw >> 32)), [tb] "s"(tb), [wid] "s"(wid), [nq] "s"(nq), [qcol0] "s"(qcol0),
    [isv] "s"(isv), [nu] "s"(nu), [f0] "s"(f0), [lds] "s"(lds), [bid] "s"(b),
    [wd_lo] "s"((u32)pd), [wd_hi] "s"((u32)(pd >> 32)), [wo_lo] "s"((u32)po), [wo_hi] "s"((u32)(po >> 32))
  : "memory", "vcc", "scc", "m0", "v0", "v1", "v2", "v3", "v4", "v5", "v6", "v7", "v8", "v9", "v10", "v11", "v12", "v13", "v14", "v15", "v16", "v17", "v18", "v19", "v20", "v21", "v22", "v23", "v24", "v25", "v26", "v27", "v28", "v29", "v30", "v31", "v32", "v33", "v34", "v35", "v36", "v37", "v38", "v39", "v40", "v41", "v42", "v43", "v44", "v45", "v46", "v47", "v48", "v49", "v50", "v51", "v52", "v53", "v54", "v55", "v56", "v57", "v58", "v59", "v60", "v61", "v62", "v63", "v64", "v65", "v66", "v67", "v68", "v69", "v70", "v71", "v72", "v73", "v74", "v75", "v76", "v77", "v78", "v79", "v80", "v81", "v82", "v83", "v84", "v85", "v86", "v87", "v88", "v89", "v90", "v91", "v92", "v93", "v94", "v95", "v96", "v97", "v98", "v99", "v100", "v101", "v102", "v103", "v104", "v105", "v106", "v107", "v108", "v109", "v110", "v111", "v112", "v113", "v114", "v115", "v116", "v117", "v118", "v119", "v120", "v121", "v122", "v123", "v124", "v125", "v126", "v127", "v128", "v129", "v130", "v131", "v132", "v133", "v134", "v135", "v136", "v137", "v138", "v139", "v140", "v141", "v142", "v143", "v144", "v145", "v146", "v147", "v148", "v149", "v150", "v151", "v152", "v153", "v154", "v155", "v156", "v157", "v158", "v159", "v160", "v161", "v162", "v163", "v164", "v165", "v166", "v167", "v168", "v169", "v170", "v171", "v172", "v173", "v174", "v175", "v176", "v177", "v178", "v179", "v180", "v181", "v182", "v183", "v184", "v185", "v186", "v187", "v188", "v189", "v190", "v191", "v192", "v193", "v194", "v195", "v196", "v197", "v198", "v199", "v200", "v201", "v202", "v203", "v204", "v205", "v206", "v207", "v208", "v209", "v210", "v211", "v212", "v213", "v214", "v215", "v216", "v217", "v218", "v219", "v220", "v221", "v222", "v223", "v224", "v225", "v226", "v227", "v228", "v229", "v230", "v231", "v232", "v233", "v234", "v235", "v236", "v237", "v238", "v239", "v240", "v241", "v242", "v243", "v244", "v245", "v246", "v247", "v248", "v249", "v250", "v251", "v252", "v253", "v254", "v255", "s40", "s41", "s42", "s43", "s44", "s45", "s46", "s47", "s48", "s49", "s50", "s51", "s52", "s53", "s54", "s55", "s56", "s57", "s58", "s59", "s60", "s61", "s62", "s63", "s64", "s65", "s66", "s67", "s68", "s69", "s70", "s71", "s72", "s73", "s74", "s75", "s76", "s77", "s78", "s79", "s80", "s81", "s82", "s83", "s84", "s85", "s86", "s87", "s88", "s89", "s90", "s91", "s92", "s93", "s94", "s95");
}

__global__ __launch_bounds__(512, 2) void kb_kernel(const u16* __restrict__ x, const u16* __restrict__ wo, const u16* __restrict__ wd, char* __restrict__ ws, u16* __restrict__ out) {
  __shared__ __attribute__((aligned(16))) char smem[156672];
  const int b = blockIdx.x, xcd = b & 7, idx = b >> 3;
  int qb = xcd * 8 + (idx & 7), hg = idx >> 3;
  const int p = xcd * 10 + idx / 3;
  int dg = idx % 3, ks = p >> 4, tb = p & 15;
  int b0 = ks < 2 ? 15 * ks : 30 + 14 * (ks - 2), nsl = idx < 30 ? (ks < 2 ? 15 : 14) : 0;
  int fin = idx < 18 ? 1 : 0, ftb = xcd * 2 + (idx >= 9 ? 1 : 0), cb = idx % 9, fdg = (idx % 9) / 3;
  const int wid = RFL(threadIdx.x >> 6);
  qb = RFL(qb); hg = RFL(hg); dg = RFL(dg); ks = RFL(ks); tb = RFL(tb); b0 = RFL(b0); nsl = RFL(nsl); fin = RFL(fin); ftb = RFL(ftb); cb = RFL(cb); fdg = RFL(fdg);
  const uint64_t px = (uint64_t)x, po = (uint64_t)wo, pd = (uint64_t)wd, pw = (uint64_t)ws, pout = (uint64_t)out;
  u32 lds = (u32)(uintptr_t)smem;
  asm volatile(
"s_mov_b32 s44, %[ws_lo]\n"
"s_and_b32 s45, %[ws_hi], 0xffff\n"
"s_mov_b32 s46, 40670464\n"
"s_mov_b32 s47, 0x20000\n"
"s_mov_b32 s55, %[wid]\n"
"s_mov_b32 s59, %[nsl]\n"
"s_and_b32 s56, s55, 3\n"
"s_lshr_b32 s57, s55, 2\n"
"s_mov_b32 s54, s56\n"
"v_mbcnt_lo_u32_b32 v0, -1, 0\n"
"v_mbcnt_hi_u32_b32 v0, -1, v0\n"
"v_mov_b32 v3, 0\n"
"s_lshl_b32 s70, %[hg], 3\n"
"s_add_u32 s70, s70, s55\n"
"s_mul_i32 s70, s70, 18\n"
"s_lshr_b32 s71, s70, 4\n"
"s_lshl_b32 s72, s71, 4\n"
"s_sub_u32 s65, s70, s72\n"
"s_mul_i32 s72, %[qb], 72\n"
"s_add_u32 s72, s72, s71\n"
"s_lshl_b32 s73, s72, 10\n"
"s_sub_u32 s64, 8, %[qb]\n"
"s_cselect_b32 s64, 0, s64\n"
"s_add_u32 s66, s73, 4294414336\n"
"v_lshlrev_b32 v2, 4, v0\n"
"buffer_load_dwordx4 v[232:235], v2, s[44:47], s73 offen offset:0\n"
"buffer_load_dwordx4 v[236:239], v2, s[44:47], s73 offen offset:1024\n"
"s_cmp_ge_u32 0, s64\n"
"s_cselect_b32 s74, s66, 1073741824\n"
"buffer_load_dwordx4 v[160:163], v2, s[44:47], s74 offen offset:0\n"
"buffer_load_dwordx4 v[164:167], v2, s[44:47], s74 offen offset:1024\n"
"s_add_u32 s66, s66, 73728\n"
"s_cmp_ge_u32 1, s64\n"
"s_cselect_b32 s74, s66, 1073741824\n"
"buffer_load_dwordx4 v[168:171], v2, s[44:47], s74 offen offset:0\n"
"buffer_load_dwordx4 v[172:175], v2, s[44:47], s74 offen offset:1024\n"
"s_add_u32 s66, s66, 73728\n"
"s_cmp_ge_u32 2, s64\n"
"s_cselect_b32 s74, s66, 1073741824\n"
"buffer_load_dwordx4 v[176:179], v2, s[44:47], s74 offen offset:0\n"
"buffer_load_dwordx4 v[180:183], v2, s[44:47], s74 offen offset:1024\n"
"s_add_u32 s66, s66, 73728\n"
"s_cmp_ge_u32 3, s64\n"
"s_cselect_b32 s74, s66, 1073741824\n"
"buffer_load_dwordx4 v[184:187], v2, s[44:47], s74 offen offset:0\n"
"buffer_load_dwordx4 v[188:191], v2, s[44:47], s74 offen offset:1024\n"
"s_add_u32 s66, s66, 73728\n"
"s_cmp_ge_u32 4, s64\n"
"s_cselect_b32 s74, s66, 1073741824\n"
"buffer_load_dwordx4 v[192:195], v2, s[44:47], s74 offen offset:0\n"
"buffer_load_dwordx4 v[196:199], v2, s[44:47], s74 offen offset:1024\n"
"s_add_u32 s66, s66, 73728\n"
"s_cmp_ge_u32 5, s64\n"
"s_cselect_b32 s74, s66, 1073741824\n"
"buffer_load_dwordx4 v[200:203], v2, s[44:47], s74 offen offset:0\n"
"buffer_load_dwordx4 v[204:207], v2, s[44:47], s74 offen offset:1024\n"
"s_add_u32 s66, s66, 73728\n"
"s_cmp_ge_u32 6, s64\n"
"s_cselect_b32 s74, s66, 1073741824\n"
"buffer_load_dwordx4 v[208:211], v2, s[44:47], s74 offen offset:0\n"
"buffer_load_dwordx4 v[212:215], v2, s[44:47], s74 offen offset:1024\n"
"s_add_u32 s66, s66, 73728\n"
"s_cmp_ge_u32 7, s64\n"
"s_cselect_b32 s74, s66, 1073741824\n"
"buffer_load_dwordx4 v[216:219], v2, s[44:47], s74 offen offset:0\n"
"buffer_load_dwordx4 v[220:223], v2, s[44:47], s74 offen offset:1024\n"
"s_add_u32 s66, s66, 73728\n"
"s_cmp_ge_u32 8, s64\n"
"s_cselect_b32 s74, s66, 1073741824\n"
"buffer_load_dwordx4 v[224:227], v2, s[44:47], s74 offen offset:0\n"
"buffer_load_dwordx4 v[228:231], v2, s[44:47], s74 offen offset:1024\n"
"s_add_u32 s66, s66, 73728\n"
"s_mov_b32 s40, %[wd_lo]\n"
"s_and_b32 s41, %[wd_hi], 0xffff\n"
"s_mov_b32 s43, 0x20000\n"
"s_cmp_lt_u32 0, s59\n"
"s_cselect_b32 s42, 5308416, 0\n"
"s_mov_b32 s48, %[wo_lo]\n"
"s_and_b32 s49, %[wo_hi], 0xffff\n"
"s_mov_b32 s51, 0x20000\n"
"s_cmp_lt_u32 0, %[fin]\n"
"s_cselect_b32 s50, 663552, 0\n"
"s_sub_u32 s58, 15, s59\n"
"s_sub_u32 s74, %[b0], s58\n"
"s_lshl_b32 s71, s74, 6\n"
"s_mul_i32 s71, s71, 1152\n"
"s_mul_i32 s72, %[dg], 384\n"
"s_add_u32 s71, s71, s72\n"
"s_mul_i32 s72, s55, 9216\n"
"s_add_u32 s67, s71, s72\n"
"s_mul_i32 s72, s54, 9216\n"
"s_add_u32 s60, s71, s72\n"
"s_lshl_b32 s68, s55, 10\n"
"s_lshl_b32 s62, s54, 10\n"
"s_lshl_b32 s71, %[cb], 7\n"
"s_add_u32 s90, s72, s71\n"
"v_lshrrev_b32 v10, 3, v0\n"
"v_and_b32 v11, 7, v0\n"
"v_bfe_u32 v12, v0, 4, 1\n"
"v_lshlrev_b32 v12, 2, v12\n"
"v_xor_b32 v12, v11, v12\n"
"v_lshlrev_b32 v12, 4, v12\n"
"v_mul_u32_u24 v1, 1152, v10\n"
"v_add_u32 v1, v1, v12\n"
"s_lshl_b32 s69, %[tb], 2\n"
"s_add_u32 s69, s69, s56\n"
"s_mul_i32 s71, s69, 288\n"
"s_lshl_b32 s72, s74, 2\n"
"s_add_u32 s71, s71, s72\n"
"s_lshl_b32 s71, s71, 10\n"
"s_add_u32 s61, s71, 7077888\n"
"s_lshr_b32 s71, %[qb], 2\n"
"s_lshl_b32 s71, s71, 7\n"
"s_add_u32 s92, s71, 40108032\n"
"v_lshrrev_b32 v246, 5, v0\n"
"v_and_b32 v243, 31, v0\n"
"v_add_u32 v244, s70, v243\n"
"v_lshrrev_b32 v245, 5, v244\n"
"v_lshlrev_b32 v245, 7, v245\n"
"v_and_b32 v244, 31, v244\n"
"v_add_u32 v245, v245, v244\n"
"v_lshl_add_u32 v245, v246, 5, v245\n"
"v_lshlrev_b32 v240, 4, v245\n"
"s_mul_i32 s71, %[qb], 36\n"
"s_sub_u32 s71, s71, 288\n"
"s_lshl_b32 s71, s71, 10\n"
"s_add_u32 s71, s71, 4718592\n"
"v_add_u32 v240, s71, v240\n"
"v_mov_b32 v241, 36864\n"
"s_lshl_b32 s75, %[bid], 31\n"
"s_lshl_b32 s75, s75, 1\n"
"s_add_u32 s75, s75, 40116224\n"
"v_mov_b32 v244, s75\n"
"v_mov_b32 v245, 1073741824\n"
"v_cmp_eq_u32 s[80:81], 18, v243\n"
"v_cmp_lt_u32 s[82:83], 18, v243\n"
"v_cmp_le_u32 s[84:85], 18, v243\n"
"s_nop 1\n"
"v_cndmask_b32 v240, v240, v244, s[80:81]\n"
"v_cndmask_b32 v240, v240, v245, s[82:83]\n"
"v_cndmask_b32 v241, v241, 0, s[84:85]\n"
"v_lshlrev_b32 v244, 2, v246\n"
"v_sub_u32 v242, v244, v243\n"
"v_subrev_u32 v244, s65, v244\n"
"v_mov_b32 v250, 0x3eae1a93\n"
"v_mov_b32 v251, 0x3eae1a93\n"
"s_waitcnt vmcnt(18)\n"
"v_add_u32 v252, 0, v244\n"
"v_add_u32 v253, 2, v244\n"
"v_add_u32 v254, 8, v244\n"
"v_add_u32 v255, 10, v244\n"
"v_cmp_gt_u32 s[80:81], 18, v252\n"
"v_cmp_gt_u32 s[82:83], 18, v253\n"
"v_cmp_gt_u32 s[84:85], 18, v254\n"
"v_cmp_gt_u32 s[86:87], 18, v255\n"
"s_nop 0\n"
"v_cndmask_b32 v232, 0, v232, s[80:81]\n"
"v_cndmask_b32 v233, 0, v233, s[82:83]\n"
"v_cndmask_b32 v234, 0, v234, s[84:85]\n"
"v_cndmask_b32 v235, 0, v235, s[86:87]\n"
"v_add_u32 v252, 16, v244\n"
"v_add_u32 v253, 18, v244\n"
"v_add_u32 v254, 24, v244\n"
"v_add_u32 v255, 26, v244\n"
"v_cmp_gt_u32 s[80:81], 18, v252\n"
"v_cmp_gt_u32 s[82:83], 18, v253\n"
"v_cmp_gt_u32 s[84:85], 18, v254\n"
"v_cmp_gt_u32 s[86:87], 18, v255\n"
"s_nop 0\n"
"v_cndmask_b32 v236, 0, v236, s[80:81]\n"
"v_cndmask_b32 v237, 0, v237, s[82:83]\n"
"v_cndmask_b32 v238, 0, v238, s[84:85]\n"
"v_cndmask_b32 v239, 0, v239, s[86:87]\n"
"s_nop 1\n"
"s_waitcnt vmcnt(16)\n"
"v_mfma_f32_32x32x16_bf16 v[16:31], v[160:163], v[232:235], 0\n"
"v_mfma_f32_32x32x16_bf16 v[16:31], v[164:167], v[236:239], v[16:31]\n"
"buffer_load_dwordx4 v[160:163], v240, s[44:47], 0 offen\n"
"buffer_load_dwordx4 v[164:167], v240, s[44:47], 0 offen offset:1024\n"
"v_add_u32 v240, v240, v241\n"
"s_waitcnt vmcnt(16)\n"
"v_mfma_f32_32x32x16_bf16 v[32:47], v[168:171], v[232:235], 0\n"
"v_mfma_f32_32x32x16_bf16 v[32:47], v[172:175], v[236:239], v[32:47]\n"
"buffer_load_dwordx4 v[168:171], v240, s[44:47], 0 offen\n"
"buffer_load_dwordx4 v[172:175], v240, s[44:47], 0 offen offset:1024\n"
"v_add_u32 v240, v240, v241\n"
"s_waitcnt vmcnt(16)\n"
"v_mfma_f32_32x32x16_bf16 v[48:63], v[176:179], v[232:235], 0\n"
"v_mfma_f32_32x32x16_bf16 v[48:63], v[180:183], v[236:239], v[48:63]\n"
"buffer_load_dwordx4 v[176:179], v240, s[44:47], 0 offen\n"
"buffer_load_dwordx4 v[180:183], v240, s[44:47], 0 offen offset:1024\n"
"v_add_u32 v240, v240, v241\n"
"s_waitcnt vmcnt(16)\n"
"v_mfma_f32_32x32x16_bf16 v[64:79], v[184:187], v[232:235], 0\n"
"v_mfma_f32_32x32x16_bf16 v[64:79], v[188:191], v[236:239], v[64:79]\n"
"buffer_load_dwordx4 v[184:187], v240, s[44:47], 0 offen\n"
"buffer_load_dwordx4 v[188:191], v240, s[44:47], 0 offen offset:1024\n"
"v_add_u32 v240, v240, v241\n"
"s_waitcnt vmcnt(16)\n"
"v_mfma_f32_32x32x16_bf16 v[80:95], v[192:195], v[232:235], 0\n"
"v_mfma_f32_32x32x16_bf16 v[80:95], v[196:199], v[236:239], v[80:95]\n"
"buffer_load_dwordx4 v[192:195], v240, s[44:47], 0 offen\n"
"buffer_load_dwordx4 v[196:199], v240, s[44:47], 0 offen offset:1024\n"
"v_add_u32 v240, v240, v241\n"
"s_waitcnt vmcnt(16)\n"
"v_mfma_f32_32x32x16_bf16 v[96:111], v[200:203], v[232:235], 0\n"
"v_mfma_f32_32x32x16_bf16 v[96:111], v[204:207], v[236:239], v[96:111]\n"
"buffer_load_dwordx4 v[200:203], v240, s[44:47], 0 offen\n"
"buffer_load_dwordx4 v[204:207], v240, s[44:47], 0 offen offset:1024\n"
"v_add_u32 v240, v240, v241\n"
"s_waitcnt vmcnt(16)\n"
"v_mfma_f32_32x32x16_bf16 v[112:127], v[208:211], v[232:235], 0\n"
"v_mfma_f32_32x32x16_bf16 v[112:127], v[212:215], v[236:239], v[112:127]\n"
"buffer_load_dwordx4 v[208:211], v240, s[44:47], 0 offen\n"
"buffer_load_dwordx4 v[212:215], v240, s[44:47], 0 offen offset:1024\n"
"v_add_u32 v240, v240, v241\n"
"s_waitcnt vmcnt(16)\n"
"v_mfma_f32_32x32x16_bf16 v[128:143], v[216:219], v[232:235], 0\n"
"v_mfma_f32_32x32x16_bf16 v[128:143], v[220:223], v[236:239], v[128:143]\n"
"buffer_load_dwordx4 v[216:219], v240, s[44:47], 0 offen\n"
"buffer_load_dwordx4 v[220:223], v240, s[44:47], 0 offen offset:1024\n"
"v_add_u32 v240, v240, v241\n"
"s_waitcnt vmcnt(16)\n"
"v_mfma_f32_32x32x16_bf16 v[144:159], v[224:227], v[232:235], 0\n"
"v_mfma_f32_32x32x16_bf16 v[144:159], v[228:231], v[236:239], v[144:159]\n"
"buffer_load_dwordx4 v[224:227], v240, s[44:47], 0 offen\n"
"buffer_load_dwordx4 v[228:231], v240, s[44:47], 0 offen offset:1024\n"
"v_add_u32 v240, v240, v241\n"
"s_nop 15\n"
"v_mov_b32 v245, 0xff61b1e6\n"
"s_cmp_eq_u32 s64, 0\n"
"s_cbranch_scc1 .Lkc_allv_1_%=\n"
"s_cmp_lt_u32 0, s64\n"
"s_cbranch_scc0 .Lkc_nk_2_%=\n"
"v_mov_b32 v16, v245\n"
"v_mov_b32 v17, v245\n"
"v_mov_b32 v18, v245\n"
"v_mov_b32 v19, v245\n"
"v_mov_b32 v20, v245\n"
"v_mov_b32 v21, v245\n"
"v_mov_b32 v22, v245\n"
"v_mov_b32 v23, v245\n"
"v_mov_b32 v24, v245\n"
"v_mov_b32 v25, v245\n"
"v_mov_b32 v26, v245\n"
"v_mov_b32 v27, v245\n"
"v_mov_b32 v28, v245\n"
"v_mov_b32 v29, v245\n"
"v_mov_b32 v30, v245\n"
"v_mov_b32 v31, v245\n"
".Lkc_nk_2_%=:\n"
"s_cmp_lt_u32 1, s64\n"
"s_cbranch_scc0 .Lkc_nk_3_%=\n"
"v_mov_b32 v32, v245\n"
"v_mov_b32 v33, v245\n"
"v_mov_b32 v34, v245\n"
"v_mov_b32 v35, v245\n"
"v_mov_b32 v36, v245\n"
"v_mov_b32 v37, v245\n"
"v_mov_b32 v38, v245\n"
"v_mov_b32 v39, v245\n"
"v_mov_b32 v40, v245\n"
"v_mov_b32 v41, v245\n"
"v_mov_b32 v42, v245\n"
"v_mov_b32 v43, v245\n"
"v_mov_b32 v44, v245\n"
"v_mov_b32 v45, v245\n"
"v_mov_b32 v46, v245\n"
"v_mov_b32 v47, v245\n"
".Lkc_nk_3_%=:\n"
"s_cmp_lt_u32 2, s64\n"
"s_cbranch_scc0 .Lkc_nk_4_%=\n"
"v_mov_b32 v48, v245\n"
"v_mov_b32 v49, v245\n"
"v_mov_b32 v50, v245\n"
"v_mov_b32 v51, v245\n"
"v_mov_b32 v52, v245\n"
"v_mov_b32 v53, v245\n"
"v_mov_b32 v54, v245\n"
"v_mov_b32 v55, v245\n"
"v_mov_b32 v56, v245\n"
"v_mov_b32 v57, v245\n"
"v_mov_b32 v58, v245\n"
"v_mov_b32 v59, v245\n"
"v_mov_b32 v60, v245\n"
"v_mov_b32 v61, v245\n"
"v_mov_b32 v62, v245\n"
"v_mov_b32 v63, v245\n"
".Lkc_nk_4_%=:\n"
"s_cmp_lt_u32 3, s64\n"
"s_cbranch_scc0 .Lkc_nk_5_%=\n"
"v_mov_b32 v64, v245\n"
"v_mov_b32 v65, v245\n"
"v_mov_b32 v66, v245\n"
"v_mov_b32 v67, v245\n"
"v_mov_b32 v68, v245\n"
"v_mov_b32 v69, v245\n"
"v_mov_b32 v70, v245\n"
"v_mov_b32 v71, v245\n"
"v_mov_b32 v72, v245\n"
"v_mov_b32 v73, v245\n"
"v_mov_b32 v74, v245\n"
"v_mov_b32 v75, v245\n"
"v_mov_b32 v76, v245\n"
"v_mov_b32 v77, v245\n"
"v_mov_b32 v78, v245\n"
"v_mov_b32 v79, v245\n"
".Lkc_nk_5_%=:\n"
"s_cmp_lt_u32 4, s64\n"
"s_cbranch_scc0 .Lkc_nk_6_%=\n"
"v_mov_b32 v80, v245\n"
"v_mov_b32 v81, v245\n"
"v_mov_b32 v82, v245\n"
"v_mov_b32 v83, v245\n"
"v_mov_b32 v84, v245\n"
"v_mov_b32 v85, v245\n"
"v_mov_b32 v86, v245\n"
"v_mov_b32 v87, v245\n"
"v_mov_b32 v88, v245\n"
"v_mov_b32 v89, v245\n"
"v_mov_b32 v90, v245\n"
"v_mov_b32 v91, v245\n"
"v_mov_b32 v92, v245\n"
"v_mov_b32 v93, v245\n"
"v_mov_b32 v94, v245\n"
"v_mov_b32 v95, v245\n"
".Lkc_nk_6_%=:\n"
"s_cmp_lt_u32 5, s64\n"
"s_cbranch_scc0 .Lkc_nk_7_%=\n"
"v_mov_b32 v96, v245\n"
"v_mov_b32 v97, v245\n"
"v_mov_b32 v98, v245\n"
"v_mov_b32 v99, v245\n"
"v_mov_b32 v100, v245\n"
"v_mov_b32 v101, v245\n"
"v_mov_b32 v102, v245\n"
"v_mov_b32 v103, v245\n"
"v_mov_b32 v104, v245\n"
"v_mov_b32 v105, v245\n"
"v_mov_b32 v106, v245\n"
"v_mov_b32 v107, v245\n"
"v_mov_b32 v108, v245\n"
"v_mov_b32 v109, v245\n"
"v_mov_b32 v110, v245\n"
"v_mov_b32 v111, v245\n"
".Lkc_nk_7_%=:\n"
"s_cmp_lt_u32 6, s64\n"
"s_cbranch_scc0 .Lkc_nk_8_%=\n"
"v_mov_b32 v112, v245\n"
"v_mov_b32 v113, v245\n"
"v_mov_b32 v114, v245\n"
"v_mov_b32 v115, v245\n"
"v_mov_b32 v116, v245\n"
"v_mov_b32 v117, v245\n"
"v_mov_b32 v118, v245\n"
"v_mov_b32 v119, v245\n"
"v_mov_b32 v120, v245\n"
"v_mov_b32 v121, v245\n"
"v_mov_b32 v122, v245\n"
"v_mov_b32 v123, v245\n"
"v_mov_b32 v124, v245\n"
"v_mov_b32 v125, v245\n"
"v_mov_b32 v126, v245\n"
"v_mov_b32 v127, v245\n"
".Lkc_nk_8_%=:\n"
"s_cmp_lt_u32 7, s64\n"
"s_cbranch_scc0 .Lkc_nk_9_%=\n"
"v_mov_b32 v128, v245\n"
"v_mov_b32 v129, v245\n"
"v_mov_b32 v130, v245\n"
"v_mov_b32 v131, v245\n"
"v_mov_b32 v132, v245\n"
"v_mov_b32 v133, v245\n"
"v_mov_b32 v134, v245\n"
"v_mov_b32 v135, v245\n"
"v_mov_b32 v136, v245\n"
"v_mov_b32 v137, v245\n"
"v_mov_b32 v138, v245\n"
"v_mov_b32 v139, v245\n"
"v_mov_b32 v140, v245\n"
"v_mov_b32 v141, v245\n"
"v_mov_b32 v142, v245\n"
"v_mov_b32 v143, v245\n"
".Lkc_nk_9_%=:\n"
".Lkc_allv_1_%=:\n"
"v_add_u32 v241, 16, v242\n"
"v_cmp_lt_i32 s[80:81], 0, v242\n"
"v_cmp_lt_i32 s[82:83], -1, v242\n"
"v_cmp_lt_i32 s[84:85], -2, v242\n"
"v_cmp_lt_i32 s[86:87], -3, v242\n"
"s_nop 0\n"
"v_cndmask_b32 v16, v245, v16, s[80:81]\n"
"v_cndmask_b32 v144, v144, v245, s[80:81]\n"
"v_cndmask_b32 v17, v245, v17, s[82:83]\n"
"v_cndmask_b32 v145, v145, v245, s[82:83]\n"
"v_cndmask_b32 v18, v245, v18, s[84:85]\n"
"v_cndmask_b32 v146, v146, v245, s[84:85]\n"
"v_cndmask_b32 v19, v245, v19, s[86:87]\n"
"v_cndmask_b32 v147, v147, v245, s[86:87]\n"
"v_cmp_lt_i32 s[80:81], -8, v242\n"
"v_cmp_lt_i32 s[82:83], -9, v242\n"
"v_cmp_lt_i32 s[84:85], -10, v242\n"
"v_cmp_lt_i32 s[86:87], -11, v242\n"
"s_nop 0\n"
"v_cndmask_b32 v20, v245, v20, s[80:81]\n"
"v_cndmask_b32 v148, v148, v245, s[80:81]\n"
"v_cndmask_b32 v21, v245, v21, s[82:83]\n"
"v_cndmask_b32 v149, v149, v245, s[82:83]\n"
"v_cndmask_b32 v22, v245, v22, s[84:85]\n"
"v_cndmask_b32 v150, v150, v245, s[84:85]\n"
"v_cndmask_b32 v23, v245, v23, s[86:87]\n"
"v_cndmask_b32 v151, v151, v245, s[86:87]\n"
"v_cmp_lt_i32 s[80:81], -16, v242\n"
"v_cmp_lt_i32 s[82:83], -1, v241\n"
"v_cmp_lt_i32 s[84:85], -2, v241\n"
"v_cmp_lt_i32 s[86:87], -3, v241\n"
"s_nop 0\n"
"v_cndmask_b32 v24, v245, v24, s[80:81]\n"
"v_cndmask_b32 v152, v152, v245, s[80:81]\n"
"v_cndmask_b32 v25, v245, v25, s[82:83]\n"
"v_cndmask_b32 v153, v153, v245, s[82:83]\n"
"v_cndmask_b32 v26, v245, v26, s[84:85]\n"
"v_cndmask_b32 v154, v154, v245, s[84:85]\n"
"v_cndmask_b32 v27, v245, v27, s[86:87]\n"
"v_cndmask_b32 v155, v155, v245, s[86:87]\n"
"v_cmp_lt_i32 s[80:81], -8, v241\n"
"v_cmp_lt_i32 s[82:83], -9, v241\n"
"v_cmp_lt_i32 s[84:85], -10, v241\n"
"v_cmp_lt_i32 s[86:87], -11, v241\n"
"s_nop 0\n"
"v_cndmask_b32 v28, v245, v28, s[80:81]\n"
"v_cndmask_b32 v156, v156, v245, s[80:81]\n"
"v_cndmask_b32 v29, v245, v29, s[82:83]\n"
"v_cndmask_b32 v157, v157, v245, s[82:83]\n"
"v_cndmask_b32 v30, v245, v30, s[84:85]\n"
"v_cndmask_b32 v158, v158, v245, s[84:85]\n"
"v_cndmask_b32 v31, v245, v31, s[86:87]\n"
"v_cndmask_b32 v159, v159, v245, s[86:87]\n"
"v_max3_f32 v252, v16, v17, v18\n"
"v_max3_f32 v253, v19, v20, v21\n"
"v_max3_f32 v254, v22, v23, v24\n"
"v_max3_f32 v255, v25, v26, v27\n"
"v_max3_f32 v252, v252, v28, v29\n"
"v_max3_f32 v253, v253, v30, v31\n"
"v_max3_f32 v254, v254, v32, v33\n"
"v_max3_f32 v255, v255, v34, v35\n"
"v_max3_f32 v252, v252, v36, v37\n"
"v_max3_f32 v253, v253, v38, v39\n"
"v_max3_f32 v254, v254, v40, v41\n"
"v_max3_f32 v255, v255, v42, v43\n"
"v_max3_f32 v252, v252, v44, v45\n"
"v_max3_f32 v253, v253, v46, v47\n"
"v_max3_f32 v254, v254, v48, v49\n"
"v_max3_f32 v255, v255, v50, v51\n"
"v_max3_f32 v252, v252, v52, v53\n"
"v_max3_f32 v253, v253, v54, v55\n"
"v_max3_f32 v254, v254, v56, v57\n"
"v_max3_f32 v255, v255, v58, v59\n"
"v_max3_f32 v252, v252, v60, v61\n"
"v_max3_f32 v253, v253, v62, v63\n"
"v_max3_f32 v254, v254, v64, v65\n"
"v_max3_f32 v255, v255, v66, v67\n"
"v_max3_f32 v252, v252, v68, v69\n"
"v_max3_f32 v253, v253, v70, v71\n"
"v_max3_f32 v254, v254, v72, v73\n"
"v_max3_f32 v255, v255, v74, v75\n"
"v_max3_f32 v252, v252, v76, v77\n"
"v_max3_f32 v253, v253, v78, v79\n"
"v_max3_f32 v254, v254, v80, v81\n"
"v_max3_f32 v255, v255, v82, v83\n"
"v_max3_f32 v252, v252, v84, v85\n"
"v_max3_f32 v253, v253, v86, v87\n"
"v_max3_f32 v254, v254, v88, v89\n"
"v_max3_f32 v255, v255, v90, v91\n"
"v_max3_f32 v252, v252, v92, v93\n"
"v_max3_f32 v253, v253, v94, v95\n"
"v_max3_f32 v254, v254, v96, v97\n"
"v_max3_f32 v255, v255, v98, v99\n"
"v_max3_f32 v252, v252, v100, v101\n"
"v_max3_f32 v253, v253, v102, v103\n"
"v_max3_f32 v254, v254, v104, v105\n"
"v_max3_f32 v255, v255, v106, v107\n"
"v_max3_f32 v252, v252, v108, v109\n"
"v_max3_f32 v253, v253, v110, v111\n"
"v_max3_f32 v254, v254, v112, v113\n"
"v_max3_f32 v255, v255, v114, v115\n"
"v_max3_f32 v252, v252, v116, v117\n"
"v_max3_f32 v253, v253, v118, v119\n"
"v_max3_f32 v254, v254, v120, v121\n"
"v_max3_f32 v255, v255, v122, v123\n"
"v_max3_f32 v252, v252, v124, v125\n"
"v_max3_f32 v253, v253, v126, v127\n"
"v_max3_f32 v254, v254, v128, v129\n"
"v_max3_f32 v255, v255, v130, v131\n"
"v_max3_f32 v252, v252, v132, v133\n"
"v_max3_f32 v253, v253, v134, v135\n"
"v_max3_f32 v254, v254, v136, v137\n"
"v_max3_f32 v255, v255, v138, v139\n"
"v_max3_f32 v252, v252, v140, v141\n"
"v_max3_f32 v253, v253, v142, v143\n"
"v_max3_f32 v254, v254, v144, v145\n"
"v_max3_f32 v255, v255, v146, v147\n"
"v_max3_f32 v252, v252, v148, v149\n"
"v_max3_f32 v253, v253, v150, v151\n"
"v_max3_f32 v254, v254, v152, v153\n"
"v_max3_f32 v255, v255, v154, v155\n"
"v_max3_f32 v252, v252, v156, v157\n"
"v_max3_f32 v253, v253, v158, v159\n"
"v_max3_f32 v252, v252, v253, v254\n"
"s_nop 0\n"
"v_max_f32 v252, v252, v255\n"
"s_nop 0\n"
"v_mov_b32 v253, v252\n"
"s_nop 1\n"
"v_permlane32_swap_b32 v252, v253\n"
"s_nop 1\n"
"v_max_f32 v252, v252, v253\n"
"v_mul_f32 v248, v250, v252\n"
"v_sub_f32 v248, 0, v248\n"
"v_mov_b32 v249, v248\n"
"v_pk_fma_f32 v[16:17], v[16:17], v[250:251], v[248:249]\n"
"v_pk_fma_f32 v[18:19], v[18:19], v[250:251], v[248:249]\n"
"v_pk_fma_f32 v[20:21], v[20:21], v[250:251], v[248:249]\n"
"v_pk_fma_f32 v[22:23], v[22:23], v[250:251], v[248:249]\n"
"v_pk_fma_f32 v[24:25], v[24:25], v[250:251], v[248:249]\n"
"v_pk_fma_f32 v[26:27], v[26:27], v[250:251], v[248:249]\n"
"v_pk_fma_f32 v[28:29], v[28:29], v[250:251], v[248:249]\n"
"v_pk_fma_f32 v[30:31], v[30:31], v[250:251], v[248:249]\n"
"v_exp_f32 v16, v16\n"
"v_exp_f32 v17, v17\n"
"v_exp_f32 v18, v18\n"
"v_exp_f32 v19, v19\n"
"v_exp_f32 v20, v20\n"
"v_exp_f32 v21, v21\n"
"v_exp_f32 v22, v22\n"
"v_exp_f32 v23, v23\n"
"v_exp_f32 v24, v24\n"
"v_exp_f32 v25, v25\n"
"v_exp_f32 v26, v26\n"
"v_exp_f32 v27, v27\n"
"v_exp_f32 v28, v28\n"
"v_exp_f32 v29, v29\n"
"v_exp_f32 v30, v30\n"
"v_exp_f32 v31, v31\n"
"v_cvt_pk_bf16_f32 v16, v16, v17\n"
"v_cvt_pk_bf16_f32 v17, v18, v19\n"
"v_cvt_pk_bf16_f32 v18, v20, v21\n"
"v_cvt_pk_bf16_f32 v19, v22, v23\n"
"v_cvt_pk_bf16_f32 v20, v24, v25\n"
"v_cvt_pk_bf16_f32 v21, v26, v27\n"
"v_cvt_pk_bf16_f32 v22, v28, v29\n"
"v_cvt_pk_bf16_f32 v23, v30, v31\n"
"s_add_u32 m0, s68, 0\n"
"s_add_u32 s75, s67, 0\n"
"s_nop 0\n"
"buffer_load_dwordx4 v1, s[40:43], s75 offen lds\n"
"s_add_u32 m0, s68, 8192\n"
"s_add_u32 s75, s67, 128\n"
"s_nop 0\n"
"buffer_load_dwordx4 v1, s[40:43], s75 offen lds\n"
"s_add_u32 m0, s68, 16384\n"
"s_add_u32 s75, s67, 256\n"
"s_nop 0\n"
"buffer_load_dwordx4 v1, s[40:43], s75 offen lds\n"
"v_pk_fma_f32 v[32:33], v[32:33], v[250:251], v[248:249]\n"
"v_pk_fma_f32 v[34:35], v[34:35], v[250:251], v[248:249]\n"
"v_pk_fma_f32 v[36:37], v[36:37], v[250:251], v[248:249]\n"
"v_pk_fma_f32 v[38:39], v[38:39], v[250:251], v[248:249]\n"
"v_pk_fma_f32 v[40:41], v[40:41], v[250:251], v[248:249]\n"
"v_pk_fma_f32 v[42:43], v[42:43], v[250:251], v[248:249]\n"
"v_pk_fma_f32 v[44:45], v[44:45], v[250:251], v[248:249]\n"
"v_pk_fma_f32 v[46:47], v[46:47], v[250:251], v[248:249]\n"
"v_exp_f32 v32, v32\n"
"v_exp_f32 v33, v33\n"
"v_exp_f32 v34, v34\n"
"v_exp_f32 v35, v35\n"
"v_exp_f32 v36, v36\n"
"v_exp_f32 v37, v37\n"
"v_exp_f32 v38, v38\n"
"v_exp_f32 v39, v39\n"
"v_exp_f32 v40, v40\n"
"v_exp_f32 v41, v41\n"
"v_exp_f32 v42, v42\n"
"v_exp_f32 v43, v43\n"
"v_exp_f32 v44, v44\n"
"v_exp_f32 v45, v45\n"
"v_exp_f32 v46, v46\n"
"v_exp_f32 v47, v47\n"
"v_cvt_pk_bf16_f32 v32, v32, v33\n"
"v_cvt_pk_bf16_f32 v33, v34, v35\n"
"v_cvt_pk_bf16_f32 v34, v36, v37\n"
"v_cvt_pk_bf16_f32 v35, v38, v39\n"
"v_cvt_pk_bf16_f32 v36, v40, v41\n"
"v_cvt_pk_bf16_f32 v37, v42, v43\n"
"v_cvt_pk_bf16_f32 v38, v44, v45\n"
"v_cvt_pk_bf16_f32 v39, v46, v47\n"
"s_add_u32 m0, s68, 24576\n"
"s_add_u32 s75, s67, 73728\n"
"s_nop 0\n"
"buffer_load_dwordx4 v1, s[40:43], s75 offen lds\n"
"s_add_u32 m0, s68, 32768\n"
"s_add_u32 s75, s67, 73856\n"
"s_nop 0\n"
"buffer_load_dwordx4 v1, s[40:43], s75 offen lds\n"
"s_add_u32 m0, s68, 40960\n"
"s_add_u32 s75, s67, 73984\n"
"s_nop 0\n"
"buffer_load_dwordx4 v1, s[40:43], s75 offen lds\n"
"s_waitcnt vmcnt(6)\n"
"v_pk_fma_f32 v[48:49], v[48:49], v[250:251], v[248:249]\n"
"v_pk_fma_f32 v[50:51], v[50:51], v[250:251], v[248:249]\n"
"v_pk_fma_f32 v[52:53], v[52:53], v[250:251], v[248:249]\n"
"v_pk_fma_f32 v[54:55], v[54:55], v[250:251], v[248:249]\n"
"v_pk_fma_f32 v[56:57], v[56:57], v[250:251], v[248:249]\n"
"v_pk_fma_f32 v[58:59], v[58:59], v[250:251], v[248:249]\n"
"v_pk_fma_f32 v[60:61], v[60:61], v[250:251], v[248:249]\n"
"v_pk_fma_f32 v[62:63], v[62:63], v[250:251], v[248:249]\n"
"v_exp_f32 v48, v48\n"
"v_exp_f32 v49, v49\n"
"v_exp_f32 v50, v50\n"
"v_exp_f32 v51, v51\n"
"v_exp_f32 v52, v52\n"
"v_exp_f32 v53, v53\n"
"v_exp_f32 v54, v54\n"
"v_exp_f32 v55, v55\n"
"v_exp_f32 v56, v56\n"
"v_exp_f32 v57, v57\n"
"v_exp_f32 v58, v58\n"
"v_exp_f32 v59, v59\n"
"v_exp_f32 v60, v60\n"
"v_exp_f32 v61, v61\n"
"v_exp_f32 v62, v62\n"
"v_exp_f32 v63, v63\n"
"v_cvt_pk_bf16_f32 v48, v48, v49\n"
"v_cvt_pk_bf16_f32 v49, v50, v51\n"
"v_cvt_pk_bf16_f32 v50, v52, v53\n"
"v_cvt_pk_bf16_f32 v51, v54, v55\n"
"v_cvt_pk_bf16_f32 v52, v56, v57\n"
"v_cvt_pk_bf16_f32 v53, v58, v59\n"
"v_cvt_pk_bf16_f32 v54, v60, v61\n"
"v_cvt_pk_bf16_f32 v55, v62, v63\n"
"v_mfma_f32_32x32x16_bf16 v[232:247], v[160:163], v[16:19], 0\n"
"v_mfma_f32_32x32x16_bf16 v[232:247], v[164:167], v[20:23], v[232:247]\n"
"s_add_u32 m0, s68, 49152\n"
"s_add_u32 s75, s67, 147456\n"
"s_nop 0\n"
"buffer_load_dwordx4 v1, s[40:43], s75 offen lds\n"
"s_add_u32 m0, s68, 57344\n"
"s_add_u32 s75, s67, 147584\n"
"s_nop 0\n"
"buffer_load_dwordx4 v1, s[40:43], s75 offen lds\n"
"s_add_u32 m0, s68, 65536\n"
"s_add_u32 s75, s67, 147712\n"
"s_nop 0\n"
"buffer_load_dwordx4 v1, s[40:43], s75 offen lds\n"
"v_pk_fma_f32 v[64:65], v[64:65], v[250:251], v[248:249]\n"
"v_pk_fma_f32 v[66:67], v[66:67], v[250:251], v[248:249]\n"
"v_pk_fma_f32 v[68:69], v[68:69], v[250:251], v[248:249]\n"
"v_pk_fma_f32 v[70:71], v[70:71], v[250:251], v[248:249]\n"
"v_pk_fma_f32 v[72:73], v[72:73], v[250:251], v[248:249]\n"
"v_pk_fma_f32 v[74:75], v[74:75], v[250:251], v[248:249]\n"
"v_pk_fma_f32 v[76:77], v[76:77], v[250:251], v[248:249]\n"
"v_pk_fma_f32 v[78:79], v[78:79], v[250:251], v[248:249]\n"
"v_exp_f32 v64, v64\n"
"v_exp_f32 v65, v65\n"
"v_exp_f32 v66, v66\n"
"v_exp_f32 v67, v67\n"
"v_exp_f32 v68, v68\n"
"v_exp_f32 v69, v69\n"
"v_exp_f32 v70, v70\n"
"v_exp_f32 v71, v71\n"
"v_exp_f32 v72, v72\n"
"v_exp_f32 v73, v73\n"
"v_exp_f32 v74, v74\n"
"v_exp_f32 v75, v75\n"
"v_exp_f32 v76, v76\n"
"v_exp_f32 v77, v77\n"
"v_exp_f32 v78, v78\n"
"v_exp_f32 v79, v79\n"
"v_cvt_pk_bf16_f32 v64, v64, v65\n"
"v_cvt_pk_bf16_f32 v65, v66, v67\n"
"v_cvt_pk_bf16_f32 v66, v68, v69\n"
"v_cvt_pk_bf16_f32 v67, v70, v71\n"
"v_cvt_pk_bf16_f32 v68, v72, v73\n"
"v_cvt_pk_bf16_f32 v69, v74, v75\n"
"v_cvt_pk_bf16_f32 v70, v76, v77\n"
"v_cvt_pk_bf16_f32 v71, v78, v79\n"
"v_mfma_f32_32x32x16_bf16 v[232:247], v[168:171], v[32:35], v[232:247]\n"
"v_mfma_f32_32x32x16_bf16 v[232:247], v[172:175], v[36:39], v[232:247]\n"
"s_add_u32 m0, s68, 73728\n"
"s_add_u32 s75, s67, 221184\n"
"s_nop 0\n"
"buffer_load_dwordx4 v1, s[40:43], s75 offen lds\n"
"s_add_u32 m0, s68, 81920\n"
"s_add_u32 s75, s67, 221312\n"
"s_nop 0\n"
"buffer_load_dwordx4 v1, s[40:43], s75 offen lds\n"
"s_add_u32 m0, s68, 90112\n"
"s_add_u32 s75, s67, 221440\n"
"s_nop 0\n"
"buffer_load_dwordx4 v1, s[40:43], s75 offen lds\n"
"v_pk_fma_f32 v[80:81], v[80:81], v[250:251], v[248:249]\n"
"v_pk_fma_f32 v[82:83], v[82:83], v[250:251], v[248:249]\n"
"v_pk_fma_f32 v[84:85], v[84:85], v[250:251], v[248:249]\n"
"v_pk_fma_f32 v[86:87], v[86:87], v[250:251], v[248:249]\n"
"v_pk_fma_f32 v[88:89], v[88:89], v[250:251], v[248:249]\n"
"v_pk_fma_f32 v[90:91], v[90:91], v[250:251], v[248:249]\n"
"v_pk_fma_f32 v[92:93], v[92:93], v[250:251], v[248:249]\n"
"v_pk_fma_f32 v[94:95], v[94:95], v[250:251], v[248:249]\n"
"v_exp_f32 v80, v80\n"
"v_exp_f32 v81, v81\n"
"v_exp_f32 v82, v82\n"
"v_exp_f32 v83, v83\n"
"v_exp_f32 v84, v84\n"
"v_exp_f32 v85, v85\n"
"v_exp_f32 v86, v86\n"
"v_exp_f32 v87, v87\n"
"v_exp_f32 v88, v88\n"
"v_exp_f32 v89, v89\n"
"v_exp_f32 v90, v90\n"
"v_exp_f32 v91, v91\n"
"v_exp_f32 v92, v92\n"
"v_exp_f32 v93, v93\n"
"v_exp_f32 v94, v94\n"
"v_exp_f32 v95, v95\n"
"v_cvt_pk_bf16_f32 v80, v80, v81\n"
"v_cvt_pk_bf16_f32 v81, v82, v83\n"
"v_cvt_pk_bf16_f32 v82, v84, v85\n"
"v_cvt_pk_bf16_f32 v83, v86, v87\n"
"v_cvt_pk_bf16_f32 v84, v88, v89\n"
"v_cvt_pk_bf16_f32 v85, v90, v91\n"
"v_cvt_pk_bf16_f32 v86, v92, v93\n"
"v_cvt_pk_bf16_f32 v87, v94, v95\n"
"v_mfma_f32_32x32x16_bf16 v[232:247], v[176:179], v[48:51], v[232:247]\n"
"v_mfma_f32_32x32x16_bf16 v[232:247], v[180:183], v[52:55], v[232:247]\n"
"s_add_u32 m0, s68, 98304\n"
"s_add_u32 s75, s67, 294912\n"
"s_nop 0\n"
"buffer_load_dwordx4 v1, s[40:43], s75 offen lds\n"
"s_add_u32 m0, s68, 106496\n"
"s_add_u32 s75, s67, 295040\n"
"s_nop 0\n"
"buffer_load_dwordx4 v1, s[40:43], s75 offen lds\n"
"s_add_u32 m0, s68, 114688\n"
"s_add_u32 s75, s67, 295168\n"
"s_nop 0\n"
"buffer_load_dwordx4 v1, s[40:43], s75 offen lds\n"
"v_pk_fma_f32 v[96:97], v[96:97], v[250:251], v[248:249]\n"
"v_pk_fma_f32 v[98:99], v[98:99], v[250:251], v[248:249]\n"
"v_pk_fma_f32 v[100:101], v[100:101], v[250:251], v[248:249]\n"
"v_pk_fma_f32 v[102:103], v[102:103], v[250:251], v[248:249]\n"
"v_pk_fma_f32 v[104:105], v[104:105], v[250:251], v[248:249]\n"
"v_pk_fma_f32 v[106:107], v[106:107], v[250:251], v[248:249]\n"
"v_pk_fma_f32 v[108:109], v[108:109], v[250:251], v[248:249]\n"
"v_pk_fma_f32 v[110:111], v[110:111], v[250:251], v[248:249]\n"
"v_exp_f32 v96, v96\n"
"v_exp_f32 v97, v97\n"
"v_exp_f32 v98, v98\n"
"v_exp_f32 v99, v99\n"
"v_exp_f32 v100, v100\n"
"v_exp_f32 v101, v101\n"
"v_exp_f32 v102, v102\n"
"v_exp_f32 v103, v103\n"
"v_exp_f32 v104, v104\n"
"v_exp_f32 v105, v105\n"
"v_exp_f32 v106, v106\n"
"v_exp_f32 v107, v107\n"
"v_exp_f32 v108, v108\n"
"v_exp_f32 v109, v109\n"
"v_exp_f32 v110, v110\n"
"v_exp_f32 v111, v111\n"
"v_cvt_pk_bf16_f32 v96, v96, v97\n"
"v_cvt_pk_bf16_f32 v97, v98, v99\n"
"v_cvt_pk_bf16_f32 v98, v100, v101\n"
"v_cvt_pk_bf16_f32 v99, v102, v103\n"
"v_cvt_pk_bf16_f32 v100, v104, v105\n"
"v_cvt_pk_bf16_f32 v101, v106, v107\n"
"v_cvt_pk_bf16_f32 v102, v108, v109\n"
"v_cvt_pk_bf16_f32 v103, v110, v111\n"
"v_mfma_f32_32x32x16_bf16 v[232:247], v[184:187], v[64:67], v[232:247]\n"
"v_mfma_f32_32x32x16_bf16 v[232:247], v[188:191], v[68:71], v[232:247]\n"
"s_add_u32 m0, s68, 122880\n"
"s_add_u32 s75, s67, 368640\n"
"s_nop 0\n"
"buffer_load_dwordx4 v1, s[40:43], s75 offen lds\n"
"s_add_u32 m0, s68, 131072\n"
"s_add_u32 s75, s67, 368768\n"
"s_nop 0\n"
"buffer_load_dwordx4 v1, s[40:43], s75 offen lds\n"
"s_add_u32 m0, s68, 139264\n"
"s_add_u32 s75, s67, 368896\n"
"s_nop 0\n"
"buffer_load_dwordx4 v1, s[40:43], s75 offen lds\n"
"v_pk_fma_f32 v[112:113], v[112:113], v[250:251], v[248:249]\n"
"v_pk_fma_f32 v[114:115], v[114:115], v[250:251], v[248:249]\n"
"v_pk_fma_f32 v[116:117], v[116:117], v[250:251], v[248:249]\n"
"v_pk_fma_f32 v[118:119], v[118:119], v[250:251], v[248:249]\n"
"v_pk_fma_f32 v[120:121], v[120:121], v[250:251], v[248:249]\n"
"v_pk_fma_f32 v[122:123], v[122:123], v[250:251], v[248:249]\n"
"v_pk_fma_f32 v[124:125], v[124:125], v[250:251], v[248:249]\n"
"v_pk_fma_f32 v[126:127], v[126:127], v[250:251], v[248:249]\n"
"v_exp_f32 v112, v112\n"
"v_exp_f32 v113, v113\n"
"v_exp_f32 v114, v114\n"
"v_exp_f32 v115, v115\n"
"v_exp_f32 v116, v116\n"
"v_exp_f32 v117, v117\n"
"v_exp_f32 v118, v118\n"
"v_exp_f32 v119, v119\n"
"v_exp_f32 v120, v120\n"
"v_exp_f32 v121, v121\n"
"v_exp_f32 v122, v122\n"
"v_exp_f32 v123, v123\n"
"v_exp_f32 v124, v124\n"
"v_exp_f32 v125, v125\n"
"v_exp_f32 v126, v126\n"
"v_exp_f32 v127, v127\n"
"v_cvt_pk_bf16_f32 v112, v112, v113\n"
"v_cvt_pk_bf16_f32 v113, v114, v115\n"
"v_cvt_pk_bf16_f32 v114, v116, v117\n"
"v_cvt_pk_bf16_f32 v115, v118, v119\n"
"v_cvt_pk_bf16_f32 v116, v120, v121\n"
"v_cvt_pk_bf16_f32 v117, v122, v123\n"
"v_cvt_pk_bf16_f32 v118, v124, v125\n"
"v_cvt_pk_bf16_f32 v119, v126, v127\n"
"v_mfma_f32_32x32x16_bf16 v[232:247], v[192:195], v[80:83], v[232:247]\n"
"v_mfma_f32_32x32x16_bf16 v[232:247], v[196:199], v[84:87], v[232:247]\n"
"v_pk_fma_f32 v[128:129], v[128:129], v[250:251], v[248:249]\n"
"v_pk_fma_f32 v[130:131], v[130:131], v[250:251], v[248:249]\n"
"v_pk_fma_f32 v[132:133], v[132:133], v[250:251], v[248:249]\n"
"v_pk_fma_f32 v[134:135], v[134:135], v[250:251], v[248:249]\n"
"v_pk_fma_f32 v[136:137], v[136:137], v[250:251], v[248:249]\n"
"v_pk_fma_f32 v[138:139], v[138:139], v[250:251], v[248:249]\n"
"v_pk_fma_f32 v[140:141], v[140:141], v[250:251], v[248:249]\n"
"v_pk_fma_f32 v[142:143], v[142:143], v[250:251], v[248:249]\n"
"v_exp_f32 v128, v128\n"
"v_exp_f32 v129, v129\n"
"v_exp_f32 v130, v130\n"
"v_exp_f32 v131, v131\n"
"v_exp_f32 v132, v132\n"
"v_exp_f32 v133, v133\n"
"v_exp_f32 v134, v134\n"
"v_exp_f32 v135, v135\n"
"v_exp_f32 v136, v136\n"
"v_exp_f32 v137, v137\n"
"v_exp_f32 v138, v138\n"
"v_exp_f32 v139, v139\n"
"v_exp_f32 v140, v140\n"
"v_exp_f32 v141, v141\n"
"v_exp_f32 v142, v142\n"
"v_exp_f32 v143, v143\n"
"v_cvt_pk_bf16_f32 v128, v128, v129\n"
"v_cvt_pk_bf16_f32 v129, v130, v131\n"
"v_cvt_pk_bf16_f32 v130, v132, v133\n"
"v_cvt_pk_bf16_f32 v131, v134, v135\n"
"v_cvt_pk_bf16_f32 v132, v136, v137\n"
"v_cvt_pk_bf16_f32 v133, v138, v139\n"
"v_cvt_pk_bf16_f32 v134, v140, v141\n"
"v_cvt_pk_bf16_f32 v135, v142, v143\n"
"v_mfma_f32_32x32x16_bf16 v[232:247], v[200:203], v[96:99], v[232:247]\n"
"v_mfma_f32_32x32x16_bf16 v[232:247], v[204:207], v[100:103], v[232:247]\n"
"v_pk_fma_f32 v[144:145], v[144:145], v[250:251], v[248:249]\n"
"v_pk_fma_f32 v[146:147], v[146:147], v[250:251], v[248:249]\n"
"v_pk_fma_f32 v[148:149], v[148:149], v[250:251], v[248:249]\n"
"v_pk_fma_f32 v[150:151], v[150:151], v[250:251], v[248:249]\n"
"v_pk_fma_f32 v[152:153], v[152:153], v[250:251], v[248:249]\n"
"v_pk_fma_f32 v[154:155], v[154:155], v[250:251], v[248:249]\n"
"v_pk_fma_f32 v[156:157], v[156:157], v[250:251], v[248:249]\n"
"v_pk_fma_f32 v[158:159], v[158:159], v[250:251], v[248:249]\n"
"v_exp_f32 v144, v144\n"
"v_exp_f32 v145, v145\n"
"v_exp_f32 v146, v146\n"
"v_exp_f32 v147, v147\n"
"v_exp_f32 v148, v148\n"
"v_exp_f32 v149, v149\n"
"v_exp_f32 v150, v150\n"
"v_exp_f32 v151, v151\n"
"v_exp_f32 v152, v152\n"
"v_exp_f32 v153, v153\n"
"v_exp_f32 v154, v154\n"
"v_exp_f32 v155, v155\n"
"v_exp_f32 v156, v156\n"
"v_exp_f32 v157, v157\n"
"v_exp_f32 v158, v158\n"
"v_exp_f32 v159, v159\n"
"v_cvt_pk_bf16_f32 v144, v144, v145\n"
"v_cvt_pk_bf16_f32 v145, v146, v147\n"
"v_cvt_pk_bf16_f32 v146, v148, v149\n"
"v_cvt_pk_bf16_f32 v147, v150, v151\n"
"v_cvt_pk_bf16_f32 v148, v152, v153\n"
"v_cvt_pk_bf16_f32 v149, v154, v155\n"
"v_cvt_pk_bf16_f32 v150, v156, v157\n"
"v_cvt_pk_bf16_f32 v151, v158, v159\n"
"v_mfma_f32_32x32x16_bf16 v[232:247], v[208:211], v[112:115], v[232:247]\n"
"v_mfma_f32_32x32x16_bf16 v[232:247], v[212:215], v[116:119], v[232:247]\n"
"s_nop 1\n"
"v_mfma_f32_32x32x16_bf16 v[232:247], v[216:219], v[128:131], v[232:247]\n"
"v_mfma_f32_32x32x16_bf16 v[232:247], v[220:223], v[132:135], v[232:247]\n"
"v_mfma_f32_32x32x16_bf16 v[232:247], v[224:227], v[144:147], v[232:247]\n"
"v_mfma_f32_32x32x16_bf16 v[232:247], v[228:231], v[148:151], v[232:247]\n"
"s_nop 7\n"
"s_cmp_eq_u32 s57, 0\n"
"s_cbranch_scc0 .Lkc_nh_10_%=\n"
"s_cmp_lt_u32 0, s59\n"
"s_cselect_b32 s61, s61, 1073741824\n"
"s_add_u32 s76, s61, 0\n"
"buffer_load_dwordx4 v[112:115], v2, s[44:47], s76 offen\n"
"buffer_load_dwordx4 v[116:119], v2, s[44:47], s76 offen offset:1024\n"
"buffer_load_dwordx4 v[120:123], v2, s[44:47], s76 offen offset:2048\n"
"buffer_load_dwordx4 v[124:127], v2, s[44:47], s76 offen offset:3072\n"
"s_add_u32 s76, s61, 4096\n"
"buffer_load_dwordx4 v[128:131], v2, s[44:47], s76 offen\n"
"buffer_load_dwordx4 v[132:135], v2, s[44:47], s76 offen offset:1024\n"
"buffer_load_dwordx4 v[136:139], v2, s[44:47], s76 offen offset:2048\n"
"buffer_load_dwordx4 v[140:143], v2, s[44:47], s76 offen offset:3072\n"
"s_add_u32 s76, s61, 8192\n"
"buffer_load_dwordx4 v[144:147], v2, s[44:47], s76 offen\n"
"buffer_load_dwordx4 v[148:151], v2, s[44:47], s76 offen offset:1024\n"
"buffer_load_dwordx4 v[152:155], v2, s[44:47], s76 offen offset:2048\n"
"buffer_load_dwordx4 v[156:159], v2, s[44:47], s76 offen offset:3072\n"
"s_add_u32 s76, s61, 12288\n"
"buffer_load_dwordx4 v[160:163], v2, s[44:47], s76 offen\n"
"buffer_load_dwordx4 v[164:167], v2, s[44:47], s76 offen offset:1024\n"
"buffer_load_dwordx4 v[168:171], v2, s[44:47], s76 offen offset:2048\n"
"buffer_load_dwordx4 v[172:175], v2, s[44:47], s76 offen offset:3072\n"
"s_add_u32 s76, s61, 16384\n"
"buffer_load_dwordx4 v[176:179], v2, s[44:47], s76 offen\n"
"buffer_load_dwordx4 v[180:183], v2, s[44:47], s76 offen offset:1024\n"
"buffer_load_dwordx4 v[184:187], v2, s[44:47], s76 offen offset:2048\n"
"buffer_load_dwordx4 v[188:191], v2, s[44:47], s76 offen offset:3072\n"
".Lkc_nh_10_%=:\n"
"s_mul_i32 s70, s55, 18\n"
"v_lshrrev_b32 v213, 5, v0\n"
"v_lshlrev_b32 v213, 2, v213\n"
"v_add_u32 v213, s70, v213\n"
"v_and_b32 v214, 31, v0\n"
"v_lshlrev_b32 v214, 4, v214\n"
"v_add_u32 v214, 147456, v214\n"
"v_add_u32 v252, 0, v213\n"
"v_lshrrev_b32 v208, 3, v252\n"
"v_and_b32 v253, 7, v252\n"
"v_lshl_add_u32 v208, v208, 9, v214\n"
"v_lshl_add_u32 v208, v253, 1, v208\n"
"v_add_u32 v252, 2, v213\n"
"v_lshrrev_b32 v209, 3, v252\n"
"v_and_b32 v253, 7, v252\n"
"v_lshl_add_u32 v209, v209, 9, v214\n"
"v_lshl_add_u32 v209, v253, 1, v209\n"
"v_add_u32 v252, 8, v213\n"
"v_lshrrev_b32 v210, 3, v252\n"
"v_and_b32 v253, 7, v252\n"
"v_lshl_add_u32 v210, v210, 9, v214\n"
"v_lshl_add_u32 v210, v253, 1, v210\n"
"v_add_u32 v252, 10, v213\n"
"v_lshrrev_b32 v211, 3, v252\n"
"v_and_b32 v253, 7, v252\n"
"v_lshl_add_u32 v211, v211, 9, v214\n"
"v_lshl_add_u32 v211, v253, 1, v211\n"
"v_add_u32 v252, 16, v213\n"
"v_lshrrev_b32 v212, 3, v252\n"
"v_and_b32 v253, 7, v252\n"
"v_lshl_add_u32 v212, v212, 9, v214\n"
"v_lshl_add_u32 v212, v253, 1, v212\n"
"v_mov_b32 v252, v242\n"
"v_mov_b32 v253, v242\n"
"s_nop 1\n"
"v_permlane32_swap_b32 v252, v253\n"
"s_nop 1\n"
"v_rcp_f32 v252, v252\n"
"s_nop 0\n"
"v_mul_f32 v232, v232, v252\n"
"v_mul_f32 v233, v233, v252\n"
"v_mul_f32 v234, v234, v252\n"
"v_mul_f32 v235, v235, v252\n"
"v_mul_f32 v236, v236, v252\n"
"v_mul_f32 v237, v237, v252\n"
"v_mul_f32 v238, v238, v252\n"
"v_mul_f32 v239, v239, v252\n"
"v_mul_f32 v240, v240, v252\n"
"v_mul_f32 v241, v241, v252\n"
"v_cvt_pk_bf16_f32 v232, v232, v233\n"
"v_cvt_pk_bf16_f32 v234, v234, v235\n"
"v_cvt_pk_bf16_f32 v236, v236, v237\n"
"v_cvt_pk_bf16_f32 v238, v238, v239\n"
"v_cvt_pk_bf16_f32 v240, v240, v241\n"
"ds_write_b32 v208, v232\n"
"ds_write_b32 v209, v234\n"
"ds_write_b32 v210, v236\n"
"ds_write_b32 v211, v238\n"
"s_mov_b32 exec_hi, 0\n"
"ds_write_b32 v212, v240\n"
"s_mov_b64 exec, -1\n"
"v_lshrrev_b32 v10, 5, v0\n"
"v_lshlrev_b32 v4, 9, v10\n"
"v_bfe_u32 v11, v0, 2, 2\n"
"v_lshl_add_u32 v4, v11, 7, v4\n"
"v_lshrrev_b32 v11, 1, v11\n"
"v_lshl_add_u32 v4, v11, 6, v4\n"
"v_bfe_u32 v11, v0, 4, 1\n"
"v_lshl_add_u32 v4, v11, 5, v4\n"
"v_and_b32 v11, 3, v0\n"
"v_lshl_add_u32 v4, v11, 3, v4\n"
"v_xor_b32 v5, 64, v4\n"
"v_add_u32 v6, 49152, v4\n"
"v_add_u32 v7, 49152, v5\n"
"v_add_u32 v8, 98304, v4\n"
"v_add_u32 v9, 98304, v5\n"
"s_waitcnt vmcnt(12) lgkmcnt(0)\n"
"s_barrier\n"
"s_lshl_b32 s70, s55, 10\n"
"v_add_u32 v252, s70, v2\n"
"v_add_u32 v253, 147456, v252\n"
"ds_read_b128 v[232:235], v253\n"
"s_mul_i32 s71, %[qb], 36\n"
"s_mul_i32 s72, %[hg], 9\n"
"s_add_u32 s71, s71, s72\n"
"s_lshl_b32 s71, s71, 10\n"
"s_add_u32 s71, s71, 25952256\n"
"s_add_u32 s72, s71, 8192\n"
"s_cmp_eq_u32 s55, 4\n"
"s_cselect_b32 s72, s72, 1073741824\n"
"v_add_u32 v253, 155648, v2\n"
"ds_read_b128 v[236:239], v253\n"
"s_waitcnt lgkmcnt(0)\n"
"s_cmp_eq_u32 s57, 0\n"
"s_cbranch_scc0 .Lkc_lrole_12_%=\n"
"buffer_store_dwordx4 v[232:235], v252, s[44:47], s71 offen sc1\n"
"buffer_store_dwordx4 v[236:239], v2, s[44:47], s72 offen sc1\n"
"s_cmp_eq_u32 s59, 0\n"
"s_cbranch_scc1 .Lkc_gidle_15_%=\n"
"v_mov_b32 v16, 0\n"
"v_mov_b32 v17, 0\n"
"v_mov_b32 v18, 0\n"
"v_mov_b32 v19, 0\n"
"v_mov_b32 v20, 0\n"
"v_mov_b32 v21, 0\n"
"v_mov_b32 v22, 0\n"
"v_mov_b32 v23, 0\n"
"v_mov_b32 v24, 0\n"
"v_mov_b32 v25, 0\n"
"v_mov_b32 v26, 0\n"
"v_mov_b32 v27, 0\n"
"v_mov_b32 v28, 0\n"
"v_mov_b32 v29, 0\n"
"v_mov_b32 v30, 0\n"
"v_mov_b32 v31, 0\n"
"v_mov_b32 v32, 0\n"
"v_mov_b32 v33, 0\n"
"v_mov_b32 v34, 0\n"
"v_mov_b32 v35, 0\n"
"v_mov_b32 v36, 0\n"
"v_mov_b32 v37, 0\n"
"v_mov_b32 v38, 0\n"
"v_mov_b32 v39, 0\n"
"v_mov_b32 v40, 0\n"
"v_mov_b32 v41, 0\n"
"v_mov_b32 v42, 0\n"
"v_mov_b32 v43, 0\n"
"v_mov_b32 v44, 0\n"
"v_mov_b32 v45, 0\n"
"v_mov_b32 v46, 0\n"
"v_mov_b32 v47, 0\n"
"v_mov_b32 v48, 0\n"
"v_mov_b32 v49, 0\n"
"v_mov_b32 v50, 0\n"
"v_mov_b32 v51, 0\n"
"v_mov_b32 v52, 0\n"
"v_mov_b32 v53, 0\n"
"v_mov_b32 v54, 0\n"
"v_mov_b32 v55, 0\n"
"v_mov_b32 v56, 0\n"
"v_mov_b32 v57, 0\n"
"v_mov_b32 v58, 0\n"
"v_mov_b32 v59, 0\n"
"v_mov_b32 v60, 0\n"
"v_mov_b32 v61, 0\n"
"v_mov_b32 v62, 0\n"
"v_mov_b32 v63, 0\n"
"v_mov_b32 v64, 0\n"
"v_mov_b32 v65, 0\n"
"v_mov_b32 v66, 0\n"
"v_mov_b32 v67, 0\n"
"v_mov_b32 v68, 0\n"
"v_mov_b32 v69, 0\n"
"v_mov_b32 v70, 0\n"
"v_mov_b32 v71, 0\n"
"v_mov_b32 v72, 0\n"
"v_mov_b32 v73, 0\n"
"v_mov_b32 v74, 0\n"
"v_mov_b32 v75, 0\n"
"v_mov_b32 v76, 0\n"
"v_mov_b32 v77, 0\n"
"v_mov_b32 v78, 0\n"
"v_mov_b32 v79, 0\n"
"v_mov_b32 v80, 0\n"
"v_mov_b32 v81, 0\n"
"v_mov_b32 v82, 0\n"
"v_mov_b32 v83, 0\n"
"v_mov_b32 v84, 0\n"
"v_mov_b32 v85, 0\n"
"v_mov_b32 v86, 0\n"
"v_mov_b32 v87, 0\n"
"v_mov_b32 v88, 0\n"
"v_mov_b32 v89, 0\n"
"v_mov_b32 v90, 0\n"
"v_mov_b32 v91, 0\n"
"v_mov_b32 v92, 0\n"
"v_mov_b32 v93, 0\n"
"v_mov_b32 v94, 0\n"
"v_mov_b32 v95, 0\n"
"v_mov_b32 v96, 0\n"
"v_mov_b32 v97, 0\n"
"v_mov_b32 v98, 0\n"
"v_mov_b32 v99, 0\n"
"v_mov_b32 v100, 0\n"
"v_mov_b32 v101, 0\n"
"v_mov_b32 v102, 0\n"
"v_mov_b32 v103, 0\n"
"v_mov_b32 v104, 0\n"
"v_mov_b32 v105, 0\n"
"v_mov_b32 v106, 0\n"
"v_mov_b32 v107, 0\n"
"v_mov_b32 v108, 0\n"
"v_mov_b32 v109, 0\n"
"v_mov_b32 v110, 0\n"
"v_mov_b32 v111, 0\n"
"s_cmp_eq_u32 s58, 0\n"
"s_cbranch_scc0 .Lkc_ent1_16_%=\n"
"ds_read_b64_tr_b16 v[208:209], v4 offset:0\n"
"ds_read_b64_tr_b16 v[210:211], v4 offset:1024\n"
"ds_read_b64_tr_b16 v[212:213], v5 offset:0\n"
"ds_read_b64_tr_b16 v[214:215], v5 offset:1024\n"
"ds_read_b64_tr_b16 v[216:217], v4 offset:8192\n"
"ds_read_b64_tr_b16 v[218:219], v4 offset:9216\n"
"ds_read_b64_tr_b16 v[220:221], v5 offset:8192\n"
"ds_read_b64_tr_b16 v[222:223], v5 offset:9216\n"
"ds_read_b64_tr_b16 v[224:225], v4 offset:16384\n"
"ds_read_b64_tr_b16 v[226:227], v4 offset:17408\n"
"ds_read_b64_tr_b16 v[228:229], v5 offset:16384\n"
"ds_read_b64_tr_b16 v[230:231], v5 offset:17408\n"
"s_waitcnt vmcnt(18)\n"
"s_waitcnt lgkmcnt(10)\n"
"v_mfma_f32_32x32x16_bf16 v[16:31], v[208:211], v[112:115], v[16:31]\n"
"ds_read_b64_tr_b16 v[208:209], v4 offset:2048\n"
"ds_read_b64_tr_b16 v[210:211], v4 offset:3072\n"
"s_waitcnt lgkmcnt(10)\n"
"v_mfma_f32_32x32x16_bf16 v[32:47], v[212:215], v[112:115], v[32:47]\n"
"ds_read_b64_tr_b16 v[212:213], v5 offset:2048\n"
"ds_read_b64_tr_b16 v[214:215], v5 offset:3072\n"
"s_waitcnt lgkmcnt(10)\n"
"v_mfma_f32_32x32x16_bf16 v[48:63], v[216:219], v[112:115], v[48:63]\n"
"ds_read_b64_tr_b16 v[216:217], v4 offset:10240\n"
"ds_read_b64_tr_b16 v[218:219], v4 offset:11264\n"
"s_add_u32 s76, s61, 20480\n"
"buffer_load_dwordx4 v[192:195], v2, s[44:47], s76 offen\n"
"s_waitcnt lgkmcnt(10)\n"
"v_mfma_f32_32x32x16_bf16 v[64:79], v[220:223], v[112:115], v[64:79]\n"
"ds_read_b64_tr_b16 v[220:221], v5 offset:10240\n"
"ds_read_b64_tr_b16 v[222:223], v5 offset:11264\n"
"s_waitcnt lgkmcnt(10)\n"
"v_mfma_f32_32x32x16_bf16 v[80:95], v[224:227], v[112:115], v[80:95]\n"
"ds_read_b64_tr_b16 v[224:225], v4 offset:18432\n"
"ds_read_b64_tr_b16 v[226:227], v4 offset:19456\n"
"s_waitcnt lgkmcnt(10)\n"
"v_mfma_f32_32x32x16_bf16 v[96:111], v[228:231], v[112:115], v[96:111]\n"
"ds_read_b64_tr_b16 v[228:229], v5 offset:18432\n"
"ds_read_b64_tr_b16 v[230:231], v5 offset:19456\n"
"buffer_load_dwordx4 v[196:199], v2, s[44:47], s76 offen offset:1024\n"
"s_waitcnt lgkmcnt(10)\n"
"v_mfma_f32_32x32x16_bf16 v[16:31], v[208:211], v[116:119], v[16:31]\n"
"ds_read_b64_tr_b16 v[208:209], v4 offset:4096\n"
"ds_read_b64_tr_b16 v[210:211], v4 offset:5120\n"
"s_waitcnt lgkmcnt(10)\n"
"v_mfma_f32_32x32x16_bf16 v[32:47], v[212:215], v[116:119], v[32:47]\n"
"ds_read_b64_tr_b16 v[212:213], v5 offset:4096\n"
"ds_read_b64_tr_b16 v[214:215], v5 offset:5120\n"
"s_waitcnt lgkmcnt(10)\n"
"v_mfma_f32_32x32x16_bf16 v[48:63], v[216:219], v[116:119], v[48:63]\n"
"ds_read_b64_tr_b16 v[216:217], v4 offset:12288\n"
"ds_read_b64_tr_b16 v[218:219], v4 offset:13312\n"
"buffer_load_dwordx4 v[200:203], v2, s[44:47], s76 offen offset:2048\n"
"s_waitcnt lgkmcnt(10)\n"
"v_mfma_f32_32x32x16_bf16 v[64:79], v[220:223], v[116:119], v[64:79]\n"
"ds_read_b64_tr_b16 v[220:221], v5 offset:12288\n"
"ds_read_b64_tr_b16 v[222:223], v5 offset:13312\n"
"s_waitcnt lgkmcnt(10)\n"
"v_mfma_f32_32x32x16_bf16 v[80:95], v[224:227], v[116:119], v[80:95]\n"
"ds_read_b64_tr_b16 v[224:225], v4 offset:20480\n"
"ds_read_b64_tr_b16 v[226:227], v4 offset:21504\n"
"s_waitcnt lgkmcnt(10)\n"
"v_mfma_f32_32x32x16_bf16 v[96:111], v[228:231], v[116:119], v[96:111]\n"
"ds_read_b64_tr_b16 v[228:229], v5 offset:20480\n"
"ds_read_b64_tr_b16 v[230:231], v5 offset:21504\n"
"buffer_load_dwordx4 v[204:207], v2, s[44:47], s76 offen offset:3072\n"
"s_waitcnt lgkmcnt(10)\n"
"v_mfma_f32_32x32x16_bf16 v[16:31], v[208:211], v[120:123], v[16:31]\n"
"ds_read_b64_tr_b16 v[208:209], v4 offset:6144\n"
"ds_read_b64_tr_b16 v[210:211], v4 offset:7168\n"
"s_waitcnt lgkmcnt(10)\n"
"v_mfma_f32_32x32x16_bf16 v[32:47], v[212:215], v[120:123], v[32:47]\n"
"ds_read_b64_tr_b16 v[212:213], v5 offset:6144\n"
"ds_read_b64_tr_b16 v[214:215], v5 offset:7168\n"
"s_waitcnt lgkmcnt(10)\n"
"v_mfma_f32_32x32x16_bf16 v[48:63], v[216:219], v[120:123], v[48:63]\n"
"ds_read_b64_tr_b16 v[216:217], v4 offset:14336\n"
"ds_read_b64_tr_b16 v[218:219], v4 offset:15360\n"
"s_waitcnt lgkmcnt(10)\n"
"v_mfma_f32_32x32x16_bf16 v[64:79], v[220:223], v[120:123], v[64:79]\n"
"ds_read_b64_tr_b16 v[220:221], v5 offset:14336\n"
"ds_read_b64_tr_b16 v[222:223], v5 offset:15360\n"
"s_waitcnt lgkmcnt(10)\n"
"v_mfma_f32_32x32x16_bf16 v[80:95], v[224:227], v[120:123], v[80:95]\n"
"ds_read_b64_tr_b16 v[224:225], v4 offset:22528\n"
"ds_read_b64_tr_b16 v[226:227], v4 offset:23552\n"
"s_waitcnt lgkmcnt(10)\n"
"v_mfma_f32_32x32x16_bf16 v[96:111], v[228:231], v[120:123], v[96:111]\n"
"ds_read_b64_tr_b16 v[228:229], v5 offset:22528\n"
"ds_read_b64_tr_b16 v[230:231], v5 offset:23552\n"
"s_waitcnt lgkmcnt(10)\n"
"v_mfma_f32_32x32x16_bf16 v[16:31], v[208:211], v[124:127], v[16:31]\n"
"s_waitcnt vmcnt(38)\n"
"s_barrier\n"
"s_waitcnt lgkmcnt(8)\n"
"v_mfma_f32_32x32x16_bf16 v[32:47], v[212:215], v[124:127], v[32:47]\n"
"ds_read_b64_tr_b16 v[208:209], v4 offset:24576\n"
"ds_read_b64_tr_b16 v[210:211], v4 offset:25600\n"
"ds_read_b64_tr_b16 v[212:213], v5 offset:24576\n"
"ds_read_b64_tr_b16 v[214:215], v5 offset:25600\n"
"s_waitcnt lgkmcnt(10)\n"
"v_mfma_f32_32x32x16_bf16 v[48:63], v[216:219], v[124:127], v[48:63]\n"
"ds_read_b64_tr_b16 v[216:217], v4 offset:32768\n"
"ds_read_b64_tr_b16 v[218:219], v4 offset:33792\n"
"s_waitcnt lgkmcnt(10)\n"
"v_mfma_f32_32x32x16_bf16 v[64:79], v[220:223], v[124:127], v[64:79]\n"
"ds_read_b64_tr_b16 v[220:221], v5 offset:32768\n"
"ds_read_b64_tr_b16 v[222:223], v5 offset:33792\n"
"s_waitcnt lgkmcnt(10)\n"
"v_mfma_f32_32x32x16_bf16 v[80:95], v[224:227], v[124:127], v[80:95]\n"
"ds_read_b64_tr_b16 v[224:225], v4 offset:40960\n"
"ds_read_b64_tr_b16 v[226:227], v4 offset:41984\n"
"s_waitcnt lgkmcnt(10)\n"
"v_mfma_f32_32x32x16_bf16 v[96:111], v[228:231], v[124:127], v[96:111]\n"
"ds_read_b64_tr_b16 v[228:229], v5 offset:40960\n"
"ds_read_b64_tr_b16 v[230:231], v5 offset:41984\n"
"s_branch .Lkc_i1_17_%=\n"
".Lkc_ent1_16_%=:\n"
"s_add_u32 s76, s61, 20480\n"
"buffer_load_dwordx4 v[192:195], v2, s[44:47], s76 offen\n"
"buffer_load_dwordx4 v[196:199], v2, s[44:47], s76 offen offset:1024\n"
"buffer_load_dwordx4 v[200:203], v2, s[44:47], s76 offen offset:2048\n"
"buffer_load_dwordx4 v[204:207], v2, s[44:47], s76 offen offset:3072\n"
"s_barrier\n"
"ds_read_b64_tr_b16 v[208:209], v4 offset:24576\n"
"ds_read_b64_tr_b16 v[210:211], v4 offset:25600\n"
"ds_read_b64_tr_b16 v[212:213], v5 offset:24576\n"
"ds_read_b64_tr_b16 v[214:215], v5 offset:25600\n"
"ds_read_b64_tr_b16 v[216:217], v4 offset:32768\n"
"ds_read_b64_tr_b16 v[218:219], v4 offset:33792\n"
"ds_read_b64_tr_b16 v[220:221], v5 offset:32768\n"
"ds_read_b64_tr_b16 v[222:223], v5 offset:33792\n"
"ds_read_b64_tr_b16 v[224:225], v4 offset:40960\n"
"ds_read_b64_tr_b16 v[226:227], v4 offset:41984\n"
"ds_read_b64_tr_b16 v[228:229], v5 offset:40960\n"
"ds_read_b64_tr_b16 v[230:231], v5 offset:41984\n"
".Lkc_i1_17_%=:\n"
"s_waitcnt vmcnt(18)\n"
"s_waitcnt lgkmcnt(10)\n"
"v_mfma_f32_32x32x16_bf16 v[16:31], v[208:211], v[128:131], v[16:31]\n"
"ds_read_b64_tr_b16 v[208:209], v4 offset:26624\n"
"ds_read_b64_tr_b16 v[210:211], v4 offset:27648\n"
"s_waitcnt lgkmcnt(10)\n"
"v_mfma_f32_32x32x16_bf16 v[32:47], v[212:215], v[128:131], v[32:47]\n"
"ds_read_b64_tr_b16 v[212:213], v5 offset:26624\n"
"ds_read_b64_tr_b16 v[214:215], v5 offset:27648\n"
"s_waitcnt lgkmcnt(10)\n"
"v_mfma_f32_32x32x16_bf16 v[48:63], v[216:219], v[128:131], v[48:63]\n"
"ds_read_b64_tr_b16 v[216:217], v4 offset:34816\n"
"ds_read_b64_tr_b16 v[218:219], v4 offset:35840\n"
"s_add_u32 s76, s61, 24576\n"
"buffer_load_dwordx4 v[112:115], v2, s[44:47], s76 offen\n"
"s_waitcnt lgkmcnt(10)\n"
"v_mfma_f32_32x32x16_bf16 v[64:79], v[220:223], v[128:131], v[64:79]\n"
"ds_read_b64_tr_b16 v[220:221], v5 offset:34816\n"
"ds_read_b64_tr_b16 v[222:223], v5 offset:35840\n"
"s_waitcnt lgkmcnt(10)\n"
"v_mfma_f32_32x32x16_bf16 v[80:95], v[224:227], v[128:131], v[80:95]\n"
"ds_read_b64_tr_b16 v[224:225], v4 offset:43008\n"
"ds_read_b64_tr_b16 v[226:227], v4 offset:44032\n"
"s_waitcnt lgkmcnt(10)\n"
"v_mfma_f32_32x32x16_bf16 v[96:111], v[228:231], v[128:131], v[96:111]\n"
"ds_read_b64_tr_b16 v[228:229], v5 offset:43008\n"
"ds_read_b64_tr_b16 v[230:231], v5 offset:44032\n"
"buffer_load_dwordx4 v[116:119], v2, s[44:47], s76 offen offset:1024\n"
"s_waitcnt lgkmcnt(10)\n"
"v_mfma_f32_32x32x16_bf16 v[16:31], v[208:211], v[132:135], v[16:31]\n"
"ds_read_b64_tr_b16 v[208:209], v4 offset:28672\n"
"ds_read_b64_tr_b16 v[210:211], v4 offset:29696\n"
"s_waitcnt lgkmcnt(10)\n"
"v_mfma_f32_32x32x16_bf16 v[32:47], v[212:215], v[132:135], v[32:47]\n"
"ds_read_b64_tr_b16 v[212:213], v5 offset:28672\n"
"ds_read_b64_tr_b16 v[214:215], v5 offset:29696\n"
"s_waitcnt lgkmcnt(10)\n"
"v_mfma_f32_32x32x16_bf16 v[48:63], v[216:219], v[132:135], v[48:63]\n"
"ds_read_b64_tr_b16 v[216:217], v4 offset:36864\n"
"ds_read_b64_tr_b16 v[218:219], v4 offset:37888\n"
"buffer_load_dwordx4 v[120:123], v2, s[44:47], s76 offen offset:2048\n"
"s_waitcnt lgkmcnt(10)\n"
"v_mfma_f32_32x32x16_bf16 v[64:79], v[220:223], v[132:135], v[64:79]\n"
"ds_read_b64_tr_b16 v[220:221], v5 offset:36864\n"
"ds_read_b64_tr_b16 v[222:223], v5 offset:37888\n"
"s_waitcnt lgkmcnt(10)\n"
"v_mfma_f32_32x32x16_bf16 v[80:95], v[224:227], v[132:135], v[80:95]\n"
"ds_read_b64_tr_b16 v[224:225], v4 offset:45056\n"
"ds_read_b64_tr_b16 v[226:227], v4 offset:46080\n"
"s_waitcnt lgkmcnt(10)\n"
"v_mfma_f32_32x32x16_bf16 v[96:111], v[228:231], v[132:135], v[96:111]\n"
"ds_read_b64_tr_b16 v[228:229], v5 offset:45056\n"
"ds_read_b64_tr_b16 v[230:231], v5 offset:46080\n"
"buffer_load_dwordx4 v[124:127], v2, s[44:47], s76 offen offset:3072\n"
"s_waitcnt lgkmcnt(10)\n"
"v_mfma_f32_32x32x16_bf16 v[16:31], v[208:211], v[136:139], v[16:31]\n"
"ds_read_b64_tr_b16 v[208:209], v4 offset:30720\n"
"ds_read_b64_tr_b16 v[210:211], v4 offset:31744\n"
"s_waitcnt lgkmcnt(10)\n"
"v_mfma_f32_32x32x16_bf16 v[32:47], v[212:215], v[136:139], v[32:47]\n"
"ds_read_b64_tr_b16 v[212:213], v5 offset:30720\n"
"ds_read_b64_tr_b16 v[214:215], v5 offset:31744\n"
"s_waitcnt lgkmcnt(10)\n"
"v_mfma_f32_32x32x16_bf16 v[48:63], v[216:219], v[136:139], v[48:63]\n"
"ds_read_b64_tr_b16 v[216:217], v4 offset:38912\n"
"ds_read_b64_tr_b16 v[218:219], v4 offset:39936\n"
"s_waitcnt lgkmcnt(10)\n"
"v_mfma_f32_32x32x16_bf16 v[64:79], v[220:223], v[136:139], v[64:79]\n"
"ds_read_b64_tr_b16 v[220:221], v5 offset:38912\n"
"ds_read_b64_tr_b16 v[222:223], v5 offset:39936\n"
"s_waitcnt lgkmcnt(10)\n"
"v_mfma_f32_32x32x16_bf16 v[80:95], v[224:227], v[136:139], v[80:95]\n"
"ds_read_b64_tr_b16 v[224:225], v4 offset:47104\n"
"ds_read_b64_tr_b16 v[226:227], v4 offset:48128\n"
"s_waitcnt lgkmcnt(10)\n"
"v_mfma_f32_32x32x16_bf16 v[96:111], v[228:231], v[136:139], v[96:111]\n"
"ds_read_b64_tr_b16 v[228:229], v5 offset:47104\n"
"ds_read_b64_tr_b16 v[230:231], v5 offset:48128\n"
"s_waitcnt lgkmcnt(10)\n"
"v_mfma_f32_32x32x16_bf16 v[16:31], v[208:211], v[140:143], v[16:31]\n"
"s_waitcnt vmcnt(39)\n"
"s_barrier\n"
"s_waitcnt lgkmcnt(8)\n"
"v_mfma_f32_32x32x16_bf16 v[32:47], v[212:215], v[140:143], v[32:47]\n"
"ds_read_b64_tr_b16 v[208:209], v6 offset:0\n"
"ds_read_b64_tr_b16 v[210:211], v6 offset:1024\n"
"ds_read_b64_tr_b16 v[212:213], v7 offset:0\n"
"ds_read_b64_tr_b16 v[214:215], v7 offset:1024\n"
"s_waitcnt lgkmcnt(10)\n"
"v_mfma_f32_32x32x16_bf16 v[48:63], v[216:219], v[140:143], v[48:63]\n"
"ds_read_b64_tr_b16 v[216:217], v6 offset:8192\n"
"ds_read_b64_tr_b16 v[218:219], v6 offset:9216\n"
"s_waitcnt lgkmcnt(10)\n"
"v_mfma_f32_32x32x16_bf16 v[64:79], v[220:223], v[140:143], v[64:79]\n"
"ds_read_b64_tr_b16 v[220:221], v7 offset:8192\n"
"ds_read_b64_tr_b16 v[222:223], v7 offset:9216\n"
"s_waitcnt lgkmcnt(10)\n"
"v_mfma_f32_32x32x16_bf16 v[80:95], v[224:227], v[140:143], v[80:95]\n"
"ds_read_b64_tr_b16 v[224:225], v6 offset:16384\n"
"ds_read_b64_tr_b16 v[226:227], v6 offset:17408\n"
"s_waitcnt lgkmcnt(10)\n"
"v_mfma_f32_32x32x16_bf16 v[96:111], v[228:231], v[140:143], v[96:111]\n"
"ds_read_b64_tr_b16 v[228:229], v7 offset:16384\n"
"ds_read_b64_tr_b16 v[230:231], v7 offset:17408\n"
"s_waitcnt vmcnt(18)\n"
"s_waitcnt lgkmcnt(10)\n"
"v_mfma_f32_32x32x16_bf16 v[16:31], v[208:211], v[144:147], v[16:31]\n"
"ds_read_b64_tr_b16 v[208:209], v6 offset:2048\n"
"ds_read_b64_tr_b16 v[210:211], v6 offset:3072\n"
"s_waitcnt lgkmcnt(10)\n"
"v_mfma_f32_32x32x16_bf16 v[32:47], v[212:215], v[144:147], v[32:47]\n"
"ds_read_b64_tr_b16 v[212:213], v7 offset:2048\n"
"ds_read_b64_tr_b16 v[214:215], v7 offset:3072\n"
"s_waitcnt lgkmcnt(10)\n"
"v_mfma_f32_32x32x16_bf16 v[48:63], v[216:219], v[144:147], v[48:63]\n"
"ds_read_b64_tr_b16 v[216:217], v6 offset:10240\n"
"ds_read_b64_tr_b16 v[218:219], v6 offset:11264\n"
"s_add_u32 s76, s61, 28672\n"
"buffer_load_dwordx4 v[128:131], v2, s[44:47], s76 offen\n"
"s_waitcnt lgkmcnt(10)\n"
"v_mfma_f32_32x32x16_bf16 v[64:79], v[220:223], v[144:147], v[64:79]\n"
"ds_read_b64_tr_b16 v[220:221], v7 offset:10240\n"
"ds_read_b64_tr_b16 v[222:223], v7 offset:11264\n"
"s_waitcnt lgkmcnt(10)\n"
"v_mfma_f32_32x32x16_bf16 v[80:95], v[224:227], v[144:147], v[80:95]\n"
"ds_read_b64_tr_b16 v[224:225], v6 offset:18432\n"
"ds_read_b64_tr_b16 v[226:227], v6 offset:19456\n"
"s_waitcnt lgkmcnt(10)\n"
"v_mfma_f32_32x32x16_bf16 v[96:111], v[228:231], v[144:147], v[96:111]\n"
"ds_read_b64_tr_b16 v[228:229], v7 offset:18432\n"
"ds_read_b64_tr_b16 v[230:231], v7 offset:19456\n"
"buffer_load_dwordx4 v[132:135], v2, s[44:47], s76 offen offset:1024\n"
"s_waitcnt lgkmcnt(10)\n"
"v_mfma_f32_32x32x16_bf16 v[16:31], v[208:211], v[148:151], v[16:31]\n"
"ds_read_b64_tr_b16 v[208:209], v6 offset:4096\n"
"ds_read_b64_tr_b16 v[210:211], v6 offset:5120\n"
"s_waitcnt lgkmcnt(10)\n"
"v_mfma_f32_32x32x16_bf16 v[32:47], v[212:215], v[148:151], v[32:47]\n"
"ds_read_b64_tr_b16 v[212:213], v7 offset:4096\n"
"ds_read_b64_tr_b16 v[214:215], v7 offset:5120\n"
"s_waitcnt lgkmcnt(10)\n"
"v_mfma_f32_32x32x16_bf16 v[48:63], v[216:219], v[148:151], v[48:63]\n"
"ds_read_b64_tr_b16 v[216:217], v6 offset:12288\n"
"ds_read_b64_tr_b16 v[218:219], v6 offset:13312\n"
"buffer_load_dwordx4 v[136:139], v2, s[44:47], s76 offen offset:2048\n"
"s_waitcnt lgkmcnt(10)\n"
"v_mfma_f32_32x32x16_bf16 v[64:79], v[220:223], v[148:151], v[64:79]\n"
"ds_read_b64_tr_b16 v[220:221], v7 offset:12288\n"
"ds_read_b64_tr_b16 v[222:223], v7 offset:13312\n"
"s_waitcnt lgkmcnt(10)\n"
"v_mfma_f32_32x32x16_bf16 v[80:95], v[224:227], v[148:151], v[80:95]\n"
"ds_read_b64_tr_b16 v[224:225], v6 offset:20480\n"
"ds_read_b64_tr_b16 v[226:227], v6 offset:21504\n"
"s_waitcnt lgkmcnt(10)\n"
"v_mfma_f32_32x32x16_bf16 v[96:111], v[228:231], v[148:151], v[96:111]\n"
"ds_read_b64_tr_b16 v[228:229], v7 offset:20480\n"
"ds_read_b64_tr_b16 v[230:231], v7 offset:21504\n"
"buffer_load_dwordx4 v[140:143], v2, s[44:47], s76 offen offset:3072\n"
"s_waitcnt lgkmcnt(10)\n"
"v_mfma_f32_32x32x16_bf16 v[16:31], v[208:211], v[152:155], v[16:31]\n"
"ds_read_b64_tr_b16 v[208:209], v6 offset:6144\n"
"ds_read_b64_tr_b16 v[210:211], v6 offset:7168\n"
"s_waitcnt lgkmcnt(10)\n"
"v_mfma_f32_32x32x16_bf16 v[32:47], v[212:215], v[152:155], v[32:47]\n"
"ds_read_b64_tr_b16 v[212:213], v7 offset:6144\n"
"ds_read_b64_tr_b16 v[214:215], v7 offset:7168\n"
"s_waitcnt lgkmcnt(10)\n"
"v_mfma_f32_32x32x16_bf16 v[48:63], v[216:219], v[152:155], v[48:63]\n"
"ds_read_b64_tr_b16 v[216:217], v6 offset:14336\n"
"ds_read_b64_tr_b16 v[218:219], v6 offset:15360\n"
"s_waitcnt lgkmcnt(10)\n"
"v_mfma_f32_32x32x16_bf16 v[64:79], v[220:223], v[152:155], v[64:79]\n"
"ds_read_b64_tr_b16 v[220:221], v7 offset:14336\n"
"ds_read_b64_tr_b16 v[222:223], v7 offset:15360\n"
"s_waitcnt lgkmcnt(10)\n"
"v_mfma_f32_32x32x16_bf16 v[80:95], v[224:227], v[152:155], v[80:95]\n"
"ds_read_b64_tr_b16 v[224:225], v6 offset:22528\n"
"ds_read_b64_tr_b16 v[226:227], v6 offset:23552\n"
"s_waitcnt lgkmcnt(10)\n"
"v_mfma_f32_32x32x16_bf16 v[96:111], v[228:231], v[152:155], v[96:111]\n"
"ds_read_b64_tr_b16 v[228:229], v7 offset:22528\n"
"ds_read_b64_tr_b16 v[230:231], v7 offset:23552\n"
"s_waitcnt lgkmcnt(10)\n"
"v_mfma_f32_32x32x16_bf16 v[16:31], v[208:211], v[156:159], v[16:31]\n"
"s_waitcnt vmcnt(40)\n"
"s_barrier\n"
"s_waitcnt lgkmcnt(8)\n"
"v_mfma_f32_32x32x16_bf16 v[32:47], v[212:215], v[156:159], v[32:47]\n"
"ds_read_b64_tr_b16 v[208:209], v6 offset:24576\n"
"ds_read_b64_tr_b16 v[210:211], v6 offset:25600\n"
"ds_read_b64_tr_b16 v[212:213], v7 offset:24576\n"
"ds_read_b64_tr_b16 v[214:215], v7 offset:25600\n"
"s_waitcnt lgkmcnt(10)\n"
"v_mfma_f32_32x32x16_bf16 v[48:63], v[216:219], v[156:159], v[48:63]\n"
"ds_read_b64_tr_b16 v[216:217], v6 offset:32768\n"
"ds_read_b64_tr_b16 v[218:219], v6 offset:33792\n"
"s_waitcnt lgkmcnt(10)\n"
"v_mfma_f32_32x32x16_bf16 v[64:79], v[220:223], v[156:159], v[64:79]\n"
"ds_read_b64_tr_b16 v[220:221], v7 offset:32768\n"
"ds_read_b64_tr_b16 v[222:223], v7 offset:33792\n"
"s_waitcnt lgkmcnt(10)\n"
"v_mfma_f32_32x32x16_bf16 v[80:95], v[224:227], v[156:159], v[80:95]\n"
"ds_read_b64_tr_b16 v[224:225], v6 offset:40960\n"
"ds_read_b64_tr_b16 v[226:227], v6 offset:41984\n"
"s_waitcnt lgkmcnt(10)\n"
"v_mfma_f32_32x32x16_bf16 v[96:111], v[228:231], v[156:159], v[96:111]\n"
"ds_read_b64_tr_b16 v[228:229], v7 offset:40960\n"
"ds_read_b64_tr_b16 v[230:231], v7 offset:41984\n"
"s_waitcnt vmcnt(18)\n"
"s_waitcnt lgkmcnt(10)\n"
"v_mfma_f32_32x32x16_bf16 v[16:31], v[208:211], v[160:163], v[16:31]\n"
"ds_read_b64_tr_b16 v[208:209], v6 offset:26624\n"
"ds_read_b64_tr_b16 v[210:211], v6 offset:27648\n"
"s_waitcnt lgkmcnt(10)\n"
"v_mfma_f32_32x32x16_bf16 v[32:47], v[212:215], v[160:163], v[32:47]\n"
"ds_read_b64_tr_b16 v[212:213], v7 offset:26624\n"
"ds_read_b64_tr_b16 v[214:215], v7 offset:27648\n"
"s_waitcnt lgkmcnt(10)\n"
"v_mfma_f32_32x32x16_bf16 v[48:63], v[216:219], v[160:163], v[48:63]\n"
"ds_read_b64_tr_b16 v[216:217], v6 offset:34816\n"
"ds_read_b64_tr_b16 v[218:219], v6 offset:35840\n"
"s_add_u32 s76, s61, 32768\n"
"buffer_load_dwordx4 v[144:147], v2, s[44:47], s76 offen\n"
"s_waitcnt lgkmcnt(10)\n"
"v_mfma_f32_32x32x16_bf16 v[64:79], v[220:223], v[160:163], v[64:79]\n"
"ds_read_b64_tr_b16 v[220:221], v7 offset:34816\n"
"ds_read_b64_tr_b16 v[222:223], v7 offset:35840\n"
"s_waitcnt lgkmcnt(10)\n"
"v_mfma_f32_32x32x16_bf16 v[80:95], v[224:227], v[160:163], v[80:95]\n"
"ds_read_b64_tr_b16 v[224:225], v6 offset:43008\n"
"ds_read_b64_tr_b16 v[226:227], v6 offset:44032\n"
"s_waitcnt lgkmcnt(10)\n"
"v_mfma_f32_32x32x16_bf16 v[96:111], v[228:231], v[160:163], v[96:111]\n"
"ds_read_b64_tr_b16 v[228:229], v7 offset:43008\n"
"ds_read_b64_tr_b16 v[230:231], v7 offset:44032\n"
"buffer_load_dwordx4 v[148:151], v2, s[44:47], s76 offen offset:1024\n"
"s_waitcnt lgkmcnt(10)\n"
"v_mfma_f32_32x32x16_bf16 v[16:31], v[208:211], v[164:167], v[16:31]\n"
"ds_read_b64_tr_b16 v[208:209], v6 offset:28672\n"
"ds_read_b64_tr_b16 v[210:211], v6 offset:29696\n"
"s_waitcnt lgkmcnt(10)\n"
"v_mfma_f32_32x32x16_bf16 v[32:47], v[212:215], v[164:167], v[32:47]\n"
"ds_read_b64_tr_b16 v[212:213], v7 offset:28672\n"
"ds_read_b64_tr_b16 v[214:215], v7 offset:29696\n"
"s_waitcnt lgkmcnt(10)\n"
"v_mfma_f32_32x32x16_bf16 v[48:63], v[216:219], v[164:167], v[48:63]\n"
"ds_read_b64_tr_b16 v[216:217], v6 offset:36864\n"
"ds_read_b64_tr_b16 v[218:219], v6 offset:37888\n"
"buffer_load_dwordx4 v[152:155], v2, s[44:47], s76 offen offset:2048\n"
"s_waitcnt lgkmcnt(10)\n"
"v_mfma_f32_32x32x16_bf16 v[64:79], v[220:223], v[164:167], v[64:79]\n"
"ds_read_b64_tr_b16 v[220:221], v7 offset:36864\n"
"ds_read_b64_tr_b16 v[222:223], v7 offset:37888\n"
"s_waitcnt lgkmcnt(10)\n"
"v_mfma_f32_32x32x16_bf16 v[80:95], v[224:227], v[164:167], v[80:95]\n"
"ds_read_b64_tr_b16 v[224:225], v6 offset:45056\n"
"ds_read_b64_tr_b16 v[226:227], v6 offset:46080\n"
"s_waitcnt lgkmcnt(10)\n"
"v_mfma_f32_32x32x16_bf16 v[96:111], v[228:231], v[164:167], v[96:111]\n"
"ds_read_b64_tr_b16 v[228:229], v7 offset:45056\n"
"ds_read_b64_tr_b16 v[230:231], v7 offset:46080\n"
"buffer_load_dwordx4 v[156:159], v2, s[44:47], s76 offen offset:3072\n"
"s_waitcnt lgkmcnt(10)\n"
"v_mfma_f32_32x32x16_bf16 v[16:31], v[208:211], v[168:171], v[16:31]\n"
"ds_read_b64_tr_b16 v[208:209], v6 offset:30720\n"
"ds_read_b64_tr_b16 v[210:211], v6 offset:31744\n"
"s_waitcnt lgkmcnt(10)\n"
"v_mfma_f32_32x32x16_bf16 v[32:47], v[212:215], v[168:171], v[32:47]\n"
"ds_read_b64_tr_b16 v[212:213], v7 offset:30720\n"
"ds_read_b64_tr_b16 v[214:215], v7 offset:31744\n"
"s_waitcnt lgkmcnt(10)\n"
"v_mfma_f32_32x32x16_bf16 v[48:63], v[216:219], v[168:171], v[48:63]\n"
"ds_read_b64_tr_b16 v[216:217], v6 offset:38912\n"
"ds_read_b64_tr_b16 v[218:219], v6 offset:39936\n"
"s_waitcnt vmcnt(16)\n"
"v_mov_b32 v13, 1\n"
"s_mov_b64 exec, 1\n"
"buffer_atomic_add v13, v3, s[44:47], s92 offen sc1\n"
"s_mov_b64 exec, -1\n"
"s_waitcnt lgkmcnt(10)\n"
"v_mfma_f32_32x32x16_bf16 v[64:79], v[220:223], v[168:171], v[64:79]\n"
"ds_read_b64_tr_b16 v[220:221], v7 offset:38912\n"
"ds_read_b64_tr_b16 v[222:223], v7 offset:39936\n"
"s_waitcnt lgkmcnt(10)\n"
"v_mfma_f32_32x32x16_bf16 v[80:95], v[224:227], v[168:171], v[80:95]\n"
"ds_read_b64_tr_b16 v[224:225], v6 offset:47104\n"
"ds_read_b64_tr_b16 v[226:227], v6 offset:48128\n"
"s_waitcnt lgkmcnt(10)\n"
"v_mfma_f32_32x32x16_bf16 v[96:111], v[228:231], v[168:171], v[96:111]\n"
"ds_read_b64_tr_b16 v[228:229], v7 offset:47104\n"
"ds_read_b64_tr_b16 v[230:231], v7 offset:48128\n"
"s_waitcnt lgkmcnt(10)\n"
"v_mfma_f32_32x32x16_bf16 v[16:31], v[208:211], v[172:175], v[16:31]\n"
"s_waitcnt vmcnt(41)\n"
"s_barrier\n"
"s_waitcnt lgkmcnt(8)\n"
"v_mfma_f32_32x32x16_bf16 v[32:47], v[212:215], v[172:175], v[32:47]\n"
"ds_read_b64_tr_b16 v[208:209], v8 offset:0\n"
"ds_read_b64_tr_b16 v[210:211], v8 offset:1024\n"
"ds_read_b64_tr_b16 v[212:213], v9 offset:0\n"
"ds_read_b64_tr_b16 v[214:215], v9 offset:1024\n"
"s_waitcnt lgkmcnt(10)\n"
"v_mfma_f32_32x32x16_bf16 v[48:63], v[216:219], v[172:175], v[48:63]\n"
"ds_read_b64_tr_b16 v[216:217], v8 offset:8192\n"
"ds_read_b64_tr_b16 v[218:219], v8 offset:9216\n"
"s_waitcnt lgkmcnt(10)\n"
"v_mfma_f32_32x32x16_bf16 v[64:79], v[220:223], v[172:175], v[64:79]\n"
"ds_read_b64_tr_b16 v[220:221], v9 offset:8192\n"
"ds_read_b64_tr_b16 v[222:223], v9 offset:9216\n"
"s_waitcnt lgkmcnt(10)\n"
"v_mfma_f32_32x32x16_bf16 v[80:95], v[224:227], v[172:175], v[80:95]\n"
"ds_read_b64_tr_b16 v[224:225], v8 offset:16384\n"
"ds_read_b64_tr_b16 v[226:227], v8 offset:17408\n"
"s_waitcnt lgkmcnt(10)\n"
"v_mfma_f32_32x32x16_bf16 v[96:111], v[228:231], v[172:175], v[96:111]\n"
"ds_read_b64_tr_b16 v[228:229], v9 offset:16384\n"
"ds_read_b64_tr_b16 v[230:231], v9 offset:17408\n"
"s_waitcnt vmcnt(18)\n"
"s_waitcnt lgkmcnt(10)\n"
"v_mfma_f32_32x32x16_bf16 v[16:31], v[208:211], v[176:179], v[16:31]\n"
"ds_read_b64_tr_b16 v[208:209], v8 offset:2048\n"
"ds_read_b64_tr_b16 v[210:211], v8 offset:3072\n"
"s_waitcnt lgkmcnt(10)\n"
"v_mfma_f32_32x32x16_bf16 v[32:47], v[212:215], v[176:179], v[32:47]\n"
"ds_read_b64_tr_b16 v[212:213], v9 offset:2048\n"
"ds_read_b64_tr_b16 v[214:215], v9 offset:3072\n"
"s_waitcnt lgkmcnt(10)\n"
"v_mfma_f32_32x32x16_bf16 v[48:63], v[216:219], v[176:179], v[48:63]\n"
"ds_read_b64_tr_b16 v[216:217], v8 offset:10240\n"
"ds_read_b64_tr_b16 v[218:219], v8 offset:11264\n"
"s_add_u32 s76, s61, 36864\n"
"buffer_load_dwordx4 v[160:163], v2, s[44:47], s76 offen\n"
"s_waitcnt lgkmcnt(10)\n"
"v_mfma_f32_32x32x16_bf16 v[64:79], v[220:223], v[176:179], v[64:79]\n"
"ds_read_b64_tr_b16 v[220:221], v9 offset:10240\n"
"ds_read_b64_tr_b16 v[222:223], v9 offset:11264\n"
"s_waitcnt lgkmcnt(10)\n"
"v_mfma_f32_32x32x16_bf16 v[80:95], v[224:227], v[176:179], v[80:95]\n"
"ds_read_b64_tr_b16 v[224:225], v8 offset:18432\n"
"ds_read_b64_tr_b16 v[226:227], v8 offset:19456\n"
"s_waitcnt lgkmcnt(10)\n"
"v_mfma_f32_32x32x16_bf16 v[96:111], v[228:231], v[176:179], v[96:111]\n"
"ds_read_b64_tr_b16 v[228:229], v9 offset:18432\n"
"ds_read_b64_tr_b16 v[230:231], v9 offset:19456\n"
"buffer_load_dwordx4 v[164:167], v2, s[44:47], s76 offen offset:1024\n"
"s_waitcnt lgkmcnt(10)\n"
"v_mfma_f32_32x32x16_bf16 v[16:31], v[208:211], v[180:183], v[16:31]\n"
"ds_read_b64_tr_b16 v[208:209], v8 offset:4096\n"
"ds_read_b64_tr_b16 v[210:211], v8 offset:5120\n"
"s_waitcnt lgkmcnt(10)\n"
"v_mfma_f32_32x32x16_bf16 v[32:47], v[212:215], v[180:183], v[32:47]\n"
"ds_read_b64_tr_b16 v[212:213], v9 offset:4096\n"
"ds_read_b64_tr_b16 v[214:215], v9 offset:5120\n"
"s_waitcnt lgkmcnt(10)\n"
"v_mfma_f32_32x32x16_bf16 v[48:63], v[216:219], v[180:183], v[48:63]\n"
"ds_read_b64_tr_b16 v[216:217], v8 offset:12288\n"
"ds_read_b64_tr_b16 v[218:219], v8 offset:13312\n"
"buffer_load_dwordx4 v[168:171], v2, s[44:47], s76 offen offset:2048\n"
"s_waitcnt lgkmcnt(10)\n"
"v_mfma_f32_32x32x16_bf16 v[64:79], v[220:223], v[180:183], v[64:79]\n"
"ds_read_b64_tr_b16 v[220:221], v9 offset:12288\n"
"ds_read_b64_tr_b16 v[222:223], v9 offset:13312\n"
"s_waitcnt lgkmcnt(10)\n"
"v_mfma_f32_32x32x16_bf16 v[80:95], v[224:227], v[180:183], v[80:95]\n"
"ds_read_b64_tr_b16 v[224:225], v8 offset:20480\n"
"ds_read_b64_tr_b16 v[226:227], v8 offset:21504\n"
"s_waitcnt lgkmcnt(10)\n"
"v_mfma_f32_32x32x16_bf16 v[96:111], v[228:231], v[180:183], v[96:111]\n"
"ds_read_b64_tr_b16 v[228:229], v9 offset:20480\n"
"ds_read_b64_tr_b16 v[230:231], v9 offset:21504\n"
"buffer_load_dwordx4 v[172:175], v2, s[44:47], s76 offen offset:3072\n"
"s_waitcnt lgkmcnt(10)\n"
"v_mfma_f32_32x32x16_bf16 v[16:31], v[208:211], v[184:187], v[16:31]\n"
"ds_read_b64_tr_b16 v[208:209], v8 offset:6144\n"
"ds_read_b64_tr_b16 v[210:211], v8 offset:7168\n"
"s_waitcnt lgkmcnt(10)\n"
"v_mfma_f32_32x32x16_bf16 v[32:47], v[212:215], v[184:187], v[32:47]\n"
"ds_read_b64_tr_b16 v[212:213], v9 offset:6144\n"
"ds_read_b64_tr_b16 v[214:215], v9 offset:7168\n"
"s_waitcnt lgkmcnt(10)\n"
"v_mfma_f32_32x32x16_bf16 v[48:63], v[216:219], v[184:187], v[48:63]\n"
"ds_read_b64_tr_b16 v[216:217], v8 offset:14336\n"
"ds_read_b64_tr_b16 v[218:219], v8 offset:15360\n"
"s_waitcnt lgkmcnt(10)\n"
"v_mfma_f32_32x32x16_bf16 v[64:79], v[220:223], v[184:187], v[64:79]\n"
"ds_read_b64_tr_b16 v[220:221], v9 offset:14336\n"
"ds_read_b64_tr_b16 v[222:223], v9 offset:15360\n"
"s_waitcnt lgkmcnt(10)\n"
"v_mfma_f32_32x32x16_bf16 v[80:95], v[224:227], v[184:187], v[80:95]\n"
"ds_read_b64_tr_b16 v[224:225], v8 offset:22528\n"
"ds_read_b64_tr_b16 v[226:227], v8 offset:23552\n"
"s_waitcnt lgkmcnt(10)\n"
"v_mfma_f32_32x32x16_bf16 v[96:111], v[228:231], v[184:187], v[96:111]\n"
"ds_read_b64_tr_b16 v[228:229], v9 offset:22528\n"
"ds_read_b64_tr_b16 v[230:231], v9 offset:23552\n"
"s_waitcnt lgkmcnt(10)\n"
"v_mfma_f32_32x32x16_bf16 v[16:31], v[208:211], v[188:191], v[16:31]\n"
"s_waitcnt vmcnt(42)\n"
"s_barrier\n"
"s_waitcnt lgkmcnt(8)\n"
"v_mfma_f32_32x32x16_bf16 v[32:47], v[212:215], v[188:191], v[32:47]\n"
"ds_read_b64_tr_b16 v[208:209], v8 offset:24576\n"
"ds_read_b64_tr_b16 v[210:211], v8 offset:25600\n"
"ds_read_b64_tr_b16 v[212:213], v9 offset:24576\n"
"ds_read_b64_tr_b16 v[214:215], v9 offset:25600\n"
"s_waitcnt lgkmcnt(10)\n"
"v_mfma_f32_32x32x16_bf16 v[48:63], v[216:219], v[188:191], v[48:63]\n"
"ds_read_b64_tr_b16 v[216:217], v8 offset:32768\n"
"ds_read_b64_tr_b16 v[218:219], v8 offset:33792\n"
"s_waitcnt lgkmcnt(10)\n"
"v_mfma_f32_32x32x16_bf16 v[64:79], v[220:223], v[188:191], v[64:79]\n"
"ds_read_b64_tr_b16 v[220:221], v9 offset:32768\n"
"ds_read_b64_tr_b16 v[222:223], v9 offset:33792\n"
"s_waitcnt lgkmcnt(10)\n"
"v_mfma_f32_32x32x16_bf16 v[80:95], v[224:227], v[188:191], v[80:95]\n"
"ds_read_b64_tr_b16 v[224:225], v8 offset:40960\n"
"ds_read_b64_tr_b16 v[226:227], v8 offset:41984\n"
"s_waitcnt lgkmcnt(10)\n"
"v_mfma_f32_32x32x16_bf16 v[96:111], v[228:231], v[188:191], v[96:111]\n"
"ds_read_b64_tr_b16 v[228:229], v9 offset:40960\n"
"ds_read_b64_tr_b16 v[230:231], v9 offset:41984\n"
"s_waitcnt vmcnt(16)\n"
"s_waitcnt lgkmcnt(10)\n"
"v_mfma_f32_32x32x16_bf16 v[16:31], v[208:211], v[192:195], v[16:31]\n"
"ds_read_b64_tr_b16 v[208:209], v8 offset:26624\n"
"ds_read_b64_tr_b16 v[210:211], v8 offset:27648\n"
"s_waitcnt lgkmcnt(10)\n"
"v_mfma_f32_32x32x16_bf16 v[32:47], v[212:215], v[192:195], v[32:47]\n"
"ds_read_b64_tr_b16 v[212:213], v9 offset:26624\n"
"ds_read_b64_tr_b16 v[214:215], v9 offset:27648\n"
"s_waitcnt lgkmcnt(10)\n"
"v_mfma_f32_32x32x16_bf16 v[48:63], v[216:219], v[192:195], v[48:63]\n"
"ds_read_b64_tr_b16 v[216:217], v8 offset:34816\n"
"ds_read_b64_tr_b16 v[218:219], v8 offset:35840\n"
"s_add_u32 s76, s61, 40960\n"
"buffer_load_dwordx4 v[176:179], v2, s[44:47], s76 offen\n"
"s_waitcnt lgkmcnt(10)\n"
"v_mfma_f32_32x32x16_bf16 v[64:79], v[220:223], v[192:195], v[64:79]\n"
"ds_read_b64_tr_b16 v[220:221], v9 offset:34816\n"
"ds_read_b64_tr_b16 v[222:223], v9 offset:35840\n"
"s_waitcnt lgkmcnt(10)\n"
"v_mfma_f32_32x32x16_bf16 v[80:95], v[224:227], v[192:195], v[80:95]\n"
"ds_read_b64_tr_b16 v[224:225], v8 offset:43008\n"
"ds_read_b64_tr_b16 v[226:227], v8 offset:44032\n"
"s_waitcnt lgkmcnt(10)\n"
"v_mfma_f32_32x32x16_bf16 v[96:111], v[228:231], v[192:195], v[96:111]\n"
"ds_read_b64_tr_b16 v[228:229], v9 offset:43008\n"
"ds_read_b64_tr_b16 v[230:231], v9 offset:44032\n"
"buffer_load_dwordx4 v[180:183], v2, s[44:47], s76 offen offset:1024\n"
"s_waitcnt lgkmcnt(10)\n"
"v_mfma_f32_32x32x16_bf16 v[16:31], v[208:211], v[196:199], v[16:31]\n"
"ds_read_b64_tr_b16 v[208:209], v8 offset:28672\n"
"ds_read_b64_tr_b16 v[210:211], v8 offset:29696\n"
"s_waitcnt lgkmcnt(10)\n"
"v_mfma_f32_32x32x16_bf16 v[32:47], v[212:215], v[196:199], v[32:47]\n"
"ds_read_b64_tr_b16 v[212:213], v9 offset:28672\n"
"ds_read_b64_tr_b16 v[214:215], v9 offset:29696\n"
"s_waitcnt lgkmcnt(10)\n"
"v_mfma_f32_32x32x16_bf16 v[48:63], v[216:219], v[196:199], v[48:63]\n"
"ds_read_b64_tr_b16 v[216:217], v8 offset:36864\n"
"ds_read_b64_tr_b16 v[218:219], v8 offset:37888\n"
"buffer_load_dwordx4 v[184:187], v2, s[44:47], s76 offen offset:2048\n"
"s_waitcnt lgkmcnt(10)\n"
"v_mfma_f32_32x32x16_bf16 v[64:79], v[220:223], v[196:199], v[64:79]\n"
"ds_read_b64_tr_b16 v[220:221], v9 offset:36864\n"
"ds_read_b64_tr_b16 v[222:223], v9 offset:37888\n"
"s_waitcnt lgkmcnt(10)\n"
"v_mfma_f32_32x32x16_bf16 v[80:95], v[224:227], v[196:199], v[80:95]\n"
"ds_read_b64_tr_b16 v[224:225], v8 offset:45056\n"
"ds_read_b64_tr_b16 v[226:227], v8 offset:46080\n"
"s_waitcnt lgkmcnt(10)\n"
"v_mfma_f32_32x32x16_bf16 v[96:111], v[228:231], v[196:199], v[96:111]\n"
"ds_read_b64_tr_b16 v[228:229], v9 offset:45056\n"
"ds_read_b64_tr_b16 v[230:231], v9 offset:46080\n"
"buffer_load_dwordx4 v[188:191], v2, s[44:47], s76 offen offset:3072\n"
"s_waitcnt lgkmcnt(10)\n"
"v_mfma_f32_32x32x16_bf16 v[16:31], v[208:211], v[200:203], v[16:31]\n"
"ds_read_b64_tr_b16 v[208:209], v8 offset:30720\n"
"ds_read_b64_tr_b16 v[210:211], v8 offset:31744\n"
"s_waitcnt lgkmcnt(10)\n"
"v_mfma_f32_32x32x16_bf16 v[32:47], v[212:215], v[200:203], v[32:47]\n"
"ds_read_b64_tr_b16 v[212:213], v9 offset:30720\n"
"ds_read_b64_tr_b16 v[214:215], v9 offset:31744\n"
"s_waitcnt lgkmcnt(10)\n"
"v_mfma_f32_32x32x16_bf16 v[48:63], v[216:219], v[200:203], v[48:63]\n"
"ds_read_b64_tr_b16 v[216:217], v8 offset:38912\n"
"ds_read_b64_tr_b16 v[218:219], v8 offset:39936\n"
"s_waitcnt lgkmcnt(10)\n"
"v_mfma_f32_32x32x16_bf16 v[64:79], v[220:223], v[200:203], v[64:79]\n"
"ds_read_b64_tr_b16 v[220:221], v9 offset:38912\n"
"ds_read_b64_tr_b16 v[222:223], v9 offset:39936\n"
"s_waitcnt lgkmcnt(10)\n"
"v_mfma_f32_32x32x16_bf16 v[80:95], v[224:227], v[200:203], v[80:95]\n"
"ds_read_b64_tr_b16 v[224:225], v8 offset:47104\n"
"ds_read_b64_tr_b16 v[226:227], v8 offset:48128\n"
"s_waitcnt lgkmcnt(10)\n"
"v_mfma_f32_32x32x16_bf16 v[96:111], v[228:231], v[200:203], v[96:111]\n"
"ds_read_b64_tr_b16 v[228:229], v9 offset:47104\n"
"ds_read_b64_tr_b16 v[230:231], v9 offset:48128\n"
"s_waitcnt lgkmcnt(10)\n"
"v_mfma_f32_32x32x16_bf16 v[16:31], v[208:211], v[204:207], v[16:31]\n"
"s_barrier\n"
"s_waitcnt lgkmcnt(8)\n"
"v_mfma_f32_32x32x16_bf16 v[32:47], v[212:215], v[204:207], v[32:47]\n"
"ds_read_b64_tr_b16 v[208:209], v4 offset:0\n"
"ds_read_b64_tr_b16 v[210:211], v4 offset:1024\n"
"ds_read_b64_tr_b16 v[212:213], v5 offset:0\n"
"ds_read_b64_tr_b16 v[214:215], v5 offset:1024\n"
"s_waitcnt lgkmcnt(10)\n"
"v_mfma_f32_32x32x16_bf16 v[48:63], v[216:219], v[204:207], v[48:63]\n"
"ds_read_b64_tr_b16 v[216:217], v4 offset:8192\n"
"ds_read_b64_tr_b16 v[218:219], v4 offset:9216\n"
"s_waitcnt lgkmcnt(10)\n"
"v_mfma_f32_32x32x16_bf16 v[64:79], v[220:223], v[204:207], v[64:79]\n"
"ds_read_b64_tr_b16 v[220:221], v5 offset:8192\n"
"ds_read_b64_tr_b16 v[222:223], v5 offset:9216\n"
"s_waitcnt lgkmcnt(10)\n"
"v_mfma_f32_32x32x16_bf16 v[80:95], v[224:227], v[204:207], v[80:95]\n"
"ds_read_b64_tr_b16 v[224:225], v4 offset:16384\n"
"ds_read_b64_tr_b16 v[226:227], v4 offset:17408\n"
"s_waitcnt lgkmcnt(10)\n"
"v_mfma_f32_32x32x16_bf16 v[96:111], v[228:231], v[204:207], v[96:111]\n"
"ds_read_b64_tr_b16 v[228:229], v5 offset:16384\n"
"ds_read_b64_tr_b16 v[230:231], v5 offset:17408\n"
"s_waitcnt vmcnt(16)\n"
"s_waitcnt lgkmcnt(10)\n"
"v_mfma_f32_32x32x16_bf16 v[16:31], v[208:211], v[112:115], v[16:31]\n"
"ds_read_b64_tr_b16 v[208:209], v4 offset:2048\n"
"ds_read_b64_tr_b16 v[210:211], v4 offset:3072\n"
"s_waitcnt lgkmcnt(10)\n"
"v_mfma_f32_32x32x16_bf16 v[32:47], v[212:215], v[112:115], v[32:47]\n"
"ds_read_b64_tr_b16 v[212:213], v5 offset:2048\n"
"ds_read_b64_tr_b16 v[214:215], v5 offset:3072\n"
"s_waitcnt lgkmcnt(10)\n"
"v_mfma_f32_32x32x16_bf16 v[48:63], v[216:219], v[112:115], v[48:63]\n"
"ds_read_b64_tr_b16 v[216:217], v4 offset:10240\n"
"ds_read_b64_tr_b16 v[218:219], v4 offset:11264\n"
"s_add_u32 s76, s61, 45056\n"
"buffer_load_dwordx4 v[192:195], v2, s[44:47], s76 offen\n"
"s_waitcnt lgkmcnt(10)\n"
"v_mfma_f32_32x32x16_bf16 v[64:79], v[220:223], v[112:115], v[64:79]\n"
"ds_read_b64_tr_b16 v[220:221], v5 offset:10240\n"
"ds_read_b64_tr_b16 v[222:223], v5 offset:11264\n"
"s_waitcnt lgkmcnt(10)\n"
"v_mfma_f32_32x32x16_bf16 v[80:95], v[224:227], v[112:115], v[80:95]\n"
"ds_read_b64_tr_b16 v[224:225], v4 offset:18432\n"
"ds_read_b64_tr_b16 v[226:227], v4 offset:19456\n"
"s_waitcnt lgkmcnt(10)\n"
"v_mfma_f32_32x32x16_bf16 v[96:111], v[228:231], v[112:115], v[96:111]\n"
"ds_read_b64_tr_b16 v[228:229], v5 offset:18432\n"
"ds_read_b64_tr_b16 v[230:231], v5 offset:19456\n"
"buffer_load_dwordx4 v[196:199], v2, s[44:47], s76 offen offset:1024\n"
"s_waitcnt lgkmcnt(10)\n"
"v_mfma_f32_32x32x16_bf16 v[16:31], v[208:211], v[116:119], v[16:31]\n"
"ds_read_b64_tr_b16 v[208:209], v4 offset:4096\n"
"ds_read_b64_tr_b16 v[210:211], v4 offset:5120\n"
"s_waitcnt lgkmcnt(10)\n"
"v_mfma_f32_32x32x16_bf16 v[32:47], v[212:215], v[116:119], v[32:47]\n"
"ds_read_b64_tr_b16 v[212:213], v5 offset:4096\n"
"ds_read_b64_tr_b16 v[214:215], v5 offset:5120\n"
"s_waitcnt lgkmcnt(10)\n"
"v_mfma_f32_32x32x16_bf16 v[48:63], v[216:219], v[116:119], v[48:63]\n"
"ds_read_b64_tr_b16 v[216:217], v4 offset:12288\n"
"ds_read_b64_tr_b16 v[218:219], v4 offset:13312\n"
"buffer_load_dwordx4 v[200:203], v2, s[44:47], s76 offen offset:2048\n"
"s_waitcnt lgkmcnt(10)\n"
"v_mfma_f32_32x32x16_bf16 v[64:79], v[220:223], v[116:119], v[64:79]\n"
"ds_read_b64_tr_b16 v[220:221], v5 offset:12288\n"
"ds_read_b64_tr_b16 v[222:223], v5 offset:13312\n"
"s_waitcnt lgkmcnt(10)\n"
"v_mfma_f32_32x32x16_bf16 v[80:95], v[224:227], v[116:119], v[80:95]\n"
"ds_read_b64_tr_b16 v[224:225], v4 offset:20480\n"
"ds_read_b64_tr_b16 v[226:227], v4 offset:21504\n"
"s_waitcnt lgkmcnt(10)\n"
"v_mfma_f32_32x32x16_bf16 v[96:111], v[228:231], v[116:119], v[96:111]\n"
"ds_read_b64_tr_b16 v[228:229], v5 offset:20480\n"
"ds_read_b64_tr_b16 v[230:231], v5 offset:21504\n"
"buffer_load_dwordx4 v[204:207], v2, s[44:47], s76 offen offset:3072\n"
"s_waitcnt lgkmcnt(10)\n"
"v_mfma_f32_32x32x16_bf16 v[16:31], v[208:211], v[120:123], v[16:31]\n"
"ds_read_b64_tr_b16 v[208:209], v4 offset:6144\n"
"ds_read_b64_tr_b16 v[210:211], v4 offset:7168\n"
"s_waitcnt lgkmcnt(10)\n"
"v_mfma_f32_32x32x16_bf16 v[32:47], v[212:215], v[120:123], v[32:47]\n"
"ds_read_b64_tr_b16 v[212:213], v5 offset:6144\n"
"ds_read_b64_tr_b16 v[214:215], v5 offset:7168\n"
"s_waitcnt lgkmcnt(10)\n"
"v_mfma_f32_32x32x16_bf16 v[48:63], v[216:219], v[120:123], v[48:63]\n"
"ds_read_b64_tr_b16 v[216:217], v4 offset:14336\n"
"ds_read_b64_tr_b16 v[218:219], v4 offset:15360\n"
"s_waitcnt lgkmcnt(10)\n"
"v_mfma_f32_32x32x16_bf16 v[64:79], v[220:223], v[120:123], v[64:79]\n"
"ds_read_b64_tr_b16 v[220:221], v5 offset:14336\n"
"ds_read_b64_tr_b16 v[222:223], v5 offset:15360\n"
"s_waitcnt lgkmcnt(10)\n"
"v_mfma_f32_32x32x16_bf16 v[80:95], v[224:227], v[120:123], v[80:95]\n"
"ds_read_b64_tr_b16 v[224:225], v4 offset:22528\n"
"ds_read_b64_tr_b16 v[226:227], v4 offset:23552\n"
"s_waitcnt lgkmcnt(10)\n"
"v_mfma_f32_32x32x16_bf16 v[96:111], v[228:231], v[120:123], v[96:111]\n"
"ds_read_b64_tr_b16 v[228:229], v5 offset:22528\n"
"ds_read_b64_tr_b16 v[230:231], v5 offset:23552\n"
"s_waitcnt lgkmcnt(10)\n"
"v_mfma_f32_32x32x16_bf16 v[16:31], v[208:211], v[124:127], v[16:31]\n"
"s_barrier\n"
"s_waitcnt lgkmcnt(8)\n"
"v_mfma_f32_32x32x16_bf16 v[32:47], v[212:215], v[124:127], v[32:47]\n"
"ds_read_b64_tr_b16 v[208:209], v4 offset:24576\n"
"ds_read_b64_tr_b16 v[210:211], v4 offset:25600\n"
"ds_read_b64_tr_b16 v[212:213], v5 offset:24576\n"
"ds_read_b64_tr_b16 v[214:215], v5 offset:25600\n"
"s_waitcnt lgkmcnt(10)\n"
"v_mfma_f32_32x32x16_bf16 v[48:63], v[216:219], v[124:127], v[48:63]\n"
"ds_read_b64_tr_b16 v[216:217], v4 offset:32768\n"
"ds_read_b64_tr_b16 v[218:219], v4 offset:33792\n"
"s_waitcnt lgkmcnt(10)\n"
"v_mfma_f32_32x32x16_bf16 v[64:79], v[220:223], v[124:127], v[64:79]\n"
"ds_read_b64_tr_b16 v[220:221], v5 offset:32768\n"
"ds_read_b64_tr_b16 v[222:223], v5 offset:33792\n"
"s_waitcnt lgkmcnt(10)\n"
"v_mfma_f32_32x32x16_bf16 v[80:95], v[224:227], v[124:127], v[80:95]\n"
"ds_read_b64_tr_b16 v[224:225], v4 offset:40960\n"
"ds_read_b64_tr_b16 v[226:227], v4 offset:41984\n"
"s_waitcnt lgkmcnt(10)\n"
"v_mfma_f32_32x32x16_bf16 v[96:111], v[228:231], v[124:127], v[96:111]\n"
"ds_read_b64_tr_b16 v[228:229], v5 offset:40960\n"
"ds_read_b64_tr_b16 v[230:231], v5 offset:41984\n"
"s_waitcnt vmcnt(16)\n"
"s_waitcnt lgkmcnt(10)\n"
"v_mfma_f32_32x32x16_bf16 v[16:31], v[208:211], v[128:131], v[16:31]\n"
"ds_read_b64_tr_b16 v[208:209], v4 offset:26624\n"
"ds_read_b64_tr_b16 v[210:211], v4 offset:27648\n"
"s_waitcnt lgkmcnt(10)\n"
"v_mfma_f32_32x32x16_bf16 v[32:47], v[212:215], v[128:131], v[32:47]\n"
"ds_read_b64_tr_b16 v[212:213], v5 offset:26624\n"
"ds_read_b64_tr_b16 v[214:215], v5 offset:27648\n"
"s_waitcnt lgkmcnt(10)\n"
"v_mfma_f32_32x32x16_bf16 v[48:63], v[216:219], v[128:131], v[48:63]\n"
"ds_read_b64_tr_b16 v[216:217], v4 offset:34816\n"
"ds_read_b64_tr_b16 v[218:219], v4 offset:35840\n"
"s_add_u32 s76, s61, 49152\n"
"buffer_load_dwordx4 v[112:115], v2, s[44:47], s76 offen\n"
"s_waitcnt lgkmcnt(10)\n"
"v_mfma_f32_32x32x16_bf16 v[64:79], v[220:223], v[128:131], v[64:79]\n"
"ds_read_b64_tr_b16 v[220:221], v5 offset:34816\n"
"ds_read_b64_tr_b16 v[222:223], v5 offset:35840\n"
"s_waitcnt lgkmcnt(10)\n"
"v_mfma_f32_32x32x16_bf16 v[80:95], v[224:227], v[128:131], v[80:95]\n"
"ds_read_b64_tr_b16 v[224:225], v4 offset:43008\n"
"ds_read_b64_tr_b16 v[226:227], v4 offset:44032\n"
"s_waitcnt lgkmcnt(10)\n"
"v_mfma_f32_32x32x16_bf16 v[96:111], v[228:231], v[128:131], v[96:111]\n"
"ds_read_b64_tr_b16 v[228:229], v5 offset:43008\n"
"ds_read_b64_tr_b16 v[230:231], v5 offset:44032\n"
"buffer_load_dwordx4 v[116:119], v2, s[44:47], s76 offen offset:1024\n"
"s_waitcnt lgkmcnt(10)\n"
"v_mfma_f32_32x32x16_bf16 v[16:31], v[208:211], v[132:135], v[16:31]\n"
"ds_read_b64_tr_b16 v[208:209], v4 offset:28672\n"
"ds_read_b64_tr_b16 v[210:211], v4 offset:29696\n"
"s_waitcnt lgkmcnt(10)\n"
"v_mfma_f32_32x32x16_bf16 v[32:47], v[212:215], v[132:135], v[32:47]\n"
"ds_read_b64_tr_b16 v[212:213], v5 offset:28672\n"
"ds_read_b64_tr_b16 v[214:215], v5 offset:29696\n"
"s_waitcnt lgkmcnt(10)\n"
"v_mfma_f32_32x32x16_bf16 v[48:63], v[216:219], v[132:135], v[48:63]\n"
"ds_read_b64_tr_b16 v[216:217], v4 offset:36864\n"
"ds_read_b64_tr_b16 v[218:219], v4 offset:37888\n"
"buffer_load_dwordx4 v[120:123], v2, s[44:47], s76 offen offset:2048\n"
"s_waitcnt lgkmcnt(10)\n"
"v_mfma_f32_32x32x16_bf16 v[64:79], v[220:223], v[132:135], v[64:79]\n"
"ds_read_b64_tr_b16 v[220:221], v5 offset:36864\n"
"ds_read_b64_tr_b16 v[222:223], v5 offset:37888\n"
"s_waitcnt lgkmcnt(10)\n"
"v_mfma_f32_32x32x16_bf16 v[80:95], v[224:227], v[132:135], v[80:95]\n"
"ds_read_b64_tr_b16 v[224:225], v4 offset:45056\n"
"ds_read_b64_tr_b16 v[226:227], v4 offset:46080\n"
"s_waitcnt lgkmcnt(10)\n"
"v_mfma_f32_32x32x16_bf16 v[96:111], v[228:231], v[132:135], v[96:111]\n"
"ds_read_b64_tr_b16 v[228:229], v5 offset:45056\n"
"ds_read_b64_tr_b16 v[230:231], v5 offset:46080\n"
"buffer_load_dwordx4 v[124:127], v2, s[44:47], s76 offen offset:3072\n"
"s_waitcnt lgkmcnt(10)\n"
"v_mfma_f32_32x32x16_bf16 v[16:31], v[208:211], v[136:139], v[16:31]\n"
"ds_read_b64_tr_b16 v[208:209], v4 offset:30720\n"
"ds_read_b64_tr_b16 v[210:211], v4 offset:31744\n"
"s_waitcnt lgkmcnt(10)\n"
"v_mfma_f32_32x32x16_bf16 v[32:47], v[212:215], v[136:139], v[32:47]\n"
"ds_read_b64_tr_b16 v[212:213], v5 offset:30720\n"
"ds_read_b64_tr_b16 v[214:215], v5 offset:31744\n"
"s_waitcnt lgkmcnt(10)\n"
"v_mfma_f32_32x32x16_bf16 v[48:63], v[216:219], v[136:139], v[48:63]\n"
"ds_read_b64_tr_b16 v[216:217], v4 offset:38912\n"
"ds_read_b64_tr_b16 v[218:219], v4 offset:39936\n"
"s_waitcnt lgkmcnt(10)\n"
"v_mfma_f32_32x32x16_bf16 v[64:79], v[220:223], v[136:139], v[64:79]\n"
"ds_read_b64_tr_b16 v[220:221], v5 offset:38912\n"
"ds_read_b64_tr_b16 v[222:223], v5 offset:39936\n"
"s_waitcnt lgkmcnt(10)\n"
"v_mfma_f32_32x32x16_bf16 v[80:95], v[224:227], v[136:139], v[80:95]\n"
"ds_read_b64_tr_b16 v[224:225], v4 offset:47104\n"
"ds_read_b64_tr_b16 v[226:227], v4 offset:48128\n"
"s_waitcnt lgkmcnt(10)\n"
"v_mfma_f32_32x32x16_bf16 v[96:111], v[228:231], v[136:139], v[96:111]\n"
"ds_read_b64_tr_b16 v[228:229], v5 offset:47104\n"
"ds_read_b64_tr_b16 v[230:231], v5 offset:48128\n"
"s_waitcnt lgkmcnt(10)\n"
"v_mfma_f32_32x32x16_bf16 v[16:31], v[208:211], v[140:143], v[16:31]\n"
"s_barrier\n"
"s_waitcnt lgkmcnt(8)\n"
"v_mfma_f32_32x32x16_bf16 v[32:47], v[212:215], v[140:143], v[32:47]\n"
"ds_read_b64_tr_b16 v[208:209], v6 offset:0\n"
"ds_read_b64_tr_b16 v[210:211], v6 offset:1024\n"
"ds_read_b64_tr_b16 v[212:213], v7 offset:0\n"
"ds_read_b64_tr_b16 v[214:215], v7 offset:1024\n"
"s_waitcnt lgkmcnt(10)\n"
"v_mfma_f32_32x32x16_bf16 v[48:63], v[216:219], v[140:143], v[48:63]\n"
"ds_read_b64_tr_b16 v[216:217], v6 offset:8192\n"
"ds_read_b64_tr_b16 v[218:219], v6 offset:9216\n"
"s_waitcnt lgkmcnt(10)\n"
"v_mfma_f32_32x32x16_bf16 v[64:79], v[220:223], v[140:143], v[64:79]\n"
"ds_read_b64_tr_b16 v[220:221], v7 offset:8192\n"
"ds_read_b64_tr_b16 v[222:223], v7 offset:9216\n"
"s_waitcnt lgkmcnt(10)\n"
"v_mfma_f32_32x32x16_bf16 v[80:95], v[224:227], v[140:143], v[80:95]\n"
"ds_read_b64_tr_b16 v[224:225], v6 offset:16384\n"
"ds_read_b64_tr_b16 v[226:227], v6 offset:17408\n"
"s_waitcnt lgkmcnt(10)\n"
"v_mfma_f32_32x32x16_bf16 v[96:111], v[228:231], v[140:143], v[96:111]\n"
"ds_read_b64_tr_b16 v[228:229], v7 offset:16384\n"
"ds_read_b64_tr_b16 v[230:231], v7 offset:17408\n"
"s_waitcnt vmcnt(16)\n"
"s_waitcnt lgkmcnt(10)\n"
"v_mfma_f32_32x32x16_bf16 v[16:31], v[208:211], v[144:147], v[16:31]\n"
"ds_read_b64_tr_b16 v[208:209], v6 offset:2048\n"
"ds_read_b64_tr_b16 v[210:211], v6 offset:3072\n"
"s_waitcnt lgkmcnt(10)\n"
"v_mfma_f32_32x32x16_bf16 v[32:47], v[212:215], v[144:147], v[32:47]\n"
"ds_read_b64_tr_b16 v[212:213], v7 offset:2048\n"
"ds_read_b64_tr_b16 v[214:215], v7 offset:3072\n"
"s_waitcnt lgkmcnt(10)\n"
"v_mfma_f32_32x32x16_bf16 v[48:63], v[216:219], v[144:147], v[48:63]\n"
"ds_read_b64_tr_b16 v[216:217], v6 offset:10240\n"
"ds_read_b64_tr_b16 v[218:219], v6 offset:11264\n"
"s_add_u32 s76, s61, 53248\n"
"buffer_load_dwordx4 v[128:131], v2, s[44:47], s76 offen\n"
"s_waitcnt lgkmcnt(10)\n"
"v_mfma_f32_32x32x16_bf16 v[64:79], v[220:223], v[144:147], v[64:79]\n"
"ds_read_b64_tr_b16 v[220:221], v7 offset:10240\n"
"ds_read_b64_tr_b16 v[222:223], v7 offset:11264\n"
"s_waitcnt lgkmcnt(10)\n"
"v_mfma_f32_32x32x16_bf16 v[80:95], v[224:227], v[144:147], v[80:95]\n"
"ds_read_b64_tr_b16 v[224:225], v6 offset:18432\n"
"ds_read_b64_tr_b16 v[226:227], v6 offset:19456\n"
"s_waitcnt lgkmcnt(10)\n"
"v_mfma_f32_32x32x16_bf16 v[96:111], v[228:231], v[144:147], v[96:111]\n"
"ds_read_b64_tr_b16 v[228:229], v7 offset:18432\n"
"ds_read_b64_tr_b16 v[230:231], v7 offset:19456\n"
"buffer_load_dwordx4 v[132:135], v2, s[44:47], s76 offen offset:1024\n"
"s_waitcnt lgkmcnt(10)\n"
"v_mfma_f32_32x32x16_bf16 v[16:31], v[208:211], v[148:151], v[16:31]\n"
"ds_read_b64_tr_b16 v[208:209], v6 offset:4096\n"
"ds_read_b64_tr_b16 v[210:211], v6 offset:5120\n"
"s_waitcnt lgkmcnt(10)\n"
"v_mfma_f32_32x32x16_bf16 v[32:47], v[212:215], v[148:151], v[32:47]\n"
"ds_read_b64_tr_b16 v[212:213], v7 offset:4096\n"
"ds_read_b64_tr_b16 v[214:215], v7 offset:5120\n"
"s_waitcnt lgkmcnt(10)\n"
"v_mfma_f32_32x32x16_bf16 v[48:63], v[216:219], v[148:151], v[48:63]\n"
"ds_read_b64_tr_b16 v[216:217], v6 offset:12288\n"
"ds_read_b64_tr_b16 v[218:219], v6 offset:13312\n"
"buffer_load_dwordx4 v[136:139], v2, s[44:47], s76 offen offset:2048\n"
"s_waitcnt lgkmcnt(10)\n"
"v_mfma_f32_32x32x16_bf16 v[64:79], v[220:223], v[148:151], v[64:79]\n"
"ds_read_b64_tr_b16 v[220:221], v7 offset:12288\n"
"ds_read_b64_tr_b16 v[222:223], v7 offset:13312\n"
"s_waitcnt lgkmcnt(10)\n"
"v_mfma_f32_32x32x16_bf16 v[80:95], v[224:227], v[148:151], v[80:95]\n"
"ds_read_b64_tr_b16 v[224:225], v6 offset:20480\n"
"ds_read_b64_tr_b16 v[226:227], v6 offset:21504\n"
"s_waitcnt lgkmcnt(10)\n"
"v_mfma_f32_32x32x16_bf16 v[96:111], v[228:231], v[148:151], v[96:111]\n"
"ds_read_b64_tr_b16 v[228:229], v7 offset:20480\n"
"ds_read_b64_tr_b16 v[230:231], v7 offset:21504\n"
"buffer_load_dwordx4 v[140:143], v2, s[44:47], s76 offen offset:3072\n"
"s_waitcnt lgkmcnt(10)\n"
"v_mfma_f32_32x32x16_bf16 v[16:31], v[208:211], v[152:155], v[16:31]\n"
"ds_read_b64_tr_b16 v[208:209], v6 offset:6144\n"
"ds_read_b64_tr_b16 v[210:211], v6 offset:7168\n"
"s_waitcnt lgkmcnt(10)\n"
"v_mfma_f32_32x32x16_bf16 v[32:47], v[212:215], v[152:155], v[32:47]\n"
"ds_read_b64_tr_b16 v[212:213], v7 offset:6144\n"
"ds_read_b64_tr_b16 v[214:215], v7 offset:7168\n"
"s_waitcnt lgkmcnt(10)\n"
"v_mfma_f32_32x32x16_bf16 v[48:63], v[216:219], v[152:155], v[48:63]\n"
"ds_read_b64_tr_b16 v[216:217], v6 offset:14336\n"
"ds_read_b64_tr_b16 v[218:219], v6 offset:15360\n"
"s_waitcnt lgkmcnt(10)\n"
"v_mfma_f32_32x32x16_bf16 v[64:79], v[220:223], v[152:155], v[64:79]\n"
"ds_read_b64_tr_b16 v[220:221], v7 offset:14336\n"
"ds_read_b64_tr_b16 v[222:223], v7 offset:15360\n"
"s_waitcnt lgkmcnt(10)\n"
"v_mfma_f32_32x32x16_bf16 v[80:95], v[224:227], v[152:155], v[80:95]\n"
"ds_read_b64_tr_b16 v[224:225], v6 offset:22528\n"
"ds_read_b64_tr_b16 v[226:227], v6 offset:23552\n"
"s_waitcnt lgkmcnt(10)\n"
"v_mfma_f32_32x32x16_bf16 v[96:111], v[228:231], v[152:155], v[96:111]\n"
"ds_read_b64_tr_b16 v[228:229], v7 offset:22528\n"
"ds_read_b64_tr_b16 v[230:231], v7 offset:23552\n"
"s_waitcnt lgkmcnt(10)\n"
"v_mfma_f32_32x32x16_bf16 v[16:31], v[208:211], v[156:159], v[16:31]\n"
"s_barrier\n"
"s_waitcnt lgkmcnt(8)\n"
"v_mfma_f32_32x32x16_bf16 v[32:47], v[212:215], v[156:159], v[32:47]\n"
"ds_read_b64_tr_b16 v[208:209], v6 offset:24576\n"
"ds_read_b64_tr_b16 v[210:211], v6 offset:25600\n"
"ds_read_b64_tr_b16 v[212:213], v7 offset:24576\n"
"ds_read_b64_tr_b16 v[214:215], v7 offset:25600\n"
"s_waitcnt lgkmcnt(10)\n"
"v_mfma_f32_32x32x16_bf16 v[48:63], v[216:219], v[156:159], v[48:63]\n"
"ds_read_b64_tr_b16 v[216:217], v6 offset:32768\n"
"ds_read_b64_tr_b16 v[218:219], v6 offset:33792\n"
"s_waitcnt lgkmcnt(10)\n"
"v_mfma_f32_32x32x16_bf16 v[64:79], v[220:223], v[156:159], v[64:79]\n"
"ds_read_b64_tr_b16 v[220:221], v7 offset:32768\n"
"ds_read_b64_tr_b16 v[222:223], v7 offset:33792\n"
"s_waitcnt lgkmcnt(10)\n"
"v_mfma_f32_32x32x16_bf16 v[80:95], v[224:227], v[156:159], v[80:95]\n"
"ds_read_b64_tr_b16 v[224:225], v6 offset:40960\n"
"ds_read_b64_tr_b16 v[226:227], v6 offset:41984\n"
"s_waitcnt lgkmcnt(10)\n"
"v_mfma_f32_32x32x16_bf16 v[96:111], v[228:231], v[156:159], v[96:111]\n"
"ds_read_b64_tr_b16 v[228:229], v7 offset:40960\n"
"ds_read_b64_tr_b16 v[230:231], v7 offset:41984\n"
"s_waitcnt vmcnt(16)\n"
"s_waitcnt lgkmcnt(10)\n"
"v_mfma_f32_32x32x16_bf16 v[16:31], v[208:211], v[160:163], v[16:31]\n"
"ds_read_b64_tr_b16 v[208:209], v6 offset:26624\n"
"ds_read_b64_tr_b16 v[210:211], v6 offset:27648\n"
"s_waitcnt lgkmcnt(10)\n"
"v_mfma_f32_32x32x16_bf16 v[32:47], v[212:215], v[160:163], v[32:47]\n"
"ds_read_b64_tr_b16 v[212:213], v7 offset:26624\n"
"ds_read_b64_tr_b16 v[214:215], v7 offset:27648\n"
"s_waitcnt lgkmcnt(10)\n"
"v_mfma_f32_32x32x16_bf16 v[48:63], v[216:219], v[160:163], v[48:63]\n"
"ds_read_b64_tr_b16 v[216:217], v6 offset:34816\n"
"ds_read_b64_tr_b16 v[218:219], v6 offset:35840\n"
"s_add_u32 s76, s61, 57344\n"
"buffer_load_dwordx4 v[144:147], v2, s[44:47], s76 offen\n"
"s_waitcnt lgkmcnt(10)\n"
"v_mfma_f32_32x32x16_bf16 v[64:79], v[220:223], v[160:163], v[64:79]\n"
"ds_read_b64_tr_b16 v[220:221], v7 offset:34816\n"
"ds_read_b64_tr_b16 v[222:223], v7 offset:35840\n"
"s_waitcnt lgkmcnt(10)\n"
"v_mfma_f32_32x32x16_bf16 v[80:95], v[224:227], v[160:163], v[80:95]\n"
"ds_read_b64_tr_b16 v[224:225], v6 offset:43008\n"
"ds_read_b64_tr_b16 v[226:227], v6 offset:44032\n"
"s_waitcnt lgkmcnt(10)\n"
"v_mfma_f32_32x32x16_bf16 v[96:111], v[228:231], v[160:163], v[96:111]\n"
"ds_read_b64_tr_b16 v[228:229], v7 offset:43008\n"
"ds_read_b64_tr_b16 v[230:231], v7 offset:44032\n"
"buffer_load_dwordx4 v[148:151], v2, s[44:47], s76 offen offset:1024\n"
"s_waitcnt lgkmcnt(10)\n"
"v_mfma_f32_32x32x16_bf16 v[16:31], v[208:211], v[164:167], v[16:31]\n"
"ds_read_b64_tr_b16 v[208:209], v6 offset:28672\n"
"ds_read_b64_tr_b16 v[210:211], v6 offset:29696\n"
"s_waitcnt lgkmcnt(10)\n"
"v_mfma_f32_32x32x16_bf16 v[32:47], v[212:215], v[164:167], v[32:47]\n"
"ds_read_b64_tr_b16 v[212:213], v7 offset:28672\n"
"ds_read_b64_tr_b16 v[214:215], v7 offset:29696\n"
"s_waitcnt lgkmcnt(10)\n"
"v_mfma_f32_32x32x16_bf16 v[48:63], v[216:219], v[164:167], v[48:63]\n"
"ds_read_b64_tr_b16 v[216:217], v6 offset:36864\n"
"ds_read_b64_tr_b16 v[218:219], v6 offset:37888\n"
"buffer_load_dwordx4 v[152:155], v2, s[44:47], s76 offen offset:2048\n"
"s_waitcnt lgkmcnt(10)\n"
"v_mfma_f32_32x32x16_bf16 v[64:79], v[220:223], v[164:167], v[64:79]\n"
"ds_read_b64_tr_b16 v[220:221], v7 offset:36864\n"
"ds_read_b64_tr_b16 v[222:223], v7 offset:37888\n"
"s_waitcnt lgkmcnt(10)\n"
"v_mfma_f32_32x32x16_bf16 v[80:95], v[224:227], v[164:167], v[80:95]\n"
"ds_read_b64_tr_b16 v[224:225], v6 offset:45056\n"
"ds_read_b64_tr_b16 v[226:227], v6 offset:46080\n"
"s_waitcnt lgkmcnt(10)\n"
"v_mfma_f32_32x32x16_bf16 v[96:111], v[228:231], v[164:167], v[96:111]\n"
"ds_read_b64_tr_b16 v[228:229], v7 offset:45056\n"
"ds_read_b64_tr_b16 v[230:231], v7 offset:46080\n"
"buffer_load_dwordx4 v[156:159], v2, s[44:47], s76 offen offset:3072\n"
"s_waitcnt lgkmcnt(10)\n"
"v_mfma_f32_32x32x16_bf16 v[16:31], v[208:211], v[168:171], v[16:31]\n"
"ds_read_b64_tr_b16 v[208:209], v6 offset:30720\n"
"ds_read_b64_tr_b16 v[210:211], v6 offset:31744\n"
"s_waitcnt lgkmcnt(10)\n"
"v_mfma_f32_32x32x16_bf16 v[32:47], v[212:215], v[168:171], v[32:47]\n"
"ds_read_b64_tr_b16 v[212:213], v7 offset:30720\n"
"ds_read_b64_tr_b16 v[214:215], v7 offset:31744\n"
"s_waitcnt lgkmcnt(10)\n"
"v_mfma_f32_32x32x16_bf16 v[48:63], v[216:219], v[168:171], v[48:63]\n"
"ds_read_b64_tr_b16 v[216:217], v6 offset:38912\n"
"ds_read_b64_tr_b16 v[218:219], v6 offset:39936\n"
"s_waitcnt lgkmcnt(10)\n"
"v_mfma_f32_32x32x16_bf16 v[64:79], v[220:223], v[168:171], v[64:79]\n"
"ds_read_b64_tr_b16 v[220:221], v7 offset:38912\n"
"ds_read_b64_tr_b16 v[222:223], v7 offset:39936\n"
"s_waitcnt lgkmcnt(10)\n"
"v_mfma_f32_32x32x16_bf16 v[80:95], v[224:227], v[168:171], v[80:95]\n"
"ds_read_b64_tr_b16 v[224:225], v6 offset:47104\n"
"ds_read_b64_tr_b16 v[226:227], v6 offset:48128\n"
"s_waitcnt lgkmcnt(10)\n"
"v_mfma_f32_32x32x16_bf16 v[96:111], v[228:231], v[168:171], v[96:111]\n"
"ds_read_b64_tr_b16 v[228:229], v7 offset:47104\n"
"ds_read_b64_tr_b16 v[230:231], v7 offset:48128\n"
"s_waitcnt lgkmcnt(10)\n"
"v_mfma_f32_32x32x16_bf16 v[16:31], v[208:211], v[172:175], v[16:31]\n"
"s_barrier\n"
"s_waitcnt lgkmcnt(8)\n"
"v_mfma_f32_32x32x16_bf16 v[32:47], v[212:215], v[172:175], v[32:47]\n"
"ds_read_b64_tr_b16 v[208:209], v8 offset:0\n"
"ds_read_b64_tr_b16 v[210:211], v8 offset:1024\n"
"ds_read_b64_tr_b16 v[212:213], v9 offset:0\n"
"ds_read_b64_tr_b16 v[214:215], v9 offset:1024\n"
"s_waitcnt lgkmcnt(10)\n"
"v_mfma_f32_32x32x16_bf16 v[48:63], v[216:219], v[172:175], v[48:63]\n"
"ds_read_b64_tr_b16 v[216:217], v8 offset:8192\n"
"ds_read_b64_tr_b16 v[218:219], v8 offset:9216\n"
"s_waitcnt lgkmcnt(10)\n"
"v_mfma_f32_32x32x16_bf16 v[64:79], v[220:223], v[172:175], v[64:79]\n"
"ds_read_b64_tr_b16 v[220:221], v9 offset:8192\n"
"ds_read_b64_tr_b16 v[222:223], v9 offset:9216\n"
"s_waitcnt lgkmcnt(10)\n"
"v_mfma_f32_32x32x16_bf16 v[80:95], v[224:227], v[172:175], v[80:95]\n"
"ds_read_b64_tr_b16 v[224:225], v8 offset:16384\n"
"ds_read_b64_tr_b16 v[226:227], v8 offset:17408\n"
"s_waitcnt lgkmcnt(10)\n"
"v_mfma_f32_32x32x16_bf16 v[96:111], v[228:231], v[172:175], v[96:111]\n"
"ds_read_b64_tr_b16 v[228:229], v9 offset:16384\n"
"ds_read_b64_tr_b16 v[230:231], v9 offset:17408\n"
"s_waitcnt vmcnt(16)\n"
"s_waitcnt lgkmcnt(10)\n"
"v_mfma_f32_32x32x16_bf16 v[16:31], v[208:211], v[176:179], v[16:31]\n"
"ds_read_b64_tr_b16 v[208:209], v8 offset:2048\n"
"ds_read_b64_tr_b16 v[210:211], v8 offset:3072\n"
"s_waitcnt lgkmcnt(10)\n"
"v_mfma_f32_32x32x16_bf16 v[32:47], v[212:215], v[176:179], v[32:47]\n"
"ds_read_b64_tr_b16 v[212:213], v9 offset:2048\n"
"ds_read_b64_tr_b16 v[214:215], v9 offset:3072\n"
"s_waitcnt lgkmcnt(10)\n"
"v_mfma_f32_32x32x16_bf16 v[48:63], v[216:219], v[176:179], v[48:63]\n"
"ds_read_b64_tr_b16 v[216:217], v8 offset:10240\n"
"ds_read_b64_tr_b16 v[218:219], v8 offset:11264\n"
"s_waitcnt lgkmcnt(10)\n"
"v_mfma_f32_32x32x16_bf16 v[64:79], v[220:223], v[176:179], v[64:79]\n"
"ds_read_b64_tr_b16 v[220:221], v9 offset:10240\n"
"ds_read_b64_tr_b16 v[222:223], v9 offset:11264\n"
"s_waitcnt lgkmcnt(10)\n"
"v_mfma_f32_32x32x16_bf16 v[80:95], v[224:227], v[176:179], v[80:95]\n"
"ds_read_b64_tr_b16 v[224:225], v8 offset:18432\n"
"ds_read_b64_tr_b16 v[226:227], v8 offset:19456\n"
"s_waitcnt lgkmcnt(10)\n"
"v_mfma_f32_32x32x16_bf16 v[96:111], v[228:231], v[176:179], v[96:111]\n"
"ds_read_b64_tr_b16 v[228:229], v9 offset:18432\n"
"ds_read_b64_tr_b16 v[230:231], v9 offset:19456\n"
"s_waitcnt lgkmcnt(10)\n"
"v_mfma_f32_32x32x16_bf16 v[16:31], v[208:211], v[180:183], v[16:31]\n"
"ds_read_b64_tr_b16 v[208:209], v8 offset:4096\n"
"ds_read_b64_tr_b16 v[210:211], v8 offset:5120\n"
"s_waitcnt lgkmcnt(10)\n"
"v_mfma_f32_32x32x16_bf16 v[32:47], v[212:215], v[180:183], v[32:47]\n"
"ds_read_b64_tr_b16 v[212:213], v9 offset:4096\n"
"ds_read_b64_tr_b16 v[214:215], v9 offset:5120\n"
"s_waitcnt lgkmcnt(10)\n"
"v_mfma_f32_32x32x16_bf16 v[48:63], v[216:219], v[180:183], v[48:63]\n"
"ds_read_b64_tr_b16 v[216:217], v8 offset:12288\n"
"ds_read_b64_tr_b16 v[218:219], v8 offset:13312\n"
"s_waitcnt lgkmcnt(10)\n"
"v_mfma_f32_32x32x16_bf16 v[64:79], v[220:223], v[180:183], v[64:79]\n"
"ds_read_b64_tr_b16 v[220:221], v9 offset:12288\n"
"ds_read_b64_tr_b16 v[222:223], v9 offset:13312\n"
"s_waitcnt lgkmcnt(10)\n"
"v_mfma_f32_32x32x16_bf16 v[80:95], v[224:227], v[180:183], v[80:95]\n"
"ds_read_b64_tr_b16 v[224:225], v8 offset:20480\n"
"ds_read_b64_tr_b16 v[226:227], v8 offset:21504\n"
"s_waitcnt lgkmcnt(10)\n"
"v_mfma_f32_32x32x16_bf16 v[96:111], v[228:231], v[180:183], v[96:111]\n"
"ds_read_b64_tr_b16 v[228:229], v9 offset:20480\n"
"ds_read_b64_tr_b16 v[230:231], v9 offset:21504\n"
"s_waitcnt lgkmcnt(10)\n"
"v_mfma_f32_32x32x16_bf16 v[16:31], v[208:211], v[184:187], v[16:31]\n"
"ds_read_b64_tr_b16 v[208:209], v8 offset:6144\n"
"ds_read_b64_tr_b16 v[210:211], v8 offset:7168\n"
"s_waitcnt lgkmcnt(10)\n"
"v_mfma_f32_32x32x16_bf16 v[32:47], v[212:215], v[184:187], v[32:47]\n"
"ds_read_b64_tr_b16 v[212:213], v9 offset:6144\n"
"ds_read_b64_tr_b16 v[214:215], v9 offset:7168\n"
"s_waitcnt lgkmcnt(10)\n"
"v_mfma_f32_32x32x16_bf16 v[48:63], v[216:219], v[184:187], v[48:63]\n"
"ds_read_b64_tr_b16 v[216:217], v8 offset:14336\n"
"ds_read_b64_tr_b16 v[218:219], v8 offset:15360\n"
"s_waitcnt lgkmcnt(10)\n"
"v_mfma_f32_32x32x16_bf16 v[64:79], v[220:223], v[184:187], v[64:79]\n"
"ds_read_b64_tr_b16 v[220:221], v9 offset:14336\n"
"ds_read_b64_tr_b16 v[222:223], v9 offset:15360\n"
"s_waitcnt lgkmcnt(10)\n"
"v_mfma_f32_32x32x16_bf16 v[80:95], v[224:227], v[184:187], v[80:95]\n"
"ds_read_b64_tr_b16 v[224:225], v8 offset:22528\n"
"ds_read_b64_tr_b16 v[226:227], v8 offset:23552\n"
"s_waitcnt lgkmcnt(10)\n"
"v_mfma_f32_32x32x16_bf16 v[96:111], v[228:231], v[184:187], v[96:111]\n"
"ds_read_b64_tr_b16 v[228:229], v9 offset:22528\n"
"ds_read_b64_tr_b16 v[230:231], v9 offset:23552\n"
"s_waitcnt lgkmcnt(10)\n"
"v_mfma_f32_32x32x16_bf16 v[16:31], v[208:211], v[188:191], v[16:31]\n"
"s_barrier\n"
"s_waitcnt lgkmcnt(8)\n"
"v_mfma_f32_32x32x16_bf16 v[32:47], v[212:215], v[188:191], v[32:47]\n"
"ds_read_b64_tr_b16 v[208:209], v8 offset:24576\n"
"ds_read_b64_tr_b16 v[210:211], v8 offset:25600\n"
"ds_read_b64_tr_b16 v[212:213], v9 offset:24576\n"
"ds_read_b64_tr_b16 v[214:215], v9 offset:25600\n"
"s_waitcnt lgkmcnt(10)\n"
"v_mfma_f32_32x32x16_bf16 v[48:63], v[216:219], v[188:191], v[48:63]\n"
"ds_read_b64_tr_b16 v[216:217], v8 offset:32768\n"
"ds_read_b64_tr_b16 v[218:219], v8 offset:33792\n"
"s_waitcnt lgkmcnt(10)\n"
"v_mfma_f32_32x32x16_bf16 v[64:79], v[220:223], v[188:191], v[64:79]\n"
"ds_read_b64_tr_b16 v[220:221], v9 offset:32768\n"
"ds_read_b64_tr_b16 v[222:223], v9 offset:33792\n"
"s_waitcnt lgkmcnt(10)\n"
"v_mfma_f32_32x32x16_bf16 v[80:95], v[224:227], v[188:191], v[80:95]\n"
"ds_read_b64_tr_b16 v[224:225], v8 offset:40960\n"
"ds_read_b64_tr_b16 v[226:227], v8 offset:41984\n"
"s_waitcnt lgkmcnt(10)\n"
"v_mfma_f32_32x32x16_bf16 v[96:111], v[228:231], v[188:191], v[96:111]\n"
"ds_read_b64_tr_b16 v[228:229], v9 offset:40960\n"
"ds_read_b64_tr_b16 v[230:231], v9 offset:41984\n"
"s_waitcnt vmcnt(12)\n"
"s_waitcnt lgkmcnt(10)\n"
"v_mfma_f32_32x32x16_bf16 v[16:31], v[208:211], v[192:195], v[16:31]\n"
"ds_read_b64_tr_b16 v[208:209], v8 offset:26624\n"
"ds_read_b64_tr_b16 v[210:211], v8 offset:27648\n"
"s_waitcnt lgkmcnt(10)\n"
"v_mfma_f32_32x32x16_bf16 v[32:47], v[212:215], v[192:195], v[32:47]\n"
"ds_read_b64_tr_b16 v[212:213], v9 offset:26624\n"
"ds_read_b64_tr_b16 v[214:215], v9 offset:27648\n"
"s_waitcnt lgkmcnt(10)\n"
"v_mfma_f32_32x32x16_bf16 v[48:63], v[216:219], v[192:195], v[48:63]\n"
"ds_read_b64_tr_b16 v[216:217], v8 offset:34816\n"
"ds_read_b64_tr_b16 v[218:219], v8 offset:35840\n"
"s_waitcnt lgkmcnt(10)\n"
"v_mfma_f32_32x32x16_bf16 v[64:79], v[220:223], v[192:195], v[64:79]\n"
"ds_read_b64_tr_b16 v[220:221], v9 offset:34816\n"
"ds_read_b64_tr_b16 v[222:223], v9 offset:35840\n"
"s_waitcnt lgkmcnt(10)\n"
"v_mfma_f32_32x32x16_bf16 v[80:95], v[224:227], v[192:195], v[80:95]\n"
"ds_read_b64_tr_b16 v[224:225], v8 offset:43008\n"
"ds_read_b64_tr_b16 v[226:227], v8 offset:44032\n"
"s_waitcnt lgkmcnt(10)\n"
"v_mfma_f32_32x32x16_bf16 v[96:111], v[228:231], v[192:195], v[96:111]\n"
"ds_read_b64_tr_b16 v[228:229], v9 offset:43008\n"
"ds_read_b64_tr_b16 v[230:231], v9 offset:44032\n"
"s_waitcnt lgkmcnt(10)\n"
"v_mfma_f32_32x32x16_bf16 v[16:31], v[208:211], v[196:199], v[16:31]\n"
"ds_read_b64_tr_b16 v[208:209], v8 offset:28672\n"
"ds_read_b64_tr_b16 v[210:211], v8 offset:29696\n"
"s_waitcnt lgkmcnt(10)\n"
"v_mfma_f32_32x32x16_bf16 v[32:47], v[212:215], v[196:199], v[32:47]\n"
"ds_read_b64_tr_b16 v[212:213], v9 offset:28672\n"
"ds_read_b64_tr_b16 v[214:215], v9 offset:29696\n"
"s_waitcnt lgkmcnt(10)\n"
"v_mfma_f32_32x32x16_bf16 v[48:63], v[216:219], v[196:199], v[48:63]\n"
"ds_read_b64_tr_b16 v[216:217], v8 offset:36864\n"
"ds_read_b64_tr_b16 v[218:219], v8 offset:37888\n"
"s_waitcnt lgkmcnt(10)\n"
"v_mfma_f32_32x32x16_bf16 v[64:79], v[220:223], v[196:199], v[64:79]\n"
"ds_read_b64_tr_b16 v[220:221], v9 offset:36864\n"
"ds_read_b64_tr_b16 v[222:223], v9 offset:37888\n"
"s_waitcnt lgkmcnt(10)\n"
"v_mfma_f32_32x32x16_bf16 v[80:95], v[224:227], v[196:199], v[80:95]\n"
"ds_read_b64_tr_b16 v[224:225], v8 offset:45056\n"
"ds_read_b64_tr_b16 v[226:227], v8 offset:46080\n"
"s_waitcnt lgkmcnt(10)\n"
"v_mfma_f32_32x32x16_bf16 v[96:111], v[228:231], v[196:199], v[96:111]\n"
"ds_read_b64_tr_b16 v[228:229], v9 offset:45056\n"
"ds_read_b64_tr_b16 v[230:231], v9 offset:46080\n"
"s_waitcnt lgkmcnt(10)\n"
"v_mfma_f32_32x32x16_bf16 v[16:31], v[208:211], v[200:203], v[16:31]\n"
"ds_read_b64_tr_b16 v[208:209], v8 offset:30720\n"
"ds_read_b64_tr_b16 v[210:211], v8 offset:31744\n"
"s_waitcnt lgkmcnt(10)\n"
"v_mfma_f32_32x32x16_bf16 v[32:47], v[212:215], v[200:203], v[32:47]\n"
"ds_read_b64_tr_b16 v[212:213], v9 offset:30720\n"
"ds_read_b64_tr_b16 v[214:215], v9 offset:31744\n"
"s_waitcnt lgkmcnt(10)\n"
"v_mfma_f32_32x32x16_bf16 v[48:63], v[216:219], v[200:203], v[48:63]\n"
"ds_read_b64_tr_b16 v[216:217], v8 offset:38912\n"
"ds_read_b64_tr_b16 v[218:219], v8 offset:39936\n"
"s_waitcnt lgkmcnt(10)\n"
"v_mfma_f32_32x32x16_bf16 v[64:79], v[220:223], v[200:203], v[64:79]\n"
"ds_read_b64_tr_b16 v[220:221], v9 offset:38912\n"
"ds_read_b64_tr_b16 v[222:223], v9 offset:39936\n"
"s_waitcnt lgkmcnt(10)\n"
"v_mfma_f32_32x32x16_bf16 v[80:95], v[224:227], v[200:203], v[80:95]\n"
"ds_read_b64_tr_b16 v[224:225], v8 offset:47104\n"
"ds_read_b64_tr_b16 v[226:227], v8 offset:48128\n"
"s_waitcnt lgkmcnt(10)\n"
"v_mfma_f32_32x32x16_bf16 v[96:111], v[228:231], v[200:203], v[96:111]\n"
"ds_read_b64_tr_b16 v[228:229], v9 offset:47104\n"
"ds_read_b64_tr_b16 v[230:231], v9 offset:48128\n"
"s_waitcnt lgkmcnt(10)\n"
"v_mfma_f32_32x32x16_bf16 v[16:31], v[208:211], v[204:207], v[16:31]\n"
"s_barrier\n"
"s_waitcnt lgkmcnt(8)\n"
"v_mfma_f32_32x32x16_bf16 v[32:47], v[212:215], v[204:207], v[32:47]\n"
"ds_read_b64_tr_b16 v[208:209], v4 offset:0\n"
"ds_read_b64_tr_b16 v[210:211], v4 offset:1024\n"
"ds_read_b64_tr_b16 v[212:213], v5 offset:0\n"
"ds_read_b64_tr_b16 v[214:215], v5 offset:1024\n"
"s_waitcnt lgkmcnt(10)\n"
"v_mfma_f32_32x32x16_bf16 v[48:63], v[216:219], v[204:207], v[48:63]\n"
"ds_read_b64_tr_b16 v[216:217], v4 offset:8192\n"
"ds_read_b64_tr_b16 v[218:219], v4 offset:9216\n"
"s_waitcnt lgkmcnt(10)\n"
"v_mfma_f32_32x32x16_bf16 v[64:79], v[220:223], v[204:207], v[64:79]\n"
"ds_read_b64_tr_b16 v[220:221], v5 offset:8192\n"
"ds_read_b64_tr_b16 v[222:223], v5 offset:9216\n"
"s_waitcnt lgkmcnt(10)\n"
"v_mfma_f32_32x32x16_bf16 v[80:95], v[224:227], v[204:207], v[80:95]\n"
"ds_read_b64_tr_b16 v[224:225], v4 offset:16384\n"
"ds_read_b64_tr_b16 v[226:227], v4 offset:17408\n"
"s_waitcnt lgkmcnt(10)\n"
"v_mfma_f32_32x32x16_bf16 v[96:111], v[228:231], v[204:207], v[96:111]\n"
"ds_read_b64_tr_b16 v[228:229], v5 offset:16384\n"
"ds_read_b64_tr_b16 v[230:231], v5 offset:17408\n"
"s_waitcnt vmcnt(8)\n"
"s_waitcnt lgkmcnt(10)\n"
"v_mfma_f32_32x32x16_bf16 v[16:31], v[208:211], v[112:115], v[16:31]\n"
"ds_read_b64_tr_b16 v[208:209], v4 offset:2048\n"
"ds_read_b64_tr_b16 v[210:211], v4 offset:3072\n"
"s_waitcnt lgkmcnt(10)\n"
"v_mfma_f32_32x32x16_bf16 v[32:47], v[212:215], v[112:115], v[32:47]\n"
"ds_read_b64_tr_b16 v[212:213], v5 offset:2048\n"
"ds_read_b64_tr_b16 v[214:215], v5 offset:3072\n"
"s_waitcnt lgkmcnt(10)\n"
"v_mfma_f32_32x32x16_bf16 v[48:63], v[216:219], v[112:115], v[48:63]\n"
"ds_read_b64_tr_b16 v[216:217], v4 offset:10240\n"
"ds_read_b64_tr_b16 v[218:219], v4 offset:11264\n"
"s_waitcnt lgkmcnt(10)\n"
"v_mfma_f32_32x32x16_bf16 v[64:79], v[220:223], v[112:115], v[64:79]\n"
"ds_read_b64_tr_b16 v[220:221], v5 offset:10240\n"
"ds_read_b64_tr_b16 v[222:223], v5 offset:11264\n"
"s_waitcnt lgkmcnt(10)\n"
"v_mfma_f32_32x32x16_bf16 v[80:95], v[224:227], v[112:115], v[80:95]\n"
"ds_read_b64_tr_b16 v[224:225], v4 offset:18432\n"
"ds_read_b64_tr_b16 v[226:227], v4 offset:19456\n"
"s_waitcnt lgkmcnt(10)\n"
"v_mfma_f32_32x32x16_bf16 v[96:111], v[228:231], v[112:115], v[96:111]\n"
"ds_read_b64_tr_b16 v[228:229], v5 offset:18432\n"
"ds_read_b64_tr_b16 v[230:231], v5 offset:19456\n"
"s_waitcnt lgkmcnt(10)\n"
"v_mfma_f32_32x32x16_bf16 v[16:31], v[208:211], v[116:119], v[16:31]\n"
"ds_read_b64_tr_b16 v[208:209], v4 offset:4096\n"
"ds_read_b64_tr_b16 v[210:211], v4 offset:5120\n"
"s_waitcnt lgkmcnt(10)\n"
"v_mfma_f32_32x32x16_bf16 v[32:47], v[212:215], v[116:119], v[32:47]\n"
"ds_read_b64_tr_b16 v[212:213], v5 offset:4096\n"
"ds_read_b64_tr_b16 v[214:215], v5 offset:5120\n"
"s_waitcnt lgkmcnt(10)\n"
"v_mfma_f32_32x32x16_bf16 v[48:63], v[216:219], v[116:119], v[48:63]\n"
"ds_read_b64_tr_b16 v[216:217], v4 offset:12288\n"
"ds_read_b64_tr_b16 v[218:219], v4 offset:13312\n"
"s_waitcnt lgkmcnt(10)\n"
"v_mfma_f32_32x32x16_bf16 v[64:79], v[220:223], v[116:119], v[64:79]\n"
"ds_read_b64_tr_b16 v[220:221], v5 offset:12288\n"
"ds_read_b64_tr_b16 v[222:223], v5 offset:13312\n"
"s_waitcnt lgkmcnt(10)\n"
"v_mfma_f32_32x32x16_bf16 v[80:95], v[224:227], v[116:119], v[80:95]\n"
"ds_read_b64_tr_b16 v[224:225], v4 offset:20480\n"
"ds_read_b64_tr_b16 v[226:227], v4 offset:21504\n"
"s_waitcnt lgkmcnt(10)\n"
"v_mfma_f32_32x32x16_bf16 v[96:111], v[228:231], v[116:119], v[96:111]\n"
"ds_read_b64_tr_b16 v[228:229], v5 offset:20480\n"
"ds_read_b64_tr_b16 v[230:231], v5 offset:21504\n"
"s_waitcnt lgkmcnt(10)\n"
"v_mfma_f32_32x32x16_bf16 v[16:31], v[208:211], v[120:123], v[16:31]\n"
"ds_read_b64_tr_b16 v[208:209], v4 offset:6144\n"
"ds_read_b64_tr_b16 v[210:211], v4 offset:7168\n"
"s_waitcnt lgkmcnt(10)\n"
"v_mfma_f32_32x32x16_bf16 v[32:47], v[212:215], v[120:123], v[32:47]\n"
"ds_read_b64_tr_b16 v[212:213], v5 offset:6144\n"
"ds_read_b64_tr_b16 v[214:215], v5 offset:7168\n"
"s_waitcnt lgkmcnt(10)\n"
"v_mfma_f32_32x32x16_bf16 v[48:63], v[216:219], v[120:123], v[48:63]\n"
"ds_read_b64_tr_b16 v[216:217], v4 offset:14336\n"
"ds_read_b64_tr_b16 v[218:219], v4 offset:15360\n"
"s_waitcnt lgkmcnt(10)\n"
"v_mfma_f32_32x32x16_bf16 v[64:79], v[220:223], v[120:123], v[64:79]\n"
"ds_read_b64_tr_b16 v[220:221], v5 offset:14336\n"
"ds_read_b64_tr_b16 v[222:223], v5 offset:15360\n"
"s_waitcnt lgkmcnt(10)\n"
"v_mfma_f32_32x32x16_bf16 v[80:95], v[224:227], v[120:123], v[80:95]\n"
"ds_read_b64_tr_b16 v[224:225], v4 offset:22528\n"
"ds_read_b64_tr_b16 v[226:227], v4 offset:23552\n"
"s_waitcnt lgkmcnt(10)\n"
"v_mfma_f32_32x32x16_bf16 v[96:111], v[228:231], v[120:123], v[96:111]\n"
"ds_read_b64_tr_b16 v[228:229], v5 offset:22528\n"
"ds_read_b64_tr_b16 v[230:231], v5 offset:23552\n"
"s_waitcnt lgkmcnt(10)\n"
"v_mfma_f32_32x32x16_bf16 v[16:31], v[208:211], v[124:127], v[16:31]\n"
"s_barrier\n"
"s_waitcnt lgkmcnt(8)\n"
"v_mfma_f32_32x32x16_bf16 v[32:47], v[212:215], v[124:127], v[32:47]\n"
"ds_read_b64_tr_b16 v[208:209], v4 offset:24576\n"
"ds_read_b64_tr_b16 v[210:211], v4 offset:25600\n"
"ds_read_b64_tr_b16 v[212:213], v5 offset:24576\n"
"ds_read_b64_tr_b16 v[214:215], v5 offset:25600\n"
"s_waitcnt lgkmcnt(10)\n"
"v_mfma_f32_32x32x16_bf16 v[48:63], v[216:219], v[124:127], v[48:63]\n"
"ds_read_b64_tr_b16 v[216:217], v4 offset:32768\n"
"ds_read_b64_tr_b16 v[218:219], v4 offset:33792\n"
"s_waitcnt lgkmcnt(10)\n"
"v_mfma_f32_32x32x16_bf16 v[64:79], v[220:223], v[124:127], v[64:79]\n"
"ds_read_b64_tr_b16 v[220:221], v5 offset:32768\n"
"ds_read_b64_tr_b16 v[222:223], v5 offset:33792\n"
"s_waitcnt lgkmcnt(10)\n"
"v_mfma_f32_32x32x16_bf16 v[80:95], v[224:227], v[124:127], v[80:95]\n"
"ds_read_b64_tr_b16 v[224:225], v4 offset:40960\n"
"ds_read_b64_tr_b16 v[226:227], v4 offset:41984\n"
"s_waitcnt lgkmcnt(10)\n"
"v_mfma_f32_32x32x16_bf16 v[96:111], v[228:231], v[124:127], v[96:111]\n"
"ds_read_b64_tr_b16 v[228:229], v5 offset:40960\n"
"ds_read_b64_tr_b16 v[230:231], v5 offset:41984\n"
"s_waitcnt vmcnt(4)\n"
"s_waitcnt lgkmcnt(10)\n"
"v_mfma_f32_32x32x16_bf16 v[16:31], v[208:211], v[128:131], v[16:31]\n"
"ds_read_b64_tr_b16 v[208:209], v4 offset:26624\n"
"ds_read_b64_tr_b16 v[210:211], v4 offset:27648\n"
"s_waitcnt lgkmcnt(10)\n"
"v_mfma_f32_32x32x16_bf16 v[32:47], v[212:215], v[128:131], v[32:47]\n"
"ds_read_b64_tr_b16 v[212:213], v5 offset:26624\n"
"ds_read_b64_tr_b16 v[214:215], v5 offset:27648\n"
"s_waitcnt lgkmcnt(10)\n"
"v_mfma_f32_32x32x16_bf16 v[48:63], v[216:219], v[128:131], v[48:63]\n"
"ds_read_b64_tr_b16 v[216:217], v4 offset:34816\n"
"ds_read_b64_tr_b16 v[218:219], v4 offset:35840\n"
"s_waitcnt lgkmcnt(10)\n"
"v_mfma_f32_32x32x16_bf16 v[64:79], v[220:223], v[128:131], v[64:79]\n"
"ds_read_b64_tr_b16 v[220:221], v5 offset:34816\n"
"ds_read_b64_tr_b16 v[222:223], v5 offset:35840\n"
"s_waitcnt lgkmcnt(10)\n"
"v_mfma_f32_32x32x16_bf16 v[80:95], v[224:227], v[128:131], v[80:95]\n"
"ds_read_b64_tr_b16 v[224:225], v4 offset:43008\n"
"ds_read_b64_tr_b16 v[226:227], v4 offset:44032\n"
"s_waitcnt lgkmcnt(10)\n"
"v_mfma_f32_32x32x16_bf16 v[96:111], v[228:231], v[128:131], v[96:111]\n"
"ds_read_b64_tr_b16 v[228:229], v5 offset:43008\n"
"ds_read_b64_tr_b16 v[230:231], v5 offset:44032\n"
"s_waitcnt lgkmcnt(10)\n"
"v_mfma_f32_32x32x16_bf16 v[16:31], v[208:211], v[132:135], v[16:31]\n"
"ds_read_b64_tr_b16 v[208:209], v4 offset:28672\n"
"ds_read_b64_tr_b16 v[210:211], v4 offset:29696\n"
"s_waitcnt lgkmcnt(10)\n"
"v_mfma_f32_32x32x16_bf16 v[32:47], v[212:215], v[132:135], v[32:47]\n"
"ds_read_b64_tr_b16 v[212:213], v5 offset:28672\n"
"ds_read_b64_tr_b16 v[214:215], v5 offset:29696\n"
"s_waitcnt lgkmcnt(10)\n"
"v_mfma_f32_32x32x16_bf16 v[48:63], v[216:219], v[132:135], v[48:63]\n"
"ds_read_b64_tr_b16 v[216:217], v4 offset:36864\n"
"ds_read_b64_tr_b16 v[218:219], v4 offset:37888\n"
"s_waitcnt lgkmcnt(10)\n"
"v_mfma_f32_32x32x16_bf16 v[64:79], v[220:223], v[132:135], v[64:79]\n"
"ds_read_b64_tr_b16 v[220:221], v5 offset:36864\n"
"ds_read_b64_tr_b16 v[222:223], v5 offset:37888\n"
"s_waitcnt lgkmcnt(10)\n"
"v_mfma_f32_32x32x16_bf16 v[80:95], v[224:227], v[132:135], v[80:95]\n"
"ds_read_b64_tr_b16 v[224:225], v4 offset:45056\n"
"ds_read_b64_tr_b16 v[226:227], v4 offset:46080\n"
"s_waitcnt lgkmcnt(10)\n"
"v_mfma_f32_32x32x16_bf16 v[96:111], v[228:231], v[132:135], v[96:111]\n"
"ds_read_b64_tr_b16 v[228:229], v5 offset:45056\n"
"ds_read_b64_tr_b16 v[230:231], v5 offset:46080\n"
"s_waitcnt lgkmcnt(10)\n"
"v_mfma_f32_32x32x16_bf16 v[16:31], v[208:211], v[136:139], v[16:31]\n"
"ds_read_b64_tr_b16 v[208:209], v4 offset:30720\n"
"ds_read_b64_tr_b16 v[210:211], v4 offset:31744\n"
"s_waitcnt lgkmcnt(10)\n"
"v_mfma_f32_32x32x16_bf16 v[32:47], v[212:215], v[136:139], v[32:47]\n"
"ds_read_b64_tr_b16 v[212:213], v5 offset:30720\n"
"ds_read_b64_tr_b16 v[214:215], v5 offset:31744\n"
"s_waitcnt lgkmcnt(10)\n"
"v_mfma_f32_32x32x16_bf16 v[48:63], v[216:219], v[136:139], v[48:63]\n"
"ds_read_b64_tr_b16 v[216:217], v4 offset:38912\n"
"ds_read_b64_tr_b16 v[218:219], v4 offset:39936\n"
"s_waitcnt lgkmcnt(10)\n"
"v_mfma_f32_32x32x16_bf16 v[64:79], v[220:223], v[136:139], v[64:79]\n"
"ds_read_b64_tr_b16 v[220:221], v5 offset:38912\n"
"ds_read_b64_tr_b16 v[222:223], v5 offset:39936\n"
"s_waitcnt lgkmcnt(10)\n"
"v_mfma_f32_32x32x16_bf16 v[80:95], v[224:227], v[136:139], v[80:95]\n"
"ds_read_b64_tr_b16 v[224:225], v4 offset:47104\n"
"ds_read_b64_tr_b16 v[226:227], v4 offset:48128\n"
"s_waitcnt lgkmcnt(10)\n"
"v_mfma_f32_32x32x16_bf16 v[96:111], v[228:231], v[136:139], v[96:111]\n"
"ds_read_b64_tr_b16 v[228:229], v5 offset:47104\n"
"ds_read_b64_tr_b16 v[230:231], v5 offset:48128\n"
"s_waitcnt lgkmcnt(10)\n"
"v_mfma_f32_32x32x16_bf16 v[16:31], v[208:211], v[140:143], v[16:31]\n"
"s_barrier\n"
"s_waitcnt lgkmcnt(8)\n"
"v_mfma_f32_32x32x16_bf16 v[32:47], v[212:215], v[140:143], v[32:47]\n"
"ds_read_b64_tr_b16 v[208:209], v6 offset:0\n"
"ds_read_b64_tr_b16 v[210:211], v6 offset:1024\n"
"ds_read_b64_tr_b16 v[212:213], v7 offset:0\n"
"ds_read_b64_tr_b16 v[214:215], v7 offset:1024\n"
"s_waitcnt lgkmcnt(10)\n"
"v_mfma_f32_32x32x16_bf16 v[48:63], v[216:219], v[140:143], v[48:63]\n"
"ds_read_b64_tr_b16 v[216:217], v6 offset:8192\n"
"ds_read_b64_tr_b16 v[218:219], v6 offset:9216\n"
"s_waitcnt lgkmcnt(10)\n"
"v_mfma_f32_32x32x16_bf16 v[64:79], v[220:223], v[140:143], v[64:79]\n"
"ds_read_b64_tr_b16 v[220:221], v7 offset:8192\n"
"ds_read_b64_tr_b16 v[222:223], v7 offset:9216\n"
"s_waitcnt lgkmcnt(10)\n"
"v_mfma_f32_32x32x16_bf16 v[80:95], v[224:227], v[140:143], v[80:95]\n"
"ds_read_b64_tr_b16 v[224:225], v6 offset:16384\n"
"ds_read_b64_tr_b16 v[226:227], v6 offset:17408\n"
"s_waitcnt lgkmcnt(10)\n"
"v_mfma_f32_32x32x16_bf16 v[96:111], v[228:231], v[140:143], v[96:111]\n"
"ds_read_b64_tr_b16 v[228:229], v7 offset:16384\n"
"ds_read_b64_tr_b16 v[230:231], v7 offset:17408\n"
"s_waitcnt vmcnt(0)\n"
"s_waitcnt lgkmcnt(10)\n"
"v_mfma_f32_32x32x16_bf16 v[16:31], v[208:211], v[144:147], v[16:31]\n"
"ds_read_b64_tr_b16 v[208:209], v6 offset:2048\n"
"ds_read_b64_tr_b16 v[210:211], v6 offset:3072\n"
"s_waitcnt lgkmcnt(10)\n"
"v_mfma_f32_32x32x16_bf16 v[32:47], v[212:215], v[144:147], v[32:47]\n"
"ds_read_b64_tr_b16 v[212:213], v7 offset:2048\n"
"ds_read_b64_tr_b16 v[214:215], v7 offset:3072\n"
"s_waitcnt lgkmcnt(10)\n"
"v_mfma_f32_32x32x16_bf16 v[48:63], v[216:219], v[144:147], v[48:63]\n"
"ds_read_b64_tr_b16 v[216:217], v6 offset:10240\n"
"ds_read_b64_tr_b16 v[218:219], v6 offset:11264\n"
"s_waitcnt lgkmcnt(10)\n"
"v_mfma_f32_32x32x16_bf16 v[64:79], v[220:223], v[144:147], v[64:79]\n"
"ds_read_b64_tr_b16 v[220:221], v7 offset:10240\n"
"ds_read_b64_tr_b16 v[222:223], v7 offset:11264\n"
"s_waitcnt lgkmcnt(10)\n"
"v_mfma_f32_32x32x16_bf16 v[80:95], v[224:227], v[144:147], v[80:95]\n"
"ds_read_b64_tr_b16 v[224:225], v6 offset:18432\n"
"ds_read_b64_tr_b16 v[226:227], v6 offset:19456\n"
"s_waitcnt lgkmcnt(10)\n"
"v_mfma_f32_32x32x16_bf16 v[96:111], v[228:231], v[144:147], v[96:111]\n"
"ds_read_b64_tr_b16 v[228:229], v7 offset:18432\n"
"ds_read_b64_tr_b16 v[230:231], v7 offset:19456\n"
"s_waitcnt lgkmcnt(10)\n"
"v_mfma_f32_32x32x16_bf16 v[16:31], v[208:211], v[148:151], v[16:31]\n"
"ds_read_b64_tr_b16 v[208:209], v6 offset:4096\n"
"ds_read_b64_tr_b16 v[210:211], v6 offset:5120\n"
"s_waitcnt lgkmcnt(10)\n"
"v_mfma_f32_32x32x16_bf16 v[32:47], v[212:215], v[148:151], v[32:47]\n"
"ds_read_b64_tr_b16 v[212:213], v7 offset:4096\n"
"ds_read_b64_tr_b16 v[214:215], v7 offset:5120\n"
"s_waitcnt lgkmcnt(10)\n"
"v_mfma_f32_32x32x16_bf16 v[48:63], v[216:219], v[148:151], v[48:63]\n"
"ds_read_b64_tr_b16 v[216:217], v6 offset:12288\n"
"ds_read_b64_tr_b16 v[218:219], v6 offset:13312\n"
"s_waitcnt lgkmcnt(10)\n"
"v_mfma_f32_32x32x16_bf16 v[64:79], v[220:223], v[148:151], v[64:79]\n"
"ds_read_b64_tr_b16 v[220:221], v7 offset:12288\n"
"ds_read_b64_tr_b16 v[222:223], v7 offset:13312\n"
"s_waitcnt lgkmcnt(10)\n"
"v_mfma_f32_32x32x16_bf16 v[80:95], v[224:227], v[148:151], v[80:95]\n"
"ds_read_b64_tr_b16 v[224:225], v6 offset:20480\n"
"ds_read_b64_tr_b16 v[226:227], v6 offset:21504\n"
"s_waitcnt lgkmcnt(10)\n"
"v_mfma_f32_32x32x16_bf16 v[96:111], v[228:231], v[148:151], v[96:111]\n"
"ds_read_b64_tr_b16 v[228:229], v7 offset:20480\n"
"ds_read_b64_tr_b16 v[230:231], v7 offset:21504\n"
"s_waitcnt lgkmcnt(10)\n"
"v_mfma_f32_32x32x16_bf16 v[16:31], v[208:211], v[152:155], v[16:31]\n"
"ds_read_b64_tr_b16 v[208:209], v6 offset:6144\n"
"ds_read_b64_tr_b16 v[210:211], v6 offset:7168\n"
"s_waitcnt lgkmcnt(10)\n"
"v_mfma_f32_32x32x16_bf16 v[32:47], v[212:215], v[152:155], v[32:47]\n"
"ds_read_b64_tr_b16 v[212:213], v7 offset:6144\n"
"ds_read_b64_tr_b16 v[214:215], v7 offset:7168\n"
"s_waitcnt lgkmcnt(10)\n"
"v_mfma_f32_32x32x16_bf16 v[48:63], v[216:219], v[152:155], v[48:63]\n"
"ds_read_b64_tr_b16 v[216:217], v6 offset:14336\n"
"ds_read_b64_tr_b16 v[218:219], v6 offset:15360\n"
"s_waitcnt lgkmcnt(10)\n"
"v_mfma_f32_32x32x16_bf16 v[64:79], v[220:223], v[152:155], v[64:79]\n"
"ds_read_b64_tr_b16 v[220:221], v7 offset:14336\n"
"ds_read_b64_tr_b16 v[222:223], v7 offset:15360\n"
"s_waitcnt lgkmcnt(10)\n"
"v_mfma_f32_32x32x16_bf16 v[80:95], v[224:227], v[152:155], v[80:95]\n"
"ds_read_b64_tr_b16 v[224:225], v6 offset:22528\n"
"ds_read_b64_tr_b16 v[226:227], v6 offset:23552\n"
"s_waitcnt lgkmcnt(10)\n"
"v_mfma_f32_32x32x16_bf16 v[96:111], v[228:231], v[152:155], v[96:111]\n"
"ds_read_b64_tr_b16 v[228:229], v7 offset:22528\n"
"ds_read_b64_tr_b16 v[230:231], v7 offset:23552\n"
"s_waitcnt lgkmcnt(10)\n"
"v_mfma_f32_32x32x16_bf16 v[16:31], v[208:211], v[156:159], v[16:31]\n"
"s_waitcnt lgkmcnt(8)\n"
"v_mfma_f32_32x32x16_bf16 v[32:47], v[212:215], v[156:159], v[32:47]\n"
"s_waitcnt lgkmcnt(6)\n"
"v_mfma_f32_32x32x16_bf16 v[48:63], v[216:219], v[156:159], v[48:63]\n"
"s_waitcnt lgkmcnt(4)\n"
"v_mfma_f32_32x32x16_bf16 v[64:79], v[220:223], v[156:159], v[64:79]\n"
"s_waitcnt lgkmcnt(2)\n"
"v_mfma_f32_32x32x16_bf16 v[80:95], v[224:227], v[156:159], v[80:95]\n"
"s_waitcnt lgkmcnt(0)\n"
"v_mfma_f32_32x32x16_bf16 v[96:111], v[228:231], v[156:159], v[96:111]\n"
"s_lshl_b32 s71, %[ks], 6\n"
"s_add_u32 s71, s71, s69\n"
"s_mul_i32 s71, s71, 18\n"
"s_mul_i32 s72, %[dg], 6\n"
"s_add_u32 s71, s71, s72\n"
"s_lshl_b32 s71, s71, 11\n"
"s_add_u32 s63, s71, 28311552\n"
"s_mul_i32 s71, %[tb], 3\n"
"s_add_u32 s71, s71, %[dg]\n"
"s_add_u32 s71, s71, 16\n"
"s_lshl_b32 s71, s71, 7\n"
"s_add_u32 s93, s71, 40108032\n"
"v_cvt_pk_bf16_f32 v112, v16, v17\n"
"v_cvt_pk_bf16_f32 v113, v18, v19\n"
"v_cvt_pk_bf16_f32 v114, v20, v21\n"
"v_cvt_pk_bf16_f32 v115, v22, v23\n"
"v_cvt_pk_bf16_f32 v116, v24, v25\n"
"v_cvt_pk_bf16_f32 v117, v26, v27\n"
"v_cvt_pk_bf16_f32 v118, v28, v29\n"
"v_cvt_pk_bf16_f32 v119, v30, v31\n"
"buffer_store_dwordx4 v[112:115], v2, s[44:47], s63 offen offset:0 sc1\n"
"buffer_store_dwordx4 v[116:119], v2, s[44:47], s63 offen offset:1024 sc1\n"
"v_cvt_pk_bf16_f32 v120, v32, v33\n"
"v_cvt_pk_bf16_f32 v121, v34, v35\n"
"v_cvt_pk_bf16_f32 v122, v36, v37\n"
"v_cvt_pk_bf16_f32 v123, v38, v39\n"
"v_cvt_pk_bf16_f32 v124, v40, v41\n"
"v_cvt_pk_bf16_f32 v125, v42, v43\n"
"v_cvt_pk_bf16_f32 v126, v44, v45\n"
"v_cvt_pk_bf16_f32 v127, v46, v47\n"
"buffer_store_dwordx4 v[120:123], v2, s[44:47], s63 offen offset:2048 sc1\n"
"buffer_store_dwordx4 v[124:127], v2, s[44:47], s63 offen offset:3072 sc1\n"
"v_cvt_pk_bf16_f32 v128, v48, v49\n"
"v_cvt_pk_bf16_f32 v129, v50, v51\n"
"v_cvt_pk_bf16_f32 v130, v52, v53\n"
"v_cvt_pk_bf16_f32 v131, v54, v55\n"
"v_cvt_pk_bf16_f32 v132, v56, v57\n"
"v_cvt_pk_bf16_f32 v133, v58, v59\n"
"v_cvt_pk_bf16_f32 v134, v60, v61\n"
"v_cvt_pk_bf16_f32 v135, v62, v63\n"
"s_add_u32 s63, s63, 4096\n"
"buffer_store_dwordx4 v[128:131], v2, s[44:47], s63 offen offset:0 sc1\n"
"buffer_store_dwordx4 v[132:135], v2, s[44:47], s63 offen offset:1024 sc1\n"
"v_cvt_pk_bf16_f32 v136, v64, v65\n"
"v_cvt_pk_bf16_f32 v137, v66, v67\n"
"v_cvt_pk_bf16_f32 v138, v68, v69\n"
"v_cvt_pk_bf16_f32 v139, v70, v71\n"
"v_cvt_pk_bf16_f32 v140, v72, v73\n"
"v_cvt_pk_bf16_f32 v141, v74, v75\n"
"v_cvt_pk_bf16_f32 v142, v76, v77\n"
"v_cvt_pk_bf16_f32 v143, v78, v79\n"
"buffer_store_dwordx4 v[136:139], v2, s[44:47], s63 offen offset:2048 sc1\n"
"buffer_store_dwordx4 v[140:143], v2, s[44:47], s63 offen offset:3072 sc1\n"
"v_cvt_pk_bf16_f32 v144, v80, v81\n"
"v_cvt_pk_bf16_f32 v145, v82, v83\n"
"v_cvt_pk_bf16_f32 v146, v84, v85\n"
"v_cvt_pk_bf16_f32 v147, v86, v87\n"
"v_cvt_pk_bf16_f32 v148, v88, v89\n"
"v_cvt_pk_bf16_f32 v149, v90, v91\n"
"v_cvt_pk_bf16_f32 v150, v92, v93\n"
"v_cvt_pk_bf16_f32 v151, v94, v95\n"
"s_add_u32 s63, s63, 4096\n"
"buffer_store_dwordx4 v[144:147], v2, s[44:47], s63 offen offset:0 sc1\n"
"buffer_store_dwordx4 v[148:151], v2, s[44:47], s63 offen offset:1024 sc1\n"
"v_cvt_pk_bf16_f32 v152, v96, v97\n"
"v_cvt_pk_bf16_f32 v153, v98, v99\n"
"v_cvt_pk_bf16_f32 v154, v100, v101\n"
"v_cvt_pk_bf16_f32 v155, v102, v103\n"
"v_cvt_pk_bf16_f32 v156, v104, v105\n"
"v_cvt_pk_bf16_f32 v157, v106, v107\n"
"v_cvt_pk_bf16_f32 v158, v108, v109\n"
"v_cvt_pk_bf16_f32 v159, v110, v111\n"
"buffer_store_dwordx4 v[152:155], v2, s[44:47], s63 offen offset:2048 sc1\n"
"buffer_store_dwordx4 v[156:159], v2, s[44:47], s63 offen offset:3072 sc1\n"
"s_mov_b32 s53, 1\n"
"s_branch .Lkc_post_13_%=\n"
".Lkc_gidle_15_%=:\n"
"s_waitcnt vmcnt(0)\n"
"v_mov_b32 v13, 1\n"
"s_mov_b64 exec, 1\n"
"buffer_atomic_add v13, v3, s[44:47], s92 offen sc1\n"
"s_mov_b64 exec, -1\n"
"s_barrier\n"
"s_barrier\n"
"s_barrier\n"
"s_barrier\n"
"s_barrier\n"
"s_barrier\n"
"s_barrier\n"
"s_barrier\n"
"s_barrier\n"
"s_barrier\n"
"s_barrier\n"
"s_barrier\n"
"s_barrier\n"
"s_barrier\n"
"s_mov_b32 s53, 0\n"
"s_branch .Lkc_post_13_%=\n"
".Lkc_lrole_12_%=:\n"
"buffer_store_dwordx4 v[232:235], v252, s[44:47], s71 offen sc1\n"
"buffer_store_dwordx4 v[236:239], v2, s[44:47], s72 offen sc1\n"
"s_lshl_b32 s94, %[ftb], 2\n"
"s_add_u32 s94, s94, s56\n"
"s_mul_i32 s91, s94, 36864\n"
"s_add_u32 s91, s91, 25952256\n"
"s_cmp_lt_u32 0, %[fin]\n"
"s_cselect_b32 s91, s91, 1073741824\n"
"s_lshl_b32 s77, %[ftb], 7\n"
"s_add_u32 s77, s77, 40108032\n"
"s_lshl_b32 s52, %[fin], 7\n"
"s_waitcnt vmcnt(14)\n"
"s_barrier\n"
"s_waitcnt vmcnt(11)\n"
"s_barrier\n"
"s_add_u32 m0, s62, 0\n"
"s_add_u32 s75, s60, 442368\n"
"s_nop 0\n"
"buffer_load_dwordx4 v1, s[40:43], s75 offen lds\n"
"s_add_u32 m0, s62, 4096\n"
"s_add_u32 s75, s60, 479232\n"
"s_nop 0\n"
"buffer_load_dwordx4 v1, s[40:43], s75 offen lds\n"
"s_add_u32 m0, s62, 8192\n"
"s_add_u32 s75, s60, 442496\n"
"s_nop 0\n"
"buffer_load_dwordx4 v1, s[40:43], s75 offen lds\n"
"s_add_u32 m0, s62, 12288\n"
"s_add_u32 s75, s60, 479360\n"
"s_nop 0\n"
"buffer_load_dwordx4 v1, s[40:43], s75 offen lds\n"
"s_add_u32 m0, s62, 16384\n"
"s_add_u32 s75, s60, 442624\n"
"s_nop 0\n"
"buffer_load_dwordx4 v1, s[40:43], s75 offen lds\n"
"s_add_u32 m0, s62, 20480\n"
"s_add_u32 s75, s60, 479488\n"
"s_nop 0\n"
"buffer_load_dwordx4 v1, s[40:43], s75 offen lds\n"
"s_waitcnt vmcnt(6)\n"
"v_mov_b32 v13, 1\n"
"s_mov_b64 exec, 1\n"
"buffer_atomic_add v13, v3, s[44:47], s92 offen sc1\n"
"s_mov_b64 exec, -1\n"
"s_waitcnt vmcnt(14)\n"
"s_barrier\n"
"s_add_u32 m0, s62, 24576\n"
"s_add_u32 s75, s60, 516096\n"
"s_nop 0\n"
"buffer_load_dwordx4 v1, s[40:43], s75 offen lds\n"
"s_add_u32 m0, s62, 28672\n"
"s_add_u32 s75, s60, 552960\n"
"s_nop 0\n"
"buffer_load_dwordx4 v1, s[40:43], s75 offen lds\n"
"s_add_u32 m0, s62, 32768\n"
"s_add_u32 s75, s60, 516224\n"
"s_nop 0\n"
"buffer_load_dwordx4 v1, s[40:43], s75 offen lds\n"
"s_add_u32 m0, s62, 36864\n"
"s_add_u32 s75, s60, 553088\n"
"s_nop 0\n"
"buffer_load_dwordx4 v1, s[40:43], s75 offen lds\n"
"s_add_u32 m0, s62, 40960\n"
"s_add_u32 s75, s60, 516352\n"
"s_nop 0\n"
"buffer_load_dwordx4 v1, s[40:43], s75 offen lds\n"
"s_add_u32 m0, s62, 45056\n"
"s_add_u32 s75, s60, 553216\n"
"s_nop 0\n"
"buffer_load_dwordx4 v1, s[40:43], s75 offen lds\n"
"s_waitcnt vmcnt(17)\n"
"s_barrier\n"
"s_add_u32 m0, s62, 49152\n"
"s_add_u32 s75, s60, 589824\n"
"s_nop 0\n"
"buffer_load_dwordx4 v1, s[40:43], s75 offen lds\n"
"s_add_u32 m0, s62, 53248\n"
"s_add_u32 s75, s60, 626688\n"
"s_nop 0\n"
"buffer_load_dwordx4 v1, s[40:43], s75 offen lds\n"
"s_add_u32 m0, s62, 57344\n"
"s_add_u32 s75, s60, 589952\n"
"s_nop 0\n"
"buffer_load_dwordx4 v1, s[40:43], s75 offen lds\n"
"s_add_u32 m0, s62, 61440\n"
"s_add_u32 s75, s60, 626816\n"
"s_nop 0\n"
"buffer_load_dwordx4 v1, s[40:43], s75 offen lds\n"
"s_add_u32 m0, s62, 65536\n"
"s_add_u32 s75, s60, 590080\n"
"s_nop 0\n"
"buffer_load_dwordx4 v1, s[40:43], s75 offen lds\n"
"s_add_u32 m0, s62, 69632\n"
"s_add_u32 s75, s60, 626944\n"
"s_nop 0\n"
"buffer_load_dwordx4 v1, s[40:43], s75 offen lds\n"
"s_waitcnt vmcnt(20)\n"
"s_barrier\n"
"s_add_u32 m0, s62, 73728\n"
"s_add_u32 s75, s60, 663552\n"
"s_nop 0\n"
"buffer_load_dwordx4 v1, s[40:43], s75 offen lds\n"
"s_add_u32 m0, s62, 77824\n"
"s_add_u32 s75, s60, 700416\n"
"s_nop 0\n"
"buffer_load_dwordx4 v1, s[40:43], s75 offen lds\n"
"s_add_u32 m0, s62, 81920\n"
"s_add_u32 s75, s60, 663680\n"
"s_nop 0\n"
"buffer_load_dwordx4 v1, s[40:43], s75 offen lds\n"
"s_add_u32 m0, s62, 86016\n"
"s_add_u32 s75, s60, 700544\n"
"s_nop 0\n"
"buffer_load_dwordx4 v1, s[40:43], s75 offen lds\n"
"s_add_u32 m0, s62, 90112\n"
"s_add_u32 s75, s60, 663808\n"
"s_nop 0\n"
"buffer_load_dwordx4 v1, s[40:43], s75 offen lds\n"
"s_add_u32 m0, s62, 94208\n"
"s_add_u32 s75, s60, 700672\n"
"s_nop 0\n"
"buffer_load_dwordx4 v1, s[40:43], s75 offen lds\n"
"s_waitcnt vmcnt(18)\n"
"s_barrier\n"
"s_add_u32 m0, s62, 98304\n"
"s_add_u32 s75, s60, 737280\n"
"s_nop 0\n"
"buffer_load_dwordx4 v1, s[40:43], s75 offen lds\n"
"s_add_u32 m0, s62, 102400\n"
"s_add_u32 s75, s60, 774144\n"
"s_nop 0\n"
"buffer_load_dwordx4 v1, s[40:43], s75 offen lds\n"
"s_add_u32 m0, s62, 106496\n"
"s_add_u32 s75, s60, 737408\n"
"s_nop 0\n"
"buffer_load_dwordx4 v1, s[40:43], s75 offen lds\n"
"s_add_u32 m0, s62, 110592\n"
"s_add_u32 s75, s60, 774272\n"
"s_nop 0\n"
"buffer_load_dwordx4 v1, s[40:43], s75 offen lds\n"
"s_add_u32 m0, s62, 114688\n"
"s_add_u32 s75, s60, 737536\n"
"s_nop 0\n"
"buffer_load_dwordx4 v1, s[40:43], s75 offen lds\n"
"s_add_u32 m0, s62, 118784\n"
"s_add_u32 s75, s60, 774400\n"
"s_nop 0\n"
"buffer_load_dwordx4 v1, s[40:43], s75 offen lds\n"
"s_waitcnt vmcnt(18)\n"
"s_barrier\n"
"s_add_u32 m0, s62, 122880\n"
"s_add_u32 s75, s60, 811008\n"
"s_nop 0\n"
"buffer_load_dwordx4 v1, s[40:43], s75 offen lds\n"
"s_add_u32 m0, s62, 126976\n"
"s_add_u32 s75, s60, 847872\n"
"s_nop 0\n"
"buffer_load_dwordx4 v1, s[40:43], s75 offen lds\n"
"s_add_u32 m0, s62, 131072\n"
"s_add_u32 s75, s60, 811136\n"
"s_nop 0\n"
"buffer_load_dwordx4 v1, s[40:43], s75 offen lds\n"
"s_add_u32 m0, s62, 135168\n"
"s_add_u32 s75, s60, 848000\n"
"s_nop 0\n"
"buffer_load_dwordx4 v1, s[40:43], s75 offen lds\n"
"s_add_u32 m0, s62, 139264\n"
"s_add_u32 s75, s60, 811264\n"
"s_nop 0\n"
"buffer_load_dwordx4 v1, s[40:43], s75 offen lds\n"
"s_add_u32 m0, s62, 143360\n"
"s_add_u32 s75, s60, 848128\n"
"s_nop 0\n"
"buffer_load_dwordx4 v1, s[40:43], s75 offen lds\n"
"s_waitcnt vmcnt(18)\n"
"s_barrier\n"
"s_add_u32 m0, s62, 0\n"
"s_add_u32 s75, s60, 884736\n"
"s_nop 0\n"
"buffer_load_dwordx4 v1, s[40:43], s75 offen lds\n"
"s_add_u32 m0, s62, 4096\n"
"s_add_u32 s75, s60, 921600\n"
"s_nop 0\n"
"buffer_load_dwordx4 v1, s[40:43], s75 offen lds\n"
"s_add_u32 m0, s62, 8192\n"
"s_add_u32 s75, s60, 884864\n"
"s_nop 0\n"
"buffer_load_dwordx4 v1, s[40:43], s75 offen lds\n"
"s_add_u32 m0, s62, 12288\n"
"s_add_u32 s75, s60, 921728\n"
"s_nop 0\n"
"buffer_load_dwordx4 v1, s[40:43], s75 offen lds\n"
"s_add_u32 m0, s62, 16384\n"
"s_add_u32 s75, s60, 884992\n"
"s_nop 0\n"
"buffer_load_dwordx4 v1, s[40:43], s75 offen lds\n"
"s_add_u32 m0, s62, 20480\n"
"s_add_u32 s75, s60, 921856\n"
"s_nop 0\n"
"buffer_load_dwordx4 v1, s[40:43], s75 offen lds\n"
"buffer_load_dword v12, v3, s[44:47], s77 offen sc1\n"
"s_waitcnt vmcnt(19)\n"
"s_barrier\n"
"s_add_u32 m0, s62, 24576\n"
"s_add_u32 s75, s60, 958464\n"
"s_nop 0\n"
"buffer_load_dwordx4 v1, s[40:43], s75 offen lds\n"
"s_add_u32 m0, s62, 28672\n"
"s_add_u32 s75, s60, 995328\n"
"s_nop 0\n"
"buffer_load_dwordx4 v1, s[40:43], s75 offen lds\n"
"s_add_u32 m0, s62, 32768\n"
"s_add_u32 s75, s60, 958592\n"
"s_nop 0\n"
"buffer_load_dwordx4 v1, s[40:43], s75 offen lds\n"
"s_add_u32 m0, s62, 36864\n"
"s_add_u32 s75, s60, 995456\n"
"s_nop 0\n"
"buffer_load_dwordx4 v1, s[40:43], s75 offen lds\n"
"s_add_u32 m0, s62, 40960\n"
"s_add_u32 s75, s60, 958720\n"
"s_nop 0\n"
"buffer_load_dwordx4 v1, s[40:43], s75 offen lds\n"
"s_add_u32 m0, s62, 45056\n"
"s_add_u32 s75, s60, 995584\n"
"s_nop 0\n"
"buffer_load_dwordx4 v1, s[40:43], s75 offen lds\n"
"s_waitcnt vmcnt(19)\n"
"s_barrier\n"
"s_add_u32 m0, s62, 49152\n"
"s_add_u32 s75, s60, 1032192\n"
"s_nop 0\n"
"buffer_load_dwordx4 v1, s[40:43], s75 offen lds\n"
"s_add_u32 m0, s62, 53248\n"
"s_add_u32 s75, s60, 1069056\n"
"s_nop 0\n"
"buffer_load_dwordx4 v1, s[40:43], s75 offen lds\n"
"s_add_u32 m0, s62, 57344\n"
"s_add_u32 s75, s60, 1032320\n"
"s_nop 0\n"
"buffer_load_dwordx4 v1, s[40:43], s75 offen lds\n"
"s_add_u32 m0, s62, 61440\n"
"s_add_u32 s75, s60, 1069184\n"
"s_nop 0\n"
"buffer_load_dwordx4 v1, s[40:43], s75 offen lds\n"
"s_add_u32 m0, s62, 65536\n"
"s_add_u32 s75, s60, 1032448\n"
"s_nop 0\n"
"buffer_load_dwordx4 v1, s[40:43], s75 offen lds\n"
"s_add_u32 m0, s62, 69632\n"
"s_add_u32 s75, s60, 1069312\n"
"s_nop 0\n"
"buffer_load_dwordx4 v1, s[40:43], s75 offen lds\n"
"s_waitcnt vmcnt(12)\n"
"s_mov_b32 s78, 0\n"
".Lkc_so_18_%=:\n"
"v_readfirstlane_b32 s79, v12\n"
"s_add_u32 s78, s78, 1\n"
"s_cmp_ge_u32 s79, s52\n"
"s_cbranch_scc1 .Lkc_sod_19_%=\n"
"s_cmp_lt_u32 s78, 0x4000\n"
"s_cbranch_scc0 .Lkc_sod_19_%=\n"
"s_sleep 2\n"
"buffer_load_dword v12, v3, s[44:47], s77 offen sc1\n"
"s_waitcnt vmcnt(0)\n"
"s_branch .Lkc_so_18_%=\n"
".Lkc_sod_19_%=:\n"
"s_add_u32 s76, s91, 0\n"
"buffer_load_dwordx4 v[16:19], v2, s[44:47], s76 offen offset:0 sc1\n"
"buffer_load_dwordx4 v[20:23], v2, s[44:47], s76 offen offset:1024 sc1\n"
"buffer_load_dwordx4 v[24:27], v2, s[44:47], s76 offen offset:2048 sc1\n"
"buffer_load_dwordx4 v[28:31], v2, s[44:47], s76 offen offset:3072 sc1\n"
"s_add_u32 s76, s91, 4096\n"
"buffer_load_dwordx4 v[32:35], v2, s[44:47], s76 offen offset:0 sc1\n"
"buffer_load_dwordx4 v[36:39], v2, s[44:47], s76 offen offset:1024 sc1\n"
"buffer_load_dwordx4 v[40:43], v2, s[44:47], s76 offen offset:2048 sc1\n"
"buffer_load_dwordx4 v[44:47], v2, s[44:47], s76 offen offset:3072 sc1\n"
"s_add_u32 s76, s91, 8192\n"
"buffer_load_dwordx4 v[48:51], v2, s[44:47], s76 offen offset:0 sc1\n"
"buffer_load_dwordx4 v[52:55], v2, s[44:47], s76 offen offset:1024 sc1\n"
"buffer_load_dwordx4 v[56:59], v2, s[44:47], s76 offen offset:2048 sc1\n"
"buffer_load_dwordx4 v[60:63], v2, s[44:47], s76 offen offset:3072 sc1\n"
"s_add_u32 s76, s91, 12288\n"
"buffer_load_dwordx4 v[64:67], v2, s[44:47], s76 offen offset:0 sc1\n"
"buffer_load_dwordx4 v[68:71], v2, s[44:47], s76 offen offset:1024 sc1\n"
"buffer_load_dwordx4 v[72:75], v2, s[44:47], s76 offen offset:2048 sc1\n"
"buffer_load_dwordx4 v[76:79], v2, s[44:47], s76 offen offset:3072 sc1\n"
"s_add_u32 s76, s91, 16384\n"
"buffer_load_dwordx4 v[80:83], v2, s[44:47], s76 offen offset:0 sc1\n"
"buffer_load_dwordx4 v[84:87], v2, s[44:47], s76 offen offset:1024 sc1\n"
"buffer_load_dwordx4 v[88:91], v2, s[44:47], s76 offen offset:2048 sc1\n"
"buffer_load_dwordx4 v[92:95], v2, s[44:47], s76 offen offset:3072 sc1\n"
"s_add_u32 s76, s91, 20480\n"
"buffer_load_dwordx4 v[96:99], v2, s[44:47], s76 offen offset:0 sc1\n"
"buffer_load_dwordx4 v[100:103], v2, s[44:47], s76 offen offset:1024 sc1\n"
"buffer_load_dwordx4 v[104:107], v2, s[44:47], s76 offen offset:2048 sc1\n"
"buffer_load_dwordx4 v[108:111], v2, s[44:47], s76 offen offset:3072 sc1\n"
"s_add_u32 s76, s91, 24576\n"
"buffer_load_dwordx4 v[112:115], v2, s[44:47], s76 offen offset:0 sc1\n"
"buffer_load_dwordx4 v[116:119], v2, s[44:47], s76 offen offset:1024 sc1\n"
"buffer_load_dwordx4 v[120:123], v2, s[44:47], s76 offen offset:2048 sc1\n"
"buffer_load_dwordx4 v[124:127], v2, s[44:47], s76 offen offset:3072 sc1\n"
"s_add_u32 s76, s91, 28672\n"
"buffer_load_dwordx4 v[128:131], v2, s[44:47], s76 offen offset:0 sc1\n"
"buffer_load_dwordx4 v[132:135], v2, s[44:47], s76 offen offset:1024 sc1\n"
"buffer_load_dwordx4 v[136:139], v2, s[44:47], s76 offen offset:2048 sc1\n"
"buffer_load_dwordx4 v[140:143], v2, s[44:47], s76 offen offset:3072 sc1\n"
"s_add_u32 s76, s91, 32768\n"
"buffer_load_dwordx4 v[144:147], v2, s[44:47], s76 offen offset:0 sc1\n"
"buffer_load_dwordx4 v[148:151], v2, s[44:47], s76 offen offset:1024 sc1\n"
"buffer_load_dwordx4 v[152:155], v2, s[44:47], s76 offen offset:2048 sc1\n"
"buffer_load_dwordx4 v[156:159], v2, s[44:47], s76 offen offset:3072 sc1\n"
"s_waitcnt vmcnt(55)\n"
"s_barrier\n"
"s_add_u32 m0, s62, 73728\n"
"s_add_u32 s75, s90, 0\n"
"s_nop 0\n"
"buffer_load_dwordx4 v1, s[48:51], s75 offen lds\n"
"s_add_u32 m0, s62, 77824\n"
"s_add_u32 s75, s90, 36864\n"
"s_nop 0\n"
"buffer_load_dwordx4 v1, s[48:51], s75 offen lds\n"
"s_add_u32 m0, s62, 81920\n"
"s_add_u32 s75, s90, 73728\n"
"s_nop 0\n"
"buffer_load_dwordx4 v1, s[48:51], s75 offen lds\n"
"s_add_u32 m0, s62, 86016\n"
"s_add_u32 s75, s90, 110592\n"
"s_nop 0\n"
"buffer_load_dwordx4 v1, s[48:51], s75 offen lds\n"
"s_add_u32 m0, s62, 90112\n"
"s_add_u32 s75, s90, 147456\n"
"s_nop 0\n"
"buffer_load_dwordx4 v1, s[48:51], s75 offen lds\n"
"s_add_u32 m0, s62, 94208\n"
"s_add_u32 s75, s90, 184320\n"
"s_nop 0\n"
"buffer_load_dwordx4 v1, s[48:51], s75 offen lds\n"
"s_waitcnt vmcnt(55)\n"
"s_barrier\n"
"s_add_u32 m0, s62, 98304\n"
"s_add_u32 s75, s90, 221184\n"
"s_nop 0\n"
"buffer_load_dwordx4 v1, s[48:51], s75 offen lds\n"
"s_add_u32 m0, s62, 102400\n"
"s_add_u32 s75, s90, 258048\n"
"s_nop 0\n"
"buffer_load_dwordx4 v1, s[48:51], s75 offen lds\n"
"s_add_u32 m0, s62, 106496\n"
"s_add_u32 s75, s90, 294912\n"
"s_nop 0\n"
"buffer_load_dwordx4 v1, s[48:51], s75 offen lds\n"
"s_add_u32 m0, s62, 110592\n"
"s_add_u32 s75, s90, 331776\n"
"s_nop 0\n"
"buffer_load_dwordx4 v1, s[48:51], s75 offen lds\n"
"s_add_u32 m0, s62, 114688\n"
"s_add_u32 s75, s90, 368640\n"
"s_nop 0\n"
"buffer_load_dwordx4 v1, s[48:51], s75 offen lds\n"
"s_add_u32 m0, s62, 118784\n"
"s_add_u32 s75, s90, 405504\n"
"s_nop 0\n"
"buffer_load_dwordx4 v1, s[48:51], s75 offen lds\n"
"s_waitcnt vmcnt(54)\n"
"s_barrier\n"
"s_add_u32 m0, s62, 122880\n"
"s_add_u32 s75, s90, 442368\n"
"s_nop 0\n"
"buffer_load_dwordx4 v1, s[48:51], s75 offen lds\n"
"s_add_u32 m0, s62, 126976\n"
"s_add_u32 s75, s90, 479232\n"
"s_nop 0\n"
"buffer_load_dwordx4 v1, s[48:51], s75 offen lds\n"
"s_add_u32 m0, s62, 131072\n"
"s_add_u32 s75, s90, 516096\n"
"s_nop 0\n"
"buffer_load_dwordx4 v1, s[48:51], s75 offen lds\n"
"s_add_u32 m0, s62, 135168\n"
"s_add_u32 s75, s90, 552960\n"
"s_nop 0\n"
"buffer_load_dwordx4 v1, s[48:51], s75 offen lds\n"
"s_add_u32 m0, s62, 139264\n"
"s_add_u32 s75, s90, 589824\n"
"s_nop 0\n"
"buffer_load_dwordx4 v1, s[48:51], s75 offen lds\n"
"s_add_u32 m0, s62, 143360\n"
"s_add_u32 s75, s90, 626688\n"
"s_nop 0\n"
"buffer_load_dwordx4 v1, s[48:51], s75 offen lds\n"
"s_waitcnt vmcnt(54)\n"
"s_barrier\n"
"s_mov_b32 s53, 0\n"
".Lkc_post_13_%=:\n"
"s_cmp_lt_u32 0, %[fin]\n"
"s_cbranch_scc1 .Lkc_final_20_%=\n"
"s_waitcnt vmcnt(0)\n"
"s_cmp_eq_u32 s53, 0\n"
"s_cbranch_scc1 .Lkc_done_14_%=\n"
"v_mov_b32 v13, 1\n"
"s_mov_b64 exec, 1\n"
"buffer_atomic_add v13, v3, s[44:47], s93 offen sc1\n"
"s_mov_b64 exec, -1\n"
"s_branch .Lkc_done_14_%=\n"
".Lkc_final_20_%=:\n"
"s_lshl_b32 s94, %[ftb], 2\n"
"s_add_u32 s94, s94, s56\n"
"s_mul_i32 s91, s94, 36864\n"
"s_add_u32 s91, s91, 25952256\n"
"s_cmp_eq_u32 s53, 0\n"
"s_cbranch_scc1 .Lkc_haveo_22_%=\n"
"s_add_u32 s76, s91, 0\n"
"buffer_load_dwordx4 v[16:19], v2, s[44:47], s76 offen offset:0 sc1\n"
"buffer_load_dwordx4 v[20:23], v2, s[44:47], s76 offen offset:1024 sc1\n"
"buffer_load_dwordx4 v[24:27], v2, s[44:47], s76 offen offset:2048 sc1\n"
"buffer_load_dwordx4 v[28:31], v2, s[44:47], s76 offen offset:3072 sc1\n"
"s_add_u32 s76, s91, 4096\n"
"buffer_load_dwordx4 v[32:35], v2, s[44:47], s76 offen offset:0 sc1\n"
"buffer_load_dwordx4 v[36:39], v2, s[44:47], s76 offen offset:1024 sc1\n"
"buffer_load_dwordx4 v[40:43], v2, s[44:47], s76 offen offset:2048 sc1\n"
"buffer_load_dwordx4 v[44:47], v2, s[44:47], s76 offen offset:3072 sc1\n"
"s_add_u32 s76, s91, 8192\n"
"buffer_load_dwordx4 v[48:51], v2, s[44:47], s76 offen offset:0 sc1\n"
"buffer_load_dwordx4 v[52:55], v2, s[44:47], s76 offen offset:1024 sc1\n"
"buffer_load_dwordx4 v[56:59], v2, s[44:47], s76 offen offset:2048 sc1\n"
"buffer_load_dwordx4 v[60:63], v2, s[44:47], s76 offen offset:3072 sc1\n"
"s_add_u32 s76, s91, 12288\n"
"buffer_load_dwordx4 v[64:67], v2, s[44:47], s76 offen offset:0 sc1\n"
"buffer_load_dwordx4 v[68:71], v2, s[44:47], s76 offen offset:1024 sc1\n"
"buffer_load_dwordx4 v[72:75], v2, s[44:47], s76 offen offset:2048 sc1\n"
"buffer_load_dwordx4 v[76:79], v2, s[44:47], s76 offen offset:3072 sc1\n"
"s_add_u32 s76, s91, 16384\n"
"buffer_load_dwordx4 v[80:83], v2, s[44:47], s76 offen offset:0 sc1\n"
"buffer_load_dwordx4 v[84:87], v2, s[44:47], s76 offen offset:1024 sc1\n"
"buffer_load_dwordx4 v[88:91], v2, s[44:47], s76 offen offset:2048 sc1\n"
"buffer_load_dwordx4 v[92:95], v2, s[44:47], s76 offen offset:3072 sc1\n"
"s_add_u32 s76, s91, 20480\n"
"buffer_load_dwordx4 v[96:99], v2, s[44:47], s76 offen offset:0 sc1\n"
"buffer_load_dwordx4 v[100:103], v2, s[44:47], s76 offen offset:1024 sc1\n"
"buffer_load_dwordx4 v[104:107], v2, s[44:47], s76 offen offset:2048 sc1\n"
"buffer_load_dwordx4 v[108:111], v2, s[44:47], s76 offen offset:3072 sc1\n"
"s_waitcnt vmcnt(24)\n"
"v_mov_b32 v13, 1\n"
"s_mov_b64 exec, 1\n"
"buffer_atomic_add v13, v3, s[44:47], s93 offen sc1\n"
"s_mov_b64 exec, -1\n"
"s_add_u32 s76, s91, 24576\n"
"buffer_load_dwordx4 v[112:115], v2, s[44:47], s76 offen offset:0 sc1\n"
"buffer_load_dwordx4 v[116:119], v2, s[44:47], s76 offen offset:1024 sc1\n"
"buffer_load_dwordx4 v[120:123], v2, s[44:47], s76 offen offset:2048 sc1\n"
"buffer_load_dwordx4 v[124:127], v2, s[44:47], s76 offen offset:3072 sc1\n"
"s_add_u32 s76, s91, 28672\n"
"buffer_load_dwordx4 v[128:131], v2, s[44:47], s76 offen offset:0 sc1\n"
"buffer_load_dwordx4 v[132:135], v2, s[44:47], s76 offen offset:1024 sc1\n"
"buffer_load_dwordx4 v[136:139], v2, s[44:47], s76 offen offset:2048 sc1\n"
"buffer_load_dwordx4 v[140:143], v2, s[44:47], s76 offen offset:3072 sc1\n"
"s_add_u32 s76, s91, 32768\n"
"buffer_load_dwordx4 v[144:147], v2, s[44:47], s76 offen offset:0 sc1\n"
"buffer_load_dwordx4 v[148:151], v2, s[44:47], s76 offen offset:1024 sc1\n"
"buffer_load_dwordx4 v[152:155], v2, s[44:47], s76 offen offset:2048 sc1\n"
"buffer_load_dwordx4 v[156:159], v2, s[44:47], s76 offen offset:3072 sc1\n"
".Lkc_haveo_22_%=:\n"
"s_mov_b32 s40, %[x_lo]\n"
"s_and_b32 s41, %[x_hi], 0xffff\n"
"s_mov_b32 s42, 2359296\n"
"s_mov_b32 s43, 0x20000\n"
"v_and_b32 v10, 31, v0\n"
"v_mul_u32_u24 v10, 1152, v10\n"
"v_lshrrev_b32 v11, 5, v0\n"
"v_lshl_add_u32 v10, v11, 3, v10\n"
"s_mul_i32 s73, s94, 36864\n"
"s_lshl_b32 s74, %[cb], 7\n"
"s_add_u32 s73, s73, s74\n"
"s_lshl_b32 s74, s57, 6\n"
"s_add_u32 s73, s73, s74\n"
"buffer_load_dwordx2 v[160:161], v10, s[40:43], s73 offen offset:0\n"
"buffer_load_dwordx2 v[162:163], v10, s[40:43], s73 offen offset:16\n"
"buffer_load_dwordx2 v[164:165], v10, s[40:43], s73 offen offset:32\n"
"buffer_load_dwordx2 v[166:167], v10, s[40:43], s73 offen offset:48\n"
"v_lshrrev_b32 v13, 5, v0\n"
"v_lshlrev_b32 v4, 10, v13\n"
"v_bfe_u32 v13, v0, 2, 2\n"
"v_lshl_add_u32 v4, v13, 7, v4\n"
"v_lshrrev_b32 v13, 1, v13\n"
"v_xor_b32 v13, s57, v13\n"
"v_lshl_add_u32 v4, v13, 6, v4\n"
"v_bfe_u32 v13, v0, 4, 1\n"
"v_lshl_add_u32 v4, v13, 5, v4\n"
"v_and_b32 v13, 3, v0\n"
"v_lshl_add_u32 v4, v13, 3, v4\n"
"v_add_u32 v4, 73728, v4\n"
"v_add_u32 v5, 0x8000, v4\n"
"s_mul_i32 s71, %[ftb], 3\n"
"s_add_u32 s71, s71, %[fdg]\n"
"s_add_u32 s71, s71, 16\n"
"s_lshl_b32 s71, s71, 7\n"
"s_add_u32 s71, s71, 40108032\n"
"s_cmp_eq_u32 s57, 0\n"
"s_cbranch_scc1 .Lkc_lw_23_%=\n"
"s_waitcnt vmcnt(4)\n"
".Lkc_lw_23_%=:\n"
"s_barrier\n"
"ds_read_b64_tr_b16 v[184:185], v4 offset:0\n"
"ds_read_b64_tr_b16 v[186:187], v4 offset:512\n"
"ds_read_b64_tr_b16 v[188:189], v4 offset:2048\n"
"ds_read_b64_tr_b16 v[190:191], v4 offset:2560\n"
"ds_read_b64_tr_b16 v[192:193], v4 offset:4096\n"
"ds_read_b64_tr_b16 v[194:195], v4 offset:4608\n"
"ds_read_b64_tr_b16 v[196:197], v4 offset:6144\n"
"ds_read_b64_tr_b16 v[198:199], v4 offset:6656\n"
"ds_read_b64_tr_b16 v[200:201], v4 offset:8192\n"
"ds_read_b64_tr_b16 v[202:203], v4 offset:8704\n"
"ds_read_b64_tr_b16 v[204:205], v4 offset:10240\n"
"ds_read_b64_tr_b16 v[206:207], v4 offset:10752\n"
"s_waitcnt vmcnt(37)\n"
"s_waitcnt lgkmcnt(10)\n"
"v_mfma_f32_32x32x16_bf16 v[168:183], v[184:187], v[16:19], 0\n"
"ds_read_b64_tr_b16 v[184:185], v4 offset:12288\n"
"ds_read_b64_tr_b16 v[186:187], v4 offset:12800\n"
"s_waitcnt lgkmcnt(10)\n"
"v_mfma_f32_32x32x16_bf16 v[168:183], v[188:191], v[20:23], v[168:183]\n"
"ds_read_b64_tr_b16 v[188:189], v4 offset:14336\n"
"ds_read_b64_tr_b16 v[190:191], v4 offset:14848\n"
"s_waitcnt lgkmcnt(10)\n"
"v_mfma_f32_32x32x16_bf16 v[168:183], v[192:195], v[24:27], v[168:183]\n"
"ds_read_b64_tr_b16 v[192:193], v4 offset:16384\n"
"ds_read_b64_tr_b16 v[194:195], v4 offset:16896\n"
"s_waitcnt vmcnt(34)\n"
"s_waitcnt lgkmcnt(10)\n"
"v_mfma_f32_32x32x16_bf16 v[168:183], v[196:199], v[28:31], v[168:183]\n"
"ds_read_b64_tr_b16 v[196:197], v4 offset:18432\n"
"ds_read_b64_tr_b16 v[198:199], v4 offset:18944\n"
"s_waitcnt lgkmcnt(10)\n"
"v_mfma_f32_32x32x16_bf16 v[168:183], v[200:203], v[32:35], v[168:183]\n"
"ds_read_b64_tr_b16 v[200:201], v4 offset:20480\n"
"ds_read_b64_tr_b16 v[202:203], v4 offset:20992\n"
"s_waitcnt lgkmcnt(10)\n"
"v_mfma_f32_32x32x16_bf16 v[168:183], v[204:207], v[36:39], v[168:183]\n"
"ds_read_b64_tr_b16 v[204:205], v4 offset:22528\n"
"ds_read_b64_tr_b16 v[206:207], v4 offset:23040\n"
"s_waitcnt vmcnt(31)\n"
"s_waitcnt lgkmcnt(10)\n"
"v_mfma_f32_32x32x16_bf16 v[168:183], v[184:187], v[40:43], v[168:183]\n"
"ds_read_b64_tr_b16 v[184:185], v4 offset:24576\n"
"ds_read_b64_tr_b16 v[186:187], v4 offset:25088\n"
"s_waitcnt lgkmcnt(10)\n"
"v_mfma_f32_32x32x16_bf16 v[168:183], v[188:191], v[44:47], v[168:183]\n"
"ds_read_b64_tr_b16 v[188:189], v4 offset:26624\n"
"ds_read_b64_tr_b16 v[190:191], v4 offset:27136\n"
"s_waitcnt lgkmcnt(10)\n"
"v_mfma_f32_32x32x16_bf16 v[168:183], v[192:195], v[48:51], v[168:183]\n"
"ds_read_b64_tr_b16 v[192:193], v4 offset:28672\n"
"ds_read_b64_tr_b16 v[194:195], v4 offset:29184\n"
"s_waitcnt vmcnt(28)\n"
"s_waitcnt lgkmcnt(10)\n"
"v_mfma_f32_32x32x16_bf16 v[168:183], v[196:199], v[52:55], v[168:183]\n"
"ds_read_b64_tr_b16 v[196:197], v4 offset:30720\n"
"ds_read_b64_tr_b16 v[198:199], v4 offset:31232\n"
"s_waitcnt lgkmcnt(10)\n"
"v_mfma_f32_32x32x16_bf16 v[168:183], v[200:203], v[56:59], v[168:183]\n"
"ds_read_b64_tr_b16 v[200:201], v5 offset:0\n"
"ds_read_b64_tr_b16 v[202:203], v5 offset:512\n"
"s_waitcnt lgkmcnt(10)\n"
"v_mfma_f32_32x32x16_bf16 v[168:183], v[204:207], v[60:63], v[168:183]\n"
"ds_read_b64_tr_b16 v[204:205], v5 offset:2048\n"
"ds_read_b64_tr_b16 v[206:207], v5 offset:2560\n"
"s_waitcnt vmcnt(25)\n"
"s_waitcnt lgkmcnt(10)\n"
"v_mfma_f32_32x32x16_bf16 v[168:183], v[184:187], v[64:67], v[168:183]\n"
"ds_read_b64_tr_b16 v[184:185], v5 offset:4096\n"
"ds_read_b64_tr_b16 v[186:187], v5 offset:4608\n"
"s_waitcnt lgkmcnt(10)\n"
"v_mfma_f32_32x32x16_bf16 v[168:183], v[188:191], v[68:71], v[168:183]\n"
"ds_read_b64_tr_b16 v[188:189], v5 offset:6144\n"
"ds_read_b64_tr_b16 v[190:191], v5 offset:6656\n"
"s_waitcnt lgkmcnt(10)\n"
"v_mfma_f32_32x32x16_bf16 v[168:183], v[192:195], v[72:75], v[168:183]\n"
"ds_read_b64_tr_b16 v[192:193], v5 offset:8192\n"
"ds_read_b64_tr_b16 v[194:195], v5 offset:8704\n"
"s_waitcnt vmcnt(22)\n"
"s_waitcnt lgkmcnt(10)\n"
"v_mfma_f32_32x32x16_bf16 v[168:183], v[196:199], v[76:79], v[168:183]\n"
"ds_read_b64_tr_b16 v[196:197], v5 offset:10240\n"
"ds_read_b64_tr_b16 v[198:199], v5 offset:10752\n"
"s_waitcnt lgkmcnt(10)\n"
"v_mfma_f32_32x32x16_bf16 v[168:183], v[200:203], v[80:83], v[168:183]\n"
"ds_read_b64_tr_b16 v[200:201], v5 offset:12288\n"
"ds_read_b64_tr_b16 v[202:203], v5 offset:12800\n"
"s_waitcnt lgkmcnt(10)\n"
"v_mfma_f32_32x32x16_bf16 v[168:183], v[204:207], v[84:87], v[168:183]\n"
"ds_read_b64_tr_b16 v[204:205], v5 offset:14336\n"
"ds_read_b64_tr_b16 v[206:207], v5 offset:14848\n"
"s_waitcnt vmcnt(19)\n"
"s_waitcnt lgkmcnt(10)\n"
"v_mfma_f32_32x32x16_bf16 v[168:183], v[184:187], v[88:91], v[168:183]\n"
"ds_read_b64_tr_b16 v[184:185], v5 offset:16384\n"
"ds_read_b64_tr_b16 v[186:187], v5 offset:16896\n"
"s_waitcnt lgkmcnt(10)\n"
"v_mfma_f32_32x32x16_bf16 v[168:183], v[188:191], v[92:95], v[168:183]\n"
"ds_read_b64_tr_b16 v[188:189], v5 offset:18432\n"
"ds_read_b64_tr_b16 v[190:191], v5 offset:18944\n"
"s_waitcnt lgkmcnt(10)\n"
"v_mfma_f32_32x32x16_bf16 v[168:183], v[192:195], v[96:99], v[168:183]\n"
"ds_read_b64_tr_b16 v[192:193], v5 offset:20480\n"
"ds_read_b64_tr_b16 v[194:195], v5 offset:20992\n"
"s_waitcnt vmcnt(16)\n"
"s_waitcnt lgkmcnt(10)\n"
"v_mfma_f32_32x32x16_bf16 v[168:183], v[196:199], v[100:103], v[168:183]\n"
"ds_read_b64_tr_b16 v[196:197], v5 offset:22528\n"
"ds_read_b64_tr_b16 v[198:199], v5 offset:23040\n"
"s_waitcnt lgkmcnt(10)\n"
"v_mfma_f32_32x32x16_bf16 v[168:183], v[200:203], v[104:107], v[168:183]\n"
"ds_read_b64_tr_b16 v[200:201], v5 offset:24576\n"
"ds_read_b64_tr_b16 v[202:203], v5 offset:25088\n"
"s_waitcnt lgkmcnt(10)\n"
"v_mfma_f32_32x32x16_bf16 v[168:183], v[204:207], v[108:111], v[168:183]\n"
"ds_read_b64_tr_b16 v[204:205], v5 offset:26624\n"
"ds_read_b64_tr_b16 v[206:207], v5 offset:27136\n"
"s_waitcnt vmcnt(13)\n"
"s_waitcnt lgkmcnt(10)\n"
"v_mfma_f32_32x32x16_bf16 v[168:183], v[184:187], v[112:115], v[168:183]\n"
"ds_read_b64_tr_b16 v[184:185], v5 offset:28672\n"
"ds_read_b64_tr_b16 v[186:187], v5 offset:29184\n"
"s_waitcnt lgkmcnt(10)\n"
"v_mfma_f32_32x32x16_bf16 v[168:183], v[188:191], v[116:119], v[168:183]\n"
"ds_read_b64_tr_b16 v[188:189], v5 offset:30720\n"
"ds_read_b64_tr_b16 v[190:191], v5 offset:31232\n"
"s_waitcnt lgkmcnt(10)\n"
"v_mfma_f32_32x32x16_bf16 v[168:183], v[192:195], v[120:123], v[168:183]\n"
"ds_read_b64_tr_b16 v[192:193], v5 offset:32768\n"
"ds_read_b64_tr_b16 v[194:195], v5 offset:33280\n"
"s_waitcnt vmcnt(10)\n"
"s_waitcnt lgkmcnt(10)\n"
"v_mfma_f32_32x32x16_bf16 v[168:183], v[196:199], v[124:127], v[168:183]\n"
"ds_read_b64_tr_b16 v[196:197], v5 offset:34816\n"
"ds_read_b64_tr_b16 v[198:199], v5 offset:35328\n"
"s_waitcnt lgkmcnt(10)\n"
"v_mfma_f32_32x32x16_bf16 v[168:183], v[200:203], v[128:131], v[168:183]\n"
"ds_read_b64_tr_b16 v[200:201], v5 offset:36864\n"
"ds_read_b64_tr_b16 v[202:203], v5 offset:37376\n"
"s_waitcnt lgkmcnt(10)\n"
"v_mfma_f32_32x32x16_bf16 v[168:183], v[204:207], v[132:135], v[168:183]\n"
"ds_read_b64_tr_b16 v[204:205], v5 offset:38912\n"
"ds_read_b64_tr_b16 v[206:207], v5 offset:39424\n"
"s_waitcnt vmcnt(7)\n"
"s_waitcnt lgkmcnt(10)\n"
"v_mfma_f32_32x32x16_bf16 v[168:183], v[184:187], v[136:139], v[168:183]\n"
"s_waitcnt lgkmcnt(8)\n"
"v_mfma_f32_32x32x16_bf16 v[168:183], v[188:191], v[140:143], v[168:183]\n"
"s_waitcnt lgkmcnt(6)\n"
"v_mfma_f32_32x32x16_bf16 v[168:183], v[192:195], v[144:147], v[168:183]\n"
"s_waitcnt vmcnt(4)\n"
"s_waitcnt lgkmcnt(4)\n"
"v_mfma_f32_32x32x16_bf16 v[168:183], v[196:199], v[148:151], v[168:183]\n"
"s_waitcnt lgkmcnt(2)\n"
"v_mfma_f32_32x32x16_bf16 v[168:183], v[200:203], v[152:155], v[168:183]\n"
"s_waitcnt lgkmcnt(0)\n"
"v_mfma_f32_32x32x16_bf16 v[168:183], v[204:207], v[156:159], v[168:183]\n"
"buffer_load_dword v12, v3, s[44:47], s71 offen sc1\n"
"s_waitcnt vmcnt(0)\n"
"s_mov_b32 s78, 0\n"
".Lkc_sp_24_%=:\n"
"v_readfirstlane_b32 s79, v12\n"
"s_add_u32 s78, s78, 1\n"
"s_cmp_ge_u32 s79, 20\n"
"s_cbranch_scc1 .Lkc_spd_25_%=\n"
"s_cmp_lt_u32 s78, 0x4000\n"
"s_cbranch_scc0 .Lkc_spd_25_%=\n"
"s_sleep 2\n"
"buffer_load_dword v12, v3, s[44:47], s71 offen sc1\n"
"s_waitcnt vmcnt(0)\n"
"s_branch .Lkc_sp_24_%=\n"
".Lkc_spd_25_%=:\n"
"s_mul_i32 s72, s94, 18\n"
"s_lshl_b32 s74, %[cb], 1\n"
"s_add_u32 s72, s72, s74\n"
"s_add_u32 s72, s72, s57\n"
"s_lshl_b32 s72, s72, 11\n"
"s_add_u32 s72, s72, 28311552\n"
"buffer_load_dwordx4 v[208:211], v2, s[44:47], s72 offen offset:0 sc1\n"
"buffer_load_dwordx4 v[212:215], v2, s[44:47], s72 offen offset:1024 sc1\n"
"s_add_u32 s72, s72, 2359296\n"
"buffer_load_dwordx4 v[216:219], v2, s[44:47], s72 offen offset:0 sc1\n"
"buffer_load_dwordx4 v[220:223], v2, s[44:47], s72 offen offset:1024 sc1\n"
"s_add_u32 s72, s72, 2359296\n"
"buffer_load_dwordx4 v[224:227], v2, s[44:47], s72 offen offset:0 sc1\n"
"buffer_load_dwordx4 v[228:231], v2, s[44:47], s72 offen offset:1024 sc1\n"
"s_add_u32 s72, s72, 2359296\n"
"buffer_load_dwordx4 v[232:235], v2, s[44:47], s72 offen offset:0 sc1\n"
"buffer_load_dwordx4 v[236:239], v2, s[44:47], s72 offen offset:1024 sc1\n"
"s_add_u32 s72, s72, 2359296\n"
"buffer_load_dwordx4 v[240:243], v2, s[44:47], s72 offen offset:0 sc1\n"
"buffer_load_dwordx4 v[244:247], v2, s[44:47], s72 offen offset:1024 sc1\n"
"s_add_u32 s72, s72, 2359296\n"
"v_mov_b32 v48, 0xffff0000\n"
"v_lshlrev_b32 v52, 16, v160\n"
"v_and_b32 v53, v48, v160\n"
"v_lshlrev_b32 v54, 16, v161\n"
"v_and_b32 v55, v48, v161\n"
"v_lshlrev_b32 v56, 16, v162\n"
"v_and_b32 v57, v48, v162\n"
"v_lshlrev_b32 v58, 16, v163\n"
"v_and_b32 v59, v48, v163\n"
"v_lshlrev_b32 v60, 16, v164\n"
"v_and_b32 v61, v48, v164\n"
"v_lshlrev_b32 v62, 16, v165\n"
"v_and_b32 v63, v48, v165\n"
"v_lshlrev_b32 v64, 16, v166\n"
"v_and_b32 v65, v48, v166\n"
"v_lshlrev_b32 v66, 16, v167\n"
"v_and_b32 v67, v48, v167\n"
"s_waitcnt vmcnt(8)\n"
"v_lshlrev_b32 v16, 16, v208\n"
"v_and_b32 v17, v48, v208\n"
"v_lshlrev_b32 v18, 16, v209\n"
"v_and_b32 v19, v48, v209\n"
"v_lshlrev_b32 v20, 16, v210\n"
"v_and_b32 v21, v48, v210\n"
"v_lshlrev_b32 v22, 16, v211\n"
"v_and_b32 v23, v48, v211\n"
"v_lshlrev_b32 v24, 16, v212\n"
"v_and_b32 v25, v48, v212\n"
"v_lshlrev_b32 v26, 16, v213\n"
"v_and_b32 v27, v48, v213\n"
"v_lshlrev_b32 v28, 16, v214\n"
"v_and_b32 v29, v48, v214\n"
"v_lshlrev_b32 v30, 16, v215\n"
"v_and_b32 v31, v48, v215\n"
"s_waitcnt vmcnt(6)\n"
"v_lshlrev_b32 v32, 16, v216\n"
"v_and_b32 v33, v48, v216\n"
"v_lshlrev_b32 v34, 16, v217\n"
"v_and_b32 v35, v48, v217\n"
"v_lshlrev_b32 v36, 16, v218\n"
"v_and_b32 v37, v48, v218\n"
"v_lshlrev_b32 v38, 16, v219\n"
"v_and_b32 v39, v48, v219\n"
"v_lshlrev_b32 v40, 16, v220\n"
"v_and_b32 v41, v48, v220\n"
"v_lshlrev_b32 v42, 16, v221\n"
"v_and_b32 v43, v48, v221\n"
"v_lshlrev_b32 v44, 16, v222\n"
"v_and_b32 v45, v48, v222\n"
"v_lshlrev_b32 v46, 16, v223\n"
"v_and_b32 v47, v48, v223\n"
"v_pk_add_f32 v[16:17], v[16:17], v[32:33]\n"
"v_pk_add_f32 v[18:19], v[18:19], v[34:35]\n"
"v_pk_add_f32 v[20:21], v[20:21], v[36:37]\n"
"v_pk_add_f32 v[22:23], v[22:23], v[38:39]\n"
"v_pk_add_f32 v[24:25], v[24:25], v[40:41]\n"
"v_pk_add_f32 v[26:27], v[26:27], v[42:43]\n"
"v_pk_add_f32 v[28:29], v[28:29], v[44:45]\n"
"v_pk_add_f32 v[30:31], v[30:31], v[46:47]\n"
"s_waitcnt vmcnt(4)\n"
"v_lshlrev_b32 v32, 16, v224\n"
"v_and_b32 v33, v48, v224\n"
"v_lshlrev_b32 v34, 16, v225\n"
"v_and_b32 v35, v48, v225\n"
"v_lshlrev_b32 v36, 16, v226\n"
"v_and_b32 v37, v48, v226\n"
"v_lshlrev_b32 v38, 16, v227\n"
"v_and_b32 v39, v48, v227\n"
"v_lshlrev_b32 v40, 16, v228\n"
"v_and_b32 v41, v48, v228\n"
"v_lshlrev_b32 v42, 16, v229\n"
"v_and_b32 v43, v48, v229\n"
"v_lshlrev_b32 v44, 16, v230\n"
"v_and_b32 v45, v48, v230\n"
"v_lshlrev_b32 v46, 16, v231\n"
"v_and_b32 v47, v48, v231\n"
"v_pk_add_f32 v[16:17], v[16:17], v[32:33]\n"
"v_pk_add_f32 v[18:19], v[18:19], v[34:35]\n"
"v_pk_add_f32 v[20:21], v[20:21], v[36:37]\n"
"v_pk_add_f32 v[22:23], v[22:23], v[38:39]\n"
"v_pk_add_f32 v[24:25], v[24:25], v[40:41]\n"
"v_pk_add_f32 v[26:27], v[26:27], v[42:43]\n"
"v_pk_add_f32 v[28:29], v[28:29], v[44:45]\n"
"v_pk_add_f32 v[30:31], v[30:31], v[46:47]\n"
"s_waitcnt vmcnt(2)\n"
"v_lshlrev_b32 v32, 16, v232\n"
"v_and_b32 v33, v48, v232\n"
"v_lshlrev_b32 v34, 16, v233\n"
"v_and_b32 v35, v48, v233\n"
"v_lshlrev_b32 v36, 16, v234\n"
"v_and_b32 v37, v48, v234\n"
"v_lshlrev_b32 v38, 16, v235\n"
"v_and_b32 v39, v48, v235\n"
"v_lshlrev_b32 v40, 16, v236\n"
"v_and_b32 v41, v48, v236\n"
"v_lshlrev_b32 v42, 16, v237\n"
"v_and_b32 v43, v48, v237\n"
"v_lshlrev_b32 v44, 16, v238\n"
"v_and_b32 v45, v48, v238\n"
"v_lshlrev_b32 v46, 16, v239\n"
"v_and_b32 v47, v48, v239\n"
"v_pk_add_f32 v[16:17], v[16:17], v[32:33]\n"
"v_pk_add_f32 v[18:19], v[18:19], v[34:35]\n"
"v_pk_add_f32 v[20:21], v[20:21], v[36:37]\n"
"v_pk_add_f32 v[22:23], v[22:23], v[38:39]\n"
"v_pk_add_f32 v[24:25], v[24:25], v[40:41]\n"
"v_pk_add_f32 v[26:27], v[26:27], v[42:43]\n"
"v_pk_add_f32 v[28:29], v[28:29], v[44:45]\n"
"v_pk_add_f32 v[30:31], v[30:31], v[46:47]\n"
"s_waitcnt vmcnt(0)\n"
"v_lshlrev_b32 v32, 16, v240\n"
"v_and_b32 v33, v48, v240\n"
"v_lshlrev_b32 v34, 16, v241\n"
"v_and_b32 v35, v48, v241\n"
"v_lshlrev_b32 v36, 16, v242\n"
"v_and_b32 v37, v48, v242\n"
"v_lshlrev_b32 v38, 16, v243\n"
"v_and_b32 v39, v48, v243\n"
"v_lshlrev_b32 v40, 16, v244\n"
"v_and_b32 v41, v48, v244\n"
"v_lshlrev_b32 v42, 16, v245\n"
"v_and_b32 v43, v48, v245\n"
"v_lshlrev_b32 v44, 16, v246\n"
"v_and_b32 v45, v48, v246\n"
"v_lshlrev_b32 v46, 16, v247\n"
"v_and_b32 v47, v48, v247\n"
"v_pk_add_f32 v[16:17], v[16:17], v[32:33]\n"
"v_pk_add_f32 v[18:19], v[18:19], v[34:35]\n"
"v_pk_add_f32 v[20:21], v[20:21], v[36:37]\n"
"v_pk_add_f32 v[22:23], v[22:23], v[38:39]\n"
"v_pk_add_f32 v[24:25], v[24:25], v[40:41]\n"
"v_pk_add_f32 v[26:27], v[26:27], v[42:43]\n"
"v_pk_add_f32 v[28:29], v[28:29], v[44:45]\n"
"v_pk_add_f32 v[30:31], v[30:31], v[46:47]\n"
"v_mul_f32 v32, 0xbfb8aa3b, v16\n"
"v_mul_f32 v33, 0xbfb8aa3b, v17\n"
"v_mul_f32 v34, 0xbfb8aa3b, v18\n"
"v_mul_f32 v35, 0xbfb8aa3b, v19\n"
"v_mul_f32 v36, 0xbfb8aa3b, v20\n"
"v_mul_f32 v37, 0xbfb8aa3b, v21\n"
"v_mul_f32 v38, 0xbfb8aa3b, v22\n"
"v_mul_f32 v39, 0xbfb8aa3b, v23\n"
"v_mul_f32 v40, 0xbfb8aa3b, v24\n"
"v_mul_f32 v41, 0xbfb8aa3b, v25\n"
"v_mul_f32 v42, 0xbfb8aa3b, v26\n"
"v_mul_f32 v43, 0xbfb8aa3b, v27\n"
"v_mul_f32 v44, 0xbfb8aa3b, v28\n"
"v_mul_f32 v45, 0xbfb8aa3b, v29\n"
"v_mul_f32 v46, 0xbfb8aa3b, v30\n"
"v_mul_f32 v47, 0xbfb8aa3b, v31\n"
"v_exp_f32 v32, v32\n"
"v_exp_f32 v33, v33\n"
"v_exp_f32 v34, v34\n"
"v_exp_f32 v35, v35\n"
"v_exp_f32 v36, v36\n"
"v_exp_f32 v37, v37\n"
"v_exp_f32 v38, v38\n"
"v_exp_f32 v39, v39\n"
"v_exp_f32 v40, v40\n"
"v_exp_f32 v41, v41\n"
"v_exp_f32 v42, v42\n"
"v_exp_f32 v43, v43\n"
"v_exp_f32 v44, v44\n"
"v_exp_f32 v45, v45\n"
"v_exp_f32 v46, v46\n"
"v_exp_f32 v47, v47\n"
"v_add_f32 v32, 1.0, v32\n"
"v_add_f32 v33, 1.0, v33\n"
"v_add_f32 v34, 1.0, v34\n"
"v_add_f32 v35, 1.0, v35\n"
"v_add_f32 v36, 1.0, v36\n"
"v_add_f32 v37, 1.0, v37\n"
"v_add_f32 v38, 1.0, v38\n"
"v_add_f32 v39, 1.0, v39\n"
"v_add_f32 v40, 1.0, v40\n"
"v_add_f32 v41, 1.0, v41\n"
"v_add_f32 v42, 1.0, v42\n"
"v_add_f32 v43, 1.0, v43\n"
"v_add_f32 v44, 1.0, v44\n"
"v_add_f32 v45, 1.0, v45\n"
"v_add_f32 v46, 1.0, v46\n"
"v_add_f32 v47, 1.0, v47\n"
"v_rcp_f32 v32, v32\n"
"v_rcp_f32 v33, v33\n"
"v_rcp_f32 v34, v34\n"
"v_rcp_f32 v35, v35\n"
"v_rcp_f32 v36, v36\n"
"v_rcp_f32 v37, v37\n"
"v_rcp_f32 v38, v38\n"
"v_rcp_f32 v39, v39\n"
"v_rcp_f32 v40, v40\n"
"v_rcp_f32 v41, v41\n"
"v_rcp_f32 v42, v42\n"
"v_rcp_f32 v43, v43\n"
"v_rcp_f32 v44, v44\n"
"v_rcp_f32 v45, v45\n"
"v_rcp_f32 v46, v46\n"
"v_rcp_f32 v47, v47\n"
"v_mul_f32 v16, v16, v168\n"
"v_mul_f32 v17, v17, v169\n"
"v_mul_f32 v18, v18, v170\n"
"v_mul_f32 v19, v19, v171\n"
"v_mul_f32 v20, v20, v172\n"
"v_mul_f32 v21, v21, v173\n"
"v_mul_f32 v22, v22, v174\n"
"v_mul_f32 v23, v23, v175\n"
"v_mul_f32 v24, v24, v176\n"
"v_mul_f32 v25, v25, v177\n"
"v_mul_f32 v26, v26, v178\n"
"v_mul_f32 v27, v27, v179\n"
"v_mul_f32 v28, v28, v180\n"
"v_mul_f32 v29, v29, v181\n"
"v_mul_f32 v30, v30, v182\n"
"v_mul_f32 v31, v31, v183\n"
"v_fma_f32 v16, v16, v32, v52\n"
"v_fma_f32 v17, v17, v33, v53\n"
"v_fma_f32 v18, v18, v34, v54\n"
"v_fma_f32 v19, v19, v35, v55\n"
"v_fma_f32 v20, v20, v36, v56\n"
"v_fma_f32 v21, v21, v37, v57\n"
"v_fma_f32 v22, v22, v38, v58\n"
"v_fma_f32 v23, v23, v39, v59\n"
"v_fma_f32 v24, v24, v40, v60\n"
"v_fma_f32 v25, v25, v41, v61\n"
"v_fma_f32 v26, v26, v42, v62\n"
"v_fma_f32 v27, v27, v43, v63\n"
"v_fma_f32 v28, v28, v44, v64\n"
"v_fma_f32 v29, v29, v45, v65\n"
"v_fma_f32 v30, v30, v46, v66\n"
"v_fma_f32 v31, v31, v47, v67\n"
"v_cvt_pk_bf16_f32 v32, v16, v17\n"
"v_cvt_pk_bf16_f32 v33, v18, v19\n"
"v_cvt_pk_bf16_f32 v34, v20, v21\n"
"v_cvt_pk_bf16_f32 v35, v22, v23\n"
"v_cvt_pk_bf16_f32 v36, v24, v25\n"
"v_cvt_pk_bf16_f32 v37, v26, v27\n"
"v_cvt_pk_bf16_f32 v38, v28, v29\n"
"v_cvt_pk_bf16_f32 v39, v30, v31\n"
"s_mov_b32 s40, %[out_lo]\n"
"s_and_b32 s41, %[out_hi], 0xffff\n"
"buffer_store_dwordx2 v[32:33], v10, s[40:43], s73 offen offset:0\n"
"buffer_store_dwordx2 v[34:35], v10, s[40:43], s73 offen offset:16\n"
"buffer_store_dwordx2 v[36:37], v10, s[40:43], s73 offen offset:32\n"
"buffer_store_dwordx2 v[38:39], v10, s[40:43], s73 offen offset:48\n"
".Lkc_done_14_%=:\n"
"kb_code_end:\n"
  :
  : [x_lo] "s"((u32)px), [x_hi] "s"((u32)(px >> 32)), [wo_lo] "s"((u32)po), [wo_hi] "s"((u32)(po >> 32)),
    [wd_lo] "s"((u32)pd), [wd_hi] "s"((u32)(pd >> 32)), [ws_lo] "s"((u32)pw), [ws_hi] "s"((u32)(pw >> 32)),
    [out_lo] "s"((u32)pout), [out_hi] "s"((u32)(pout >> 32)), [wid] "s"(wid), [qb] "s"(qb), [hg] "s"(hg), [dg] "s"(dg), [ks] "s"(ks),
    [tb] "s"(tb), [b0] "s"(b0), [nsl] "s"(nsl), [fin] "s"(fin), [ftb] "s"(ftb), [cb] "s"(cb), [fdg] "s"(fdg), [lds] "s"(lds), [bid] "s"(b)
  : "memory", "vcc", "scc", "m0", "v0", "v1", "v2", "v3", "v4", "v5", "v6", "v7", "v8", "v9", "v10", "v11", "v12", "v13", "v14", "v15", "v16", "v17", "v18", "v19", "v20", "v21", "v22", "v23", "v24", "v25", "v26", "v27", "v28", "v29", "v30", "v31", "v32", "v33", "v34", "v35", "v36", "v37", "v38", "v39", "v40", "v41", "v42", "v43", "v44", "v45", "v46", "v47", "v48", "v49", "v50", "v51", "v52", "v53", "v54", "v55", "v56", "v57", "v58", "v59", "v60", "v61", "v62", "v63", "v64", "v65", "v66", "v67", "v68", "v69", "v70", "v71", "v72", "v73", "v74", "v75", "v76", "v77", "v78", "v79", "v80", "v81", "v82", "v83", "v84", "v85", "v86", "v87", "v88", "v89", "v90", "v91", "v92", "v93", "v94", "v95", "v96", "v97", "v98", "v99", "v100", "v101", "v102", "v103", "v104", "v105", "v106", "v107", "v108", "v109", "v110", "v111", "v112", "v113", "v114", "v115", "v116", "v117", "v118", "v119", "v120", "v121", "v122", "v123", "v124", "v125", "v126", "v127", "v128", "v129", "v130", "v131", "v132", "v133", "v134", "v135", "v136", "v137", "v138", "v139", "v140", "v141", "v142", "v143", "v144", "v145", "v146", "v147", "v148", "v149", "v150", "v151", "v152", "v153", "v154", "v155", "v156", "v157", "v158", "v159", "v160", "v161", "v162", "v163", "v164", "v165", "v166", "v167", "v168", "v169", "v170", "v171", "v172", "v173", "v174", "v175", "v176", "v177", "v178", "v179", "v180", "v181", "v182", "v183", "v184", "v185", "v186", "v187", "v188", "v189", "v190", "v191", "v192", "v193", "v194", "v195", "v196", "v197", "v198", "v199", "v200", "v201", "v202", "v203", "v204", "v205", "v206", "v207", "v208", "v209", "v210", "v211", "v212", "v213", "v214", "v215", "v216", "v217", "v218", "v219", "v220", "v221", "v222", "v223", "v224", "v225", "v226", "v227", "v228", "v229", "v230", "v231", "v232", "v233", "v234", "v235", "v236", "v237", "v238", "v239", "v240", "v241", "v242", "v243", "v244", "v245", "v246", "v247", "v248", "v249", "v250", "v251", "v252", "v253", "v254", "v255", "s40", "s41", "s42", "s43", "s44", "s45", "s46", "s47", "s48", "s49", "s50", "s51", "s52", "s53", "s54", "s55", "s56", "s57", "s58", "s59", "s60", "s61", "s62", "s63", "s64", "s65", "s66", "s67", "s68", "s69", "s70", "s71", "s72", "s73", "s74", "s75", "s76", "s77", "s78", "s79", "s80", "s81", "s82", "s83", "s84", "s85", "s86", "s87", "s88", "s89", "s90", "s91", "s92", "s93", "s94", "s95", "s96", "s97", "s98", "s99");
}

extern "C" void kernel_launch(void* const* d_in, const int* in_sizes, int n_in, void* d_out, int out_size, void* d_ws, size_t ws_size, hipStream_t stream) {
  const u16* x = (const u16*)d_in[0];
  char* ws = (char*)d_ws;
  k1_kernel<<<256, 512, 0, stream>>>(x, (const u16*)d_in[1], (const u16*)d_in[4], (const u16*)d_in[5], (const u16*)d_in[6], (const u16*)d_in[2], ws);
  kb_kernel<<<256, 512, 0, stream>>>(x, (const u16*)d_in[2], (const u16*)d_in[6], ws, (u16*)d_out);
}
```
